```python
import torch

def _softmax(x, axis=-1):
    return torch.softmax(x, dim=axis)
def _gelu(x):
    return torch.nn.functional.gelu(x, approximate="tanh")
def _silu(x):
    return torch.nn.functional.silu(x)
def _sigmoid(x):
    return torch.sigmoid(x)
def _layernorm(x, axis=-1, eps=1e-5):
    m = x.mean(dim=axis, keepdim=True)
    v = x.var(dim=axis, keepdim=True, unbiased=False)
    return (x - m) / torch.sqrt(v + eps)
def _scatter_add(tgt, idx, src, axis=0):
    return tgt.clone().index_add_(axis, idx, src)
def _segment_sum(src, seg, n):
    out = torch.zeros((n,) + src.shape[1:], dtype=src.dtype)
    return out.index_add_(0, seg, src)
def _pad(x, pw):
    flat = tuple(v for pair in reversed(pw) for v in pair)
    return torch.nn.functional.pad(x, flat)

kernel_name = 'sequential_preLN__linear_attn_block__geglu_ffn__prompt'

def setup_inputs():

    _w = (lambda a: (lambda t: t.to(torch.bfloat16) if t.is_floating_point() else t)(torch.as_tensor(a)))
    def _A_setup():
        B, S, D = 1, 2048, 640
        return {
            "x": _w((lambda *s: torch.randn(*s, dtype=torch.bfloat16) * 0.05)(B, S, D)),
            "wqkv": _w((lambda *s: torch.randn(*s, dtype=torch.bfloat16) * 0.05)(D, 3 * D)),
            "wo": _w((lambda *s: torch.randn(*s, dtype=torch.bfloat16) * 0.05)(D, D)),
        }
    _A_in = _A_setup()

    def _B_setup():
        B, S, D, F = 1, 2048, 640, 1728
        return {
            "x": _w((lambda *s: torch.randn(*s, dtype=torch.bfloat16) * 0.05)(B, S, D)),
            "w_gate": _w((lambda *s: torch.randn(*s, dtype=torch.bfloat16) * 0.05)(D, F)),
            "w_up": _w((lambda *s: torch.randn(*s, dtype=torch.bfloat16) * 0.05)(D, F)),
            "w_down": _w((lambda *s: torch.randn(*s, dtype=torch.bfloat16) * 0.05)(F, D)),
        }
    _B_in = _B_setup()

    return {
        "A_x": _A_in["x"],
        "A_wqkv": _A_in["wqkv"],
        "A_wo": _A_in["wo"],
        "B_x": _B_in["x"],
        "B_w_gate": _B_in["w_gate"],
        "B_w_up": _B_in["w_up"],
        "B_w_down": _B_in["w_down"],
    }

def reference(A_x, A_wqkv, A_wo, B_x, B_w_gate, B_w_up, B_w_down):
    def _A_ref(x, wqkv, wo):
        B, S, D = x.shape
        H = 32; Dh = D // H
        qkv = (lambda x, *d: x.permute(*d))((x @ wqkv).reshape(B, S, 3, H, Dh), 2, 0, 3, 1, 4)
        q, k, v = qkv[0], qkv[1], qkv[2]
        phi = lambda t: torch.where(t > 0, t + 1, torch.exp(t)) + 1e-6
        pq, pk = phi(q), phi(k)
        kv = torch.einsum("bhsd,bhse->bhde", pk, v)
        z = torch.einsum("bhsd->bhd", pk)
        num = torch.einsum("bhqd,bhde->bhqe", pq, kv)
        den = torch.einsum("bhqd,bhd->bhq", pq, z)[..., None] + 1e-6
        o = num / den
        return (lambda x, *d: x.permute(*d))(o, 0, 2, 1, 3).reshape(B, S, D) @ wo

    def _B_ref(x, w_gate, w_up, w_down):
        return (_gelu(x @ w_gate) * (x @ w_up)) @ w_down

    x = A_x
    h = x + _A_ref(_layernorm(x, axis=-1), A_wqkv, A_wo)
    h2 = h + _B_ref(_layernorm(h, axis=-1), B_w_gate, B_w_up, B_w_down)
    return h2
```

```cpp
#include <hip/hip_runtime.h>
#include <hip/hip_bf16.h>
typedef unsigned short u16;
__device__ __forceinline__ float bf2f(u16 b){ return __uint_as_float(((unsigned)b)<<16); }
__device__ __forceinline__ u16 f2bf(float f){ __hip_bfloat16 h = __float2bfloat16(f); return *(u16*)&h; }
constexpr int S=2048, D=640, F=1728, H=32, DH=20;

__global__ void k_ln(const u16* x, u16* y){
  int row = blockIdx.x*4 + (threadIdx.x>>6); int lane = threadIdx.x&63;
  float v[10]; float s=0;
  for(int i=0;i<10;i++){ v[i]=bf2f(x[row*D+lane+64*i]); s+=v[i]; }
  for(int o=32;o;o>>=1) s+=__shfl_xor(s,o);
  float m=s/D; float q=0;
  for(int i=0;i<10;i++){ float d=v[i]-m; q+=d*d; }
  for(int o=32;o;o>>=1) q+=__shfl_xor(q,o);
  float r=1.0f/sqrtf(q/D+1e-5f);
  for(int i=0;i<10;i++) y[row*D+lane+64*i]=f2bf((v[i]-m)*r);
}
__global__ void k_gemm(const u16* A, const u16* B, float* C, int M, int N, int K){
  __shared__ float As[16][17], Bs[16][17];
  int tx=threadIdx.x&15, ty=threadIdx.x>>4;
  int r=blockIdx.y*16+ty, c=blockIdx.x*16+tx; float acc=0;
  for(int k0=0;k0<K;k0+=16){
    As[ty][tx]=bf2f(A[r*K+k0+tx]); Bs[ty][tx]=bf2f(B[(k0+ty)*N+c]);
    __syncthreads();
    for(int k=0;k<16;k++) acc+=As[ty][k]*Bs[k][tx];
    __syncthreads();
  }
  C[r*N+c]=acc;
}
__device__ __forceinline__ float phi(float t){ return (t>0? t+1.0f : __expf(t)) + 1e-6f; }
__global__ void k_kv(const float* qkv, float* kv, float* z){
  int h=blockIdx.x; int t=threadIdx.x;
  if(t<420){
    int d=t/21, e=t%21; float acc=0;
    for(int s=0;s<S;s++){ float k=phi(qkv[s*1920+640+h*20+d]); float v=(e<20)? qkv[s*1920+1280+h*20+e]:1.0f; acc+=k*v; }
    if(e<20) kv[h*400+d*20+e]=acc; else z[h*20+d]=acc;
  }
}
__global__ void k_o(const float* qkv, const float* kv, const float* z, u16* o){
  int idx=blockIdx.x*256+threadIdx.x; int s=idx/640, c=idx%640, h=c/20, e=c%20;
  float num=0, den=0;
  for(int d=0;d<20;d++){ float q=phi(qkv[s*1920+h*20+d]); num+=q*kv[h*400+d*20+e]; den+=q*z[h*20+d]; }
  o[idx]=f2bf(num/(den+1e-6f));
}
__global__ void k_res(const u16* x, const float* a, u16* h){ int i=blockIdx.x*256+threadIdx.x; h[i]=f2bf(bf2f(x[i])+a[i]); }
__global__ void k_act(const float* g, const float* u, u16* a){ int i=blockIdx.x*256+threadIdx.x; float x=g[i];
  float t=tanhf(0.7978845608028654f*(x+0.044715f*x*x*x)); a[i]=f2bf(0.5f*x*(1.0f+t)*u[i]); }

extern "C" void kernel_launch(void* const* d_in, const int* in_sizes, int n_in,
                              void* d_out, int out_size, void* d_ws, size_t ws_size,
                              hipStream_t stream) {
  const u16* x=(const u16*)d_in[0]; const u16* wqkv=(const u16*)d_in[1]; const u16* wo=(const u16*)d_in[2];
  const u16* wg=(const u16*)d_in[4]; const u16* wu=(const u16*)d_in[5]; const u16* wd=(const u16*)d_in[6];
  char* ws=(char*)d_ws;
  u16* ln=(u16*)ws; float* qkv=(float*)(ws+(4<<20)); float* kv=(float*)(ws+(24<<20)); float* z=(float*)(ws+(25<<20));
  u16* o=(u16*)(ws+(26<<20)); float* att=(float*)(ws+(30<<20)); u16* h=(u16*)(ws+(40<<20));
  float* g=(float*)(ws+(50<<20)); float* u=(float*)(ws+(70<<20)); u16* act=(u16*)(ws+(90<<20)); float* dn=(float*)(ws+(100<<20));
  k_ln<<<S/4,256,0,stream>>>(x,ln);
  k_gemm<<<dim3(1920/16,S/16),256,0,stream>>>(ln,wqkv,qkv,S,1920,D);
  k_kv<<<H,512,0,stream>>>(qkv,kv,z);
  k_o<<<S*D/256,256,0,stream>>>(qkv,kv,z,o);
  k_gemm<<<dim3(D/16,S/16),256,0,stream>>>(o,wo,att,S,D,D);
  k_res<<<S*D/256,256,0,stream>>>(x,att,h);
  k_ln<<<S/4,256,0,stream>>>(h,ln);
  k_gemm<<<dim3(F/16,S/16),256,0,stream>>>(ln,wg,g,S,F,D);
  k_gemm<<<dim3(F/16,S/16),256,0,stream>>>(ln,wu,u,S,F,D);
  k_act<<<S*F/256,256,0,stream>>>(g,u,act);
  k_gemm<<<dim3(D/16,S/16),256,0,stream>>>(act,wd,dn,S,D,F);
  k_res<<<S*D/256,256,0,stream>>>(h,dn,(u16*)d_out);
}
```

```cpp
#include <hip/hip_runtime.h>
#include <hip/hip_bf16.h>
#include <stdint.h>
#include <type_traits>

#define LAS __attribute__((address_space(3)))
#define GAS __attribute__((address_space(1)))
typedef __bf16 bf16x8 __attribute__((ext_vector_type(8)));
typedef __bf16 bf16x2 __attribute__((ext_vector_type(2)));
typedef short s16x4 __attribute__((ext_vector_type(4)));
typedef float f32x4 __attribute__((ext_vector_type(4)));
typedef float f32x2 __attribute__((ext_vector_type(2)));
typedef unsigned v4u __attribute__((ext_vector_type(4)));
typedef unsigned v2u __attribute__((ext_vector_type(2)));
typedef GAS unsigned gu32;
typedef unsigned short u16;
#define RLX_AGENT __ATOMIC_RELAXED, __HIP_MEMORY_SCOPE_AGENT

constexpr unsigned WS_EPOCH = 0, WS_TMO = 256;
constexpr unsigned WS_FKV = 4096, WS_FO = 8192, WS_FH = 12288, WS_FA = 16384, WS_FC = 20480, WS_FD = 24576;
constexpr unsigned WS_KVP = 32768, KVP_STRIDE = 3456;
constexpr unsigned WS_O = WS_KVP + 256 * KVP_STRIDE;
constexpr unsigned WS_H = WS_O + 2621440;
constexpr unsigned WS_HS = WS_H + 2621440;
constexpr unsigned WS_ACT = WS_HS + 262144;
constexpr unsigned WS_CS = WS_ACT + 7077888;
constexpr unsigned WS_W3 = WS_CS + 16 * 40 * 448;
constexpr unsigned WS_W4 = WS_W3 + 16 * 280 * 1024;
constexpr unsigned WS_STAMP = WS_W4 + 8 * 270 * 1024;
constexpr unsigned WS_END = WS_STAMP + 256 * 64 * 8;

constexpr int LDS_BYTES = 163840;
constexpr int L_KVF = 102400;
constexpr int L_KVC = 135168;
constexpr int L_BD = 138752;
constexpr int L_STAT = 146944;
constexpr int L_CSUM = 147968;
constexpr int L_SIDE = 148480;
constexpr int L_CS3 = 151168;
constexpr int L_HT = 153600;
constexpr int L_RED = 86016;
constexpr int L_X3 = 86016;

#define STAMPS 0

__device__ __forceinline__ void glds(unsigned voff, const void* sbase, unsigned ldsaddr) {
  asm volatile("s_mov_b32 m0, %2\n\ts_nop 0\n\tglobal_load_lds_dwordx4 %0, %1" ::"v"(voff), "s"(sbase), "s"(ldsaddr) : "memory");
}
__device__ __forceinline__ void glds2_sc1(unsigned voff, const void* sbase, unsigned ldsaddr) {
  asm volatile("s_mov_b32 m0, %2\n\ts_nop 0\n\tglobal_load_lds_dwordx4 %0, %1 sc1\n\tglobal_load_lds_dwordx4 %0, %1 offset:1024 sc1" ::"v"(voff), "s"(sbase), "s"(ldsaddr) : "memory");
}
template <int IMM> __device__ __forceinline__ void gldsi(unsigned voff, const void* sbase, unsigned lbase) {
  asm volatile("s_add_u32 m0, %2, %3\n\ts_nop 0\n\tglobal_load_lds_dwordx4 %0, %1" ::"v"(voff), "s"(sbase), "s"(lbase), "i"(IMM) : "memory", "scc");
}
template <int IMM> __device__ __forceinline__ void glds2i_sc1(unsigned voff, const void* sbase, unsigned lbase) {
  asm volatile("s_add_u32 m0, %2, %3\n\ts_nop 0\n\tglobal_load_lds_dwordx4 %0, %1 sc1\n\tglobal_load_lds_dwordx4 %0, %1 offset:1024 sc1" ::"v"(voff), "s"(sbase), "s"(lbase), "i"(IMM) : "memory", "scc");
}
template <int N, int IMM> __device__ __forceinline__ void gldsn_sc1(unsigned voff, const void* sbase, unsigned lbase) {
  static_assert(N >= 1 && N <= 4, "");
  if constexpr (N == 1) asm volatile("s_add_u32 m0, %2, %3\n\ts_nop 0\n\tglobal_load_lds_dwordx4 %0, %1 sc1" ::"v"(voff), "s"(sbase), "s"(lbase), "i"(IMM) : "memory", "scc");
  if constexpr (N == 2) asm volatile("s_add_u32 m0, %2, %3\n\ts_nop 0\n\tglobal_load_lds_dwordx4 %0, %1 sc1\n\tglobal_load_lds_dwordx4 %0, %1 offset:1024 sc1" ::"v"(voff), "s"(sbase), "s"(lbase), "i"(IMM) : "memory", "scc");
  if constexpr (N == 3) asm volatile("s_add_u32 m0, %2, %3\n\ts_nop 0\n\tglobal_load_lds_dwordx4 %0, %1 sc1\n\tglobal_load_lds_dwordx4 %0, %1 offset:1024 sc1\n\tglobal_load_lds_dwordx4 %0, %1 offset:2048 sc1" ::"v"(voff), "s"(sbase), "s"(lbase), "i"(IMM) : "memory", "scc");
  if constexpr (N == 4) asm volatile("s_add_u32 m0, %2, %3\n\ts_nop 0\n\tglobal_load_lds_dwordx4 %0, %1 sc1\n\tglobal_load_lds_dwordx4 %0, %1 offset:1024 sc1\n\tglobal_load_lds_dwordx4 %0, %1 offset:2048 sc1\n\tglobal_load_lds_dwordx4 %0, %1 offset:3072 sc1" ::"v"(voff), "s"(sbase), "s"(lbase), "i"(IMM) : "memory", "scc");
}
template <int IMM, int OFF> __device__ __forceinline__ void gldso_sc1(unsigned voff, const void* sbase, unsigned lbase) {
  asm volatile("s_add_u32 m0, %2, %3\n\ts_nop 0\n\tglobal_load_lds_dwordx4 %0, %1 offset:%4 sc1" ::"v"(voff), "s"(sbase), "s"(lbase), "i"(IMM), "i"(OFF) : "memory", "scc");
}
template <int N> __device__ __forceinline__ void waitvm() { asm volatile("s_waitcnt vmcnt(%0)" ::"i"(N) : "memory"); }
__device__ __forceinline__ f32x2 pfma(f32x2 a, f32x2 b, f32x2 c) { return __builtin_elementwise_fma(a, b, c); }
__device__ __forceinline__ f32x2 sp2(float a) { return f32x2{a, a}; }
__device__ __forceinline__ f32x2 lo2(f32x4 v) { return __builtin_shufflevector(v, v, 0, 1); }
__device__ __forceinline__ f32x2 hi2(f32x4 v) { return __builtin_shufflevector(v, v, 2, 3); }
__device__ __forceinline__ unsigned pk2v(f32x2 v) { return __builtin_bit_cast(unsigned, __builtin_convertvector(v, bf16x2)); }
__device__ __forceinline__ f32x2 phi2(f32x2 te) {
  const f32x2 y = pfma(te, sp2(1.44269504088896f), sp2(-1.44269504088896e-6f));
  const f32x2 e = {__builtin_amdgcn_fmed3f(__builtin_amdgcn_exp2f(y.x), 0.f, 1.f), __builtin_amdgcn_fmed3f(__builtin_amdgcn_exp2f(y.y), 0.f, 1.f)};
  const f32x2 m = {fmaxf(te.x, 1e-6f), fmaxf(te.y, 1e-6f)};
  return m + e;
}
__device__ __forceinline__ unsigned geglu2(f32x2 ag, f32x2 au, f32x2 cg, f32x2 cu, float rstd, float mr) {
  const f32x2 gv = pfma(ag, sp2(rstd), cg * sp2(mr)), uv = pfma(au, sp2(rstd), cu * sp2(mr));
  const f32x2 z = gv * pfma(gv * gv, sp2(-0.1029432397f), sp2(-2.302208198f));
  const f32x2 d = {1.0f + __builtin_amdgcn_exp2f(z.x), 1.0f + __builtin_amdgcn_exp2f(z.y)};
  const f32x2 rc = {__builtin_amdgcn_rcpf(d.x), __builtin_amdgcn_rcpf(d.y)};
  return pk2v(gv * uv * rc);
}
template <int V> using IC = std::integral_constant<int, V>;
template <int I, int N, class F> __device__ __forceinline__ void static_for(F&& f) {
  if constexpr (I < N) { f(std::integral_constant<int, I>{}); static_for<I + 1, N>(f); }
}
#define LBAR() asm volatile("s_waitcnt lgkmcnt(0)\n\ts_barrier" ::: "memory")
#define DRAIN() asm volatile("s_waitcnt vmcnt(0)" ::: "memory")
#define SB() __builtin_amdgcn_sched_barrier(0)
template <int N> __device__ __forceinline__ void waitbar() { asm volatile("s_waitcnt vmcnt(%0) lgkmcnt(0)\n\ts_barrier" ::"i"(N) : "memory"); }

__device__ __forceinline__ unsigned pk2(float a, float b) {
  bf16x2 v = {(__bf16)a, (__bf16)b};
  return __builtin_bit_cast(unsigned, v);
}
__device__ __forceinline__ float bflo(unsigned u) { return __uint_as_float(u << 16); }
__device__ __forceinline__ float bfhi(unsigned u) { return __uint_as_float(u & 0xffff0000u); }
__device__ __forceinline__ f32x4 mfma(v4u a, v4u b, f32x4 c) {
  return __builtin_amdgcn_mfma_f32_16x16x32_bf16(__builtin_bit_cast(bf16x8, a), __builtin_bit_cast(bf16x8, b), c, 0, 0, 0);
}
__device__ __forceinline__ v4u lds128(LAS char* L, int off) { return *(const LAS v4u*)(L + off); }
__device__ __forceinline__ v4u ldtr(LAS char* L, int off, int d) {
  s16x4 a = __builtin_amdgcn_ds_read_tr16_b64_v4i16((LAS s16x4*)(L + off));
  s16x4 b = __builtin_amdgcn_ds_read_tr16_b64_v4i16((LAS s16x4*)(L + off + d));
  v2u x = __builtin_bit_cast(v2u, a), y = __builtin_bit_cast(v2u, b);
  return v4u{x.x, x.y, y.x, y.y};
}
__device__ __forceinline__ float phi_eps(float te) {
  const float e = __builtin_amdgcn_fmed3f(__builtin_amdgcn_exp2f((te - 1e-6f) * 1.44269504088896f), 0.f, 1.f);
  return fmaxf(te, 1e-6f) + e;
}
__device__ __forceinline__ v4u ldg_sc1(__amdgpu_buffer_rsrc_t rs, unsigned off) { return __builtin_amdgcn_raw_buffer_load_b128(rs, off, 0, 16); }
__device__ __forceinline__ v2u ldg2_sc1(__amdgpu_buffer_rsrc_t rs, unsigned off) { return __builtin_amdgcn_raw_buffer_load_b64(rs, off, 0, 16); }
__device__ __forceinline__ unsigned ldg1_sc1(__amdgpu_buffer_rsrc_t rs, unsigned off) { return __builtin_amdgcn_raw_buffer_load_b32(rs, off, 0, 16); }
__device__ __forceinline__ void stg_sc1(__amdgpu_buffer_rsrc_t rs, unsigned off, v4u v) { __builtin_amdgcn_raw_buffer_store_b128(v, rs, off, 0, 16); }
__device__ __forceinline__ void stg2_sc1(__amdgpu_buffer_rsrc_t rs, unsigned off, v2u v) { __builtin_amdgcn_raw_buffer_store_b64(v, rs, off, 0, 16); }
__device__ __forceinline__ void stg1_sc1(__amdgpu_buffer_rsrc_t rs, unsigned off, unsigned v) { __builtin_amdgcn_raw_buffer_store_b32(v, rs, off, 0, 16); }

__device__ __forceinline__ void wait16(char* ws, unsigned off, unsigned tag, int lane) {
  gu32* f = (gu32*)(ws + off) + (lane & 15);
  for (unsigned sp = 0; sp < (1u << 15); ++sp) {
    unsigned v = __hip_atomic_load(f, RLX_AGENT);
    if (__all(v == tag)) return;
    __builtin_amdgcn_s_sleep(1);
  }
  if (lane == 0) __hip_atomic_store((gu32*)(ws + WS_TMO), 1u, RLX_AGENT);
}
__device__ __forceinline__ void wait32(char* ws, unsigned off, unsigned tag, int lane) {
  gu32* f = (gu32*)(ws + off) + (lane & 31);
  for (unsigned sp = 0; sp < (1u << 15); ++sp) {
    unsigned v = __hip_atomic_load(f, RLX_AGENT);
    if (__all(v == tag)) return;
    __builtin_amdgcn_s_sleep(1);
  }
  if (lane == 0) __hip_atomic_store((gu32*)(ws + WS_TMO), 1u, RLX_AGENT);
}
__device__ __forceinline__ void set_flag(char* ws, unsigned off, unsigned tag) { __hip_atomic_store((gu32*)(ws + off), tag, RLX_AGENT); }
#define STAMPW(i, wv) do {} while (0)
#define STAMP(i) STAMPW(i, 0)

__device__ __forceinline__ void splitk_exchange(LAS char* L, f32x4 (&acc)[5][2], f32x4 (&own)[3], int th, int kq, int l) {
#pragma unroll
  for (int T = 0; T < 5; ++T)
#pragma unroll
    for (int s = 0; s < 2; ++s) {
      const int ko = s + ((T == 2 || T == 3) ? 2 : 0);
      if (kq != ko) *(LAS f32x4*)(L + L_RED + ((((th * 2 + s) * 5 + T) * 3) + (kq < ko ? kq : kq - 1)) * 1024 + l * 16) = acc[T][s];
    }
  f32x4 t[5][2];
#pragma unroll
  for (int T = 0; T < 5; ++T)
#pragma unroll
    for (int s = 0; s < 2; ++s) { t[T][s] = acc[T][s]; asm volatile("" : "+v"(t[T][s])); }
  f32x4 a0 = t[0][0], a1 = t[1][0], a2 = t[4][0];
  if (kq == 1) { a0 = t[0][1]; a1 = t[1][1]; a2 = t[4][1]; asm volatile("" : "+v"(a0), "+v"(a1), "+v"(a2)); }
  else if (kq == 2) { a0 = t[2][0]; a1 = t[3][0]; asm volatile("" : "+v"(a0), "+v"(a1)); }
  else if (kq == 3) { a0 = t[2][1]; a1 = t[3][1]; asm volatile("" : "+v"(a0), "+v"(a1)); }
  LBAR();
  const bool hi = kq >= 2;
  const int tb = L_RED + (th * 2 + (kq & 1)) * 5 * 3 * 1024 + l * 16;
  const int T0 = hi ? 2 : 0, T1 = hi ? 3 : 1;
  f32x4 v[9];
#pragma unroll
  for (int k = 0; k < 3; ++k) {
    v[k] = *(const LAS f32x4*)(L + tb + (T0 * 3 + k) * 1024);
    v[3 + k] = *(const LAS f32x4*)(L + tb + (T1 * 3 + k) * 1024);
    v[6 + k] = *(const LAS f32x4*)(L + tb + (4 * 3 + k) * 1024);
  }
  own[0] = (a0 + v[0]) + (v[1] + v[2]);
  own[1] = (a1 + v[3]) + (v[4] + v[5]);
  own[2] = (a2 + v[6]) + (v[7] + v[8]);
}

template <int NT> struct Frag3 { v4u x[2], g[NT], u[NT]; };

template <int NT, int TILE0, int NWI>
__device__ __forceinline__ void p3_body(LAS char* L, char* ws, __amdgpu_buffer_rsrc_t rs, int r, int p, int w, int l, unsigned tag, int wi0) {
  const int c = l & 15, g = l >> 4, tg = w & 3;
  const unsigned l16 = l * 16;
  const char* wsrc = ws + WS_W3 + (size_t)p * 280 * 1024 + wi0 * 1024;
  const char* hx = ws + WS_H + (size_t)(8 * r + w) * 20 * 1024;
  const unsigned lw = wi0 * 1024, lx = L_X3 + w * 2048;
  gldsn_sc1<NWI, 0>(l16, wsrc, lw);
  gldsn_sc1<NWI, 28672>(l16, wsrc + 28672, lw);
  gldsn_sc1<NWI, 2 * 28672>(l16, wsrc + 2 * 28672, lw);
  STAMP(32);
  if (w == 7) { wait16(ws, WS_FH + r * 256, tag, l); STAMPW(33, 7); }
  DRAIN();
  STAMP(34);
  LBAR();
  STAMP(10);
  gldsn_sc1<2, 0>(l16, hx, lx); gldsn_sc1<2, 16384>(l16, hx + 2048, lx); gldsn_sc1<2, 32768>(l16, hx + 4096, lx);
  v2u stp[2][4];
#pragma unroll
  for (int s = 0; s < 2; ++s)
#pragma unroll
    for (int i = 0; i < 4; ++i) stp[s][i] = ldg2_sc1(rs, WS_HS + ((8 * r + 2 * tg + s) * 16 + 4 * g + i) * 128 + c * 8);

  f32x4 ag[NT][2], au[NT][2];
#pragma unroll
  for (int t = 0; t < NT; ++t) { ag[t][0] = ag[t][1] = au[t][0] = au[t][1] = f32x4{0, 0, 0, 0}; }
  const int xo = L_X3 + tg * 4096 + l16;
  constexpr int NPS = NWI + 2;
  int wb0 = TILE0 * 1024 + l16, wb2 = wb0 + 2 * 28672;
  asm volatile("" : "+v"(wb0), "+v"(wb2));
  auto rd1 = [&](Frag3<NT>& f, auto SLOT, auto KS, auto I) {
    constexpr int slot = SLOT, ks = KS, i = I;
    if constexpr (i < 2) f.x[i] = lds128(L, xo + slot * 16384 + ks * 1024 + i * 2048);
    else {
      constexpr int j = i - 2, t = j >> 1, u = j & 1, off = ks * 14336 + u * 7168 + t * 1024;
      const v4u v = slot < 2 ? lds128(L, wb0 + slot * 28672 + off) : lds128(L, wb2 + off);
      if constexpr (u == 0) f.g[t] = v; else f.u[t] = v;
    }
  };
  auto mma1 = [&](const Frag3<NT>& f, auto I) {
    constexpr int i = I, t = i >> 2, u = (i >> 1) & 1, s = i & 1;
    if constexpr (u == 0) ag[t][s] = mfma(f.g[t], f.x[s], ag[t][s]); else au[t][s] = mfma(f.u[t], f.x[s], au[t][s]);
  };
  constexpr int NM = 4 * NT, NR = 2 + 2 * NT;
  Frag3<NT> A, B;
  waitbar<4 + 8>();
  STAMP(35);
  static_for<0, NR>([&](auto I) { rd1(A, IC<0>{}, IC<0>{}, I); });
  static_for<0, 10>([&](auto ST) {
    constexpr int st = ST, slot = st % 3, nslot = (st + 1) % 3;
    static_for<0, NM>([&](auto I) {
      constexpr int i = I;
      mma1(A, I);
      if constexpr (i < NR) rd1(B, IC<slot>{}, IC<1>{}, I);
      SB();
    });
    if constexpr (st < 9) {
      if constexpr (st == 0) waitbar<2 + 8>(); else if constexpr (st < 8) waitbar<NPS>(); else waitbar<0>();
    }
    if constexpr (st == 2) STAMP(45);
    if constexpr (st == 5) STAMP(46);
    if constexpr (st == 8) STAMP(47);
    static_for<0, NM>([&](auto I) {
      constexpr int i = I;
      mma1(B, I);
      if constexpr (st <= 6 && (i & 1) == 0) {
        constexpr int ns = st + 3, q = i >> 1;
        if constexpr (q < NWI) gldso_sc1<slot * 28672, q * 1024>(l16, wsrc + ns * 28672, lw);
        else if constexpr (q < NWI + 2) gldso_sc1<slot * 16384, (q - NWI) * 1024>(l16, hx + ns * 2048, lx);
      }
      if constexpr (st < 9) {
        if constexpr (i < NR) rd1(A, IC<nslot>{}, IC<0>{}, I);
      }
      SB();
    });
  });
  STAMP(11);
  f32x4 cgv[NT], cuv[NT];
#pragma unroll
  for (int t = 0; t < NT; ++t) {
    cgv[t] = *(const LAS f32x4*)(L + L_CS3 + ((TILE0 + t) * 16 + 4 * g) * 4);
    cuv[t] = *(const LAS f32x4*)(L + L_CS3 + (112 + (TILE0 + t) * 16 + 4 * g) * 4);
  }
#pragma unroll
  for (int s = 0; s < 2; ++s) {
    float sm = 0.f, sq = 0.f;
#pragma unroll
    for (int i = 0; i < 4; ++i) { sm += __uint_as_float(stp[s][i].x); sq += __uint_as_float(stp[s][i].y); }
    sm += __shfl_xor(sm, 16); sq += __shfl_xor(sq, 16);
    sm += __shfl_xor(sm, 32); sq += __shfl_xor(sq, 32);
    const float mean = sm * (1.0f / 640.0f);
    const float rstd = __builtin_amdgcn_rsqf(fmaxf(sq * (1.0f / 640.0f) - mean * mean, 0.f) + 1e-5f);
    const float mr = -mean * rstd;
    unsigned y[NT][2];
    const unsigned ab = WS_ACT + (8 * r + 2 * tg + s) * 54 * 1024 + l16;
#pragma unroll
    for (int t = 0; t < NT; ++t) {
      y[t][0] = geglu2(lo2(ag[t][s]), lo2(au[t][s]), lo2(cgv[t]), lo2(cuv[t]), rstd, mr);
      y[t][1] = geglu2(hi2(ag[t][s]), hi2(au[t][s]), hi2(cgv[t]), hi2(cuv[t]), rstd, mr);
      if constexpr (true) {
        if (t & 1) {
          if (TILE0 == 0) { if (p < 15) stg_sc1(rs, ab + (3 * p + (t >> 1)) * 1024, v4u{y[t - 1][0], y[t - 1][1], y[t][0], y[t][1]}); }
          else stg_sc1(rs, ab + (p < 15 ? 3 * p + 2 : 45) * 1024, v4u{y[t - 1][0], y[t - 1][1], y[t][0], y[t][1]});
        }
      }
    }
    if (TILE0 != 0) stg2_sc1(rs, ab + (46 + (p >> 1)) * 1024 + (p & 1) * 8, v2u{y[NT - 1][0], y[NT - 1][1]});
  }
}

struct FragKV { v4u x[2], w[5]; };
struct FragQ { v4u x[2], w[3], e0, e1; };

__global__ __launch_bounds__(512, 2) void fused_block(const u16* __restrict__ x, const u16* __restrict__ wqkv, const u16* __restrict__ wo,
                                                      const u16* __restrict__ wg, const u16* __restrict__ wu, const u16* __restrict__ wd,
                                                      u16* __restrict__ out, char* __restrict__ ws) {
  extern __shared__ __attribute__((aligned(16))) char smem_[];
  LAS char* L = (LAS char*)smem_;
  const int tid = threadIdx.x, l = tid & 63, c = l & 15, g = l >> 4;
  const int w = __builtin_amdgcn_readfirstlane(tid >> 6);
  const int b = blockIdx.x, xcd = b & 7, jj = b >> 3, rg = xcd >> 1, cgp = xcd & 1;
  const int r = 4 * rg + (jj >> 3), p = 8 * cgp + (jj & 7);
  const int r2 = 8 * rg + (jj >> 2), cs = 4 * cgp + (jj & 3);
  const __amdgpu_buffer_rsrc_t rs = __builtin_amdgcn_make_buffer_rsrc(ws, 0, WS_END, 0x00020000);
  const v4u ONES = {0x3F803F80u, 0x3F803F80u, 0x3F803F80u, 0x3F803F80u};
  const int q4 = (l & 15) >> 2, pp = l & 3;
  STAMP(0);

  unsigned tag;
  f32x4 qacc[3][2];
#pragma unroll
  for (int t = 0; t < 3; ++t) { qacc[t][0] = f32x4{0, 0, 0, 0}; qacc[t][1] = f32x4{0, 0, 0, 0}; }

  {
    unsigned vx[2], vw[2];
    bool aw[2];
#pragma unroll
    for (int i = 0; i < 2; ++i) {
      const int tok = 8 * (2 * w + i) + (l >> 3);
      vx[i] = tok * 1280 + (((l & 7) ^ (tok & 7)) << 4);
      const int a = 2 * w + i;
      int pos, col;
      if (a < 10) {
        const int n = 64 * a + l; pos = n / 10; const int ch = n - pos * 10;
        col = (ch < 5) ? 640 + 40 * p + 8 * ch : 1280 + 40 * p + 8 * (ch - 5); aw[i] = true;
      } else {
        const int n = 64 * (a - 10) + l; pos = n / 6; const int ch = n - pos * 6;
        col = 40 * p + 8 * ch; aw[i] = ch < 5;
      }
      const int k = (pos & ~12) | ((pos & 4) << 1) | ((pos & 8) >> 1);
      vw[i] = k * 3840 + col * 2;
    }
    const char* xb = (const char*)x + (size_t)r * 128 * 1280;
    const unsigned lxw = 2 * w * 1024;
    auto issue1 = [&](auto ST, auto I) {
      constexpr int st = ST, i = I;
      if constexpr (i < 2) gldsi<(st & 3) * 32768 + i * 1024>(vx[i], xb + st * 128, lxw);
      else { if (aw[i - 2]) gldsi<(st & 3) * 32768 + 16384 + (i - 2) * 1024>(vw[i - 2], (const char*)wqkv + (size_t)st * 64 * 3840, lxw); }
    };
    static_for<0, 4>([&](auto ST) { static_for<0, 4>([&](auto I) { issue1(ST, I); }); });
    if (tid < 256) {
      const int sl = tid >> 6, row = tid & 63;
      *(LAS v4u*)(L + sl * 32768 + 26624 + row * 96 + 80) = v4u{0x00003F80u, 0u, 0u, 0u};
    }
    *(LAS v4u*)(L + L_BD + tid * 16) = v4u{0, 0, 0, 0};
    { unsigned e; asm volatile("s_load_dword %0, %1, 0x0\n\ts_waitcnt lgkmcnt(0)" : "=s"(e) : "s"(ws + WS_EPOCH) : "memory"); tag = e + 1u; }
    STAMP(1);

    const int xo0 = c * 128 + ((g ^ (c & 7)) << 4);
    const int wrow = 16 * (g >> 1) + 4 * (g & 1) + q4;
    const int kvo = 16384 + wrow * 160 + 8 * pp;
    const int qo = 16384 + 10240 + wrow * 96 + 8 * pp;

    if (w < 4) {
      f32x4 acc[2][5];
#pragma unroll
      for (int s = 0; s < 2; ++s)
#pragma unroll
        for (int t = 0; t < 5; ++t) acc[s][t] = f32x4{0, 0, 0, 0};
      const int xw = w * 32 * 128;
      int xbs[2][2], kbs[4];
      xbs[0][0] = xw + xo0; xbs[0][1] = xw + (xo0 ^ 64); xbs[1][0] = xbs[0][0] + 65536; xbs[1][1] = xbs[0][1] + 65536;
      kbs[0] = kvo; kbs[1] = kvo + 32768; kbs[2] = kvo + 65536; kbs[3] = kvo + 98304;
      asm volatile("" : "+v"(xbs[0][0]), "+v"(xbs[0][1]), "+v"(xbs[1][0]), "+v"(xbs[1][1]), "+v"(kbs[0]), "+v"(kbs[1]), "+v"(kbs[2]), "+v"(kbs[3]));
      auto rd1 = [&](FragKV& f, auto SLOT, auto KS, auto I) {
        constexpr int slot = SLOT, ks = KS, i = I;
        if constexpr (i < 2) f.x[i] = lds128(L, xbs[slot >> 1][ks] + (slot & 1) * 32768 + i * 2048);
        else {
          constexpr int j = i - 2, t = j >> 1, h = j & 1;
          const v2u v = __builtin_bit_cast(v2u, __builtin_amdgcn_ds_read_tr16_b64_v4i16((LAS s16x4*)(L + kbs[slot] + ks * 32 * 160 + t * 32 + h * 8 * 160)));
          if constexpr (h == 0) { f.w[t].x = v.x; f.w[t].y = v.y; } else { f.w[t].z = v.x; f.w[t].w = v.y; }
        }
      };
      auto mma1 = [&](const FragKV& f, auto I) { constexpr int i = I, t = i >> 1, s = i & 1; acc[s][t] = mfma(f.x[s], f.w[t], acc[s][t]); };
      FragKV A, B;
      waitbar<12>();
      static_for<0, 12>([&](auto I) { rd1(A, IC<0>{}, IC<0>{}, I); });
      static_for<0, 10>([&](auto ST) {
        constexpr int st = ST;
        static_for<0, 10>([&](auto I) {
          constexpr int i = I;
          mma1(A, I);
          if constexpr (i < 6) { rd1(B, IC<(st & 3)>{}, IC<1>{}, IC<2 * i>{}); rd1(B, IC<(st & 3)>{}, IC<1>{}, IC<2 * i + 1>{}); }
          SB();
        });
        if constexpr (st < 9) { if constexpr (st < 7) waitbar<8>(); else if constexpr (st == 7) waitbar<4>(); else waitbar<0>(); }
        static_for<0, 10>([&](auto I) {
          constexpr int i = I;
          mma1(B, I);
          if constexpr (st + 4 < 10 && i < 4) issue1(IC<st + 4>{}, I);
          if constexpr (st < 9 && i >= 2 && i < 8) { rd1(A, IC<((st + 1) & 3)>{}, IC<0>{}, IC<2 * (i - 2)>{}); rd1(A, IC<((st + 1) & 3)>{}, IC<0>{}, IC<2 * (i - 2) + 1>{}); }
          SB();
        });
      });
      STAMP(2);
      __builtin_amdgcn_s_setprio(2);
      LBAR();
      STAMP(16);
      f32x4 rstd[2], mr[2];
#pragma unroll
      for (int s = 0; s < 2; ++s) {
        const f32x4 sm = *(const LAS f32x4*)(L + L_STAT + (32 * w + 16 * s + 4 * g) * 4) * (1.0f / 640.0f);
        const f32x4 sq = *(const LAS f32x4*)(L + L_STAT + 512 + (32 * w + 16 * s + 4 * g) * 4);
        const f32x2 v0 = pfma(lo2(sq), sp2(1.0f / 640.0f), pfma(-lo2(sm), lo2(sm), sp2(1e-5f)));
        const f32x2 v1 = pfma(hi2(sq), sp2(1.0f / 640.0f), pfma(-hi2(sm), hi2(sm), sp2(1e-5f)));
        rstd[s] = f32x4{__builtin_amdgcn_rsqf(fmaxf(v0.x, 1e-5f)), __builtin_amdgcn_rsqf(fmaxf(v0.y, 1e-5f)), __builtin_amdgcn_rsqf(fmaxf(v1.x, 1e-5f)), __builtin_amdgcn_rsqf(fmaxf(v1.y, 1e-5f))};
        mr[s] = -sm * rstd[s];
      }
      float cw[5];
#pragma unroll
      for (int t = 0; t < 5; ++t) cw[t] = *(const LAS float*)(L + L_CSUM + (16 * t + c) * 4);
#pragma unroll
      for (int t = 0; t < 5; ++t) {
        unsigned o[4];
#pragma unroll
        for (int s = 0; s < 2; ++s)
#pragma unroll
          for (int h = 0; h < 2; ++h) {
            const f32x2 a2 = h ? hi2(acc[s][t]) : lo2(acc[s][t]), r2_ = h ? hi2(rstd[s]) : lo2(rstd[s]), m2 = h ? hi2(mr[s]) : lo2(mr[s]);
            f32x2 y;
            if (t < 2) y = phi2(pfma(a2, r2_, pfma(m2, sp2(cw[t]), sp2(1e-6f))));
            else if (t == 2) {
              const f32x2 te = pfma(a2, r2_, pfma(m2, sp2(cw[t]), sp2(1e-6f))), ph = phi2(te), v = te - sp2(1e-6f);
              y = c < 8 ? ph : v;
            } else y = pfma(a2, r2_, m2 * sp2(cw[t]));
            o[2 * s + h] = pk2v(y);
          }
        *(LAS v4u*)(L + L_KVF + (w * 5 + t) * 1024 + l * 16) = v4u{o[0], o[1], o[2], o[3]};
      }
    } else {
      const int tw = w - 4;
      f32x4 sq[2], cs0, cs1;
      sq[0] = sq[1] = cs0 = cs1 = f32x4{0, 0, 0, 0};
      const int xw = tw * 32 * 128;
      const int c1o = kvo + (tw < 3 ? tw : 4) * 32;
      const int c0o = (tw < 3) ? qo + tw * 32 : kvo + 3 * 32;
      const int c0d = (tw < 3) ? 8 * 96 : 8 * 160, c0k = (tw < 3) ? 32 * 96 : 32 * 160;
      int xbs[2][2], qbs[4], e0b[4][2], e1b[4];
      xbs[0][0] = xw + xo0; xbs[0][1] = xw + (xo0 ^ 64); xbs[1][0] = xbs[0][0] + 65536; xbs[1][1] = xbs[0][1] + 65536;
#pragma unroll
      for (int q = 0; q < 4; ++q) { qbs[q] = qo + q * 32768; e1b[q] = c1o + q * 32768; e0b[q][0] = c0o + q * 32768; e0b[q][1] = c0o + c0k + q * 32768; }
      asm volatile("" : "+v"(xbs[0][0]), "+v"(xbs[0][1]), "+v"(xbs[1][0]), "+v"(xbs[1][1]), "+v"(qbs[0]), "+v"(qbs[1]), "+v"(qbs[2]), "+v"(qbs[3]));
      asm volatile("" : "+v"(e1b[0]), "+v"(e1b[1]), "+v"(e1b[2]), "+v"(e1b[3]), "+v"(e0b[0][0]), "+v"(e0b[1][0]), "+v"(e0b[2][0]), "+v"(e0b[3][0]),
                        "+v"(e0b[0][1]), "+v"(e0b[1][1]), "+v"(e0b[2][1]), "+v"(e0b[3][1]));
      int c0dv = c0d; asm volatile("" : "+v"(c0dv));
      int e0c[4][2];
#pragma unroll
      for (int q = 0; q < 4; ++q) { e0c[q][0] = e0b[q][0] + c0dv; e0c[q][1] = e0b[q][1] + c0dv; }
      auto rd1 = [&](FragQ& f, auto SLOT, auto KS, auto I) {
        constexpr int slot = SLOT, ks = KS, i = I;
        if constexpr (i < 2) f.x[i] = lds128(L, xbs[slot >> 1][ks] + (slot & 1) * 32768 + i * 2048);
        else {
          constexpr int j = i - 2, t = j >> 1, h = j & 1;
          int a;
          if constexpr (t < 3) a = qbs[slot] + ks * 32 * 96 + t * 32 + h * 8 * 96;
          else if constexpr (t == 3) a = e1b[slot] + ks * 32 * 160 + h * 8 * 160;
          else a = h ? e0c[slot][ks] : e0b[slot][ks];
          const v2u v = __builtin_bit_cast(v2u, __builtin_amdgcn_ds_read_tr16_b64_v4i16((LAS s16x4*)(L + a)));
          v4u& d = *(t < 3 ? &f.w[t < 3 ? t : 0] : t == 3 ? &f.e1 : &f.e0);
          if constexpr (h == 0) { d.x = v.x; d.y = v.y; } else { d.z = v.x; d.w = v.y; }
        }
      };
      auto mma1 = [&](const FragQ& f, auto I) {
        constexpr int i = I;
        if constexpr (i < 6) qacc[i >> 1][i & 1] = mfma(f.w[i >> 1], f.x[i & 1], qacc[i >> 1][i & 1]);
        else if constexpr (i < 8) sq[i - 6] = mfma(f.x[i - 6], f.x[i - 6], sq[i - 6]);
        else if constexpr (i == 8) cs1 = mfma(ONES, f.e1, cs1);
        else cs0 = mfma(ONES, f.e0, cs0);
      };
      FragQ A, B;
      waitbar<12>();
      static_for<0, 12>([&](auto I) { rd1(A, IC<0>{}, IC<0>{}, I); });
      static_for<0, 10>([&](auto ST) {
        constexpr int st = ST;
        static_for<0, 10>([&](auto I) {
          constexpr int i = I;
          mma1(A, I);
          if constexpr (i < 6) { rd1(B, IC<(st & 3)>{}, IC<1>{}, IC<2 * i>{}); rd1(B, IC<(st & 3)>{}, IC<1>{}, IC<2 * i + 1>{}); }
          SB();
        });
        if constexpr (st < 9) { if constexpr (st < 7) waitbar<8>(); else if constexpr (st == 7) waitbar<4>(); else waitbar<0>(); }
        static_for<0, 10>([&](auto I) {
          constexpr int i = I;
          mma1(B, I);
          if constexpr (st + 4 < 10 && i < 4) issue1(IC<st + 4>{}, I);
          if constexpr (st < 9 && i >= 2 && i < 8) { rd1(A, IC<((st + 1) & 3)>{}, IC<0>{}, IC<2 * (i - 2)>{}); rd1(A, IC<((st + 1) & 3)>{}, IC<0>{}, IC<2 * (i - 2) + 1>{}); }
          SB();
        });
      });
#pragma unroll
      for (int s = 0; s < 2; ++s) {
        if (g == 2) *(LAS float*)(L + L_STAT + (32 * tw + 16 * s + c) * 4) = qacc[2][s][0];
        if (g == (c >> 2)) {
          const int e = c & 3;
          const float v = e == 0 ? sq[s][0] : e == 1 ? sq[s][1] : e == 2 ? sq[s][2] : sq[s][3];
          *(LAS float*)(L + L_STAT + 512 + (32 * tw + 16 * s + c) * 4) = v;
        }
      }
      if (g == 0) {
        if (tw < 3) { *(LAS float*)(L + L_CSUM + (80 + 16 * tw + c) * 4) = cs0[0]; *(LAS float*)(L + L_CSUM + (16 * tw + c) * 4) = cs1[0]; }
        else { *(LAS float*)(L + L_CSUM + (48 + c) * 4) = cs0[0]; *(LAS float*)(L + L_CSUM + (64 + c) * 4) = cs1[0]; }
      }
      LBAR();
    }
    STAMP(17);
    LBAR();
    STAMP(18);
    {
      const int Ta = w < 2 ? 0 : w < 5 ? 1 : 2;
      const int Tb = (w == 0 || w == 2 || w == 7) ? 2 : (w == 4 || w == 6) ? 4 : 3;
      v4u A[4], B[4];
#pragma unroll
      for (int tg = 0; tg < 4; ++tg) {
        A[tg] = lds128(L, L_KVF + (tg * 5 + Ta) * 1024 + l * 16);
        B[tg] = lds128(L, L_KVF + (tg * 5 + Tb) * 1024 + l * 16);
      }
      f32x4 d = {0, 0, 0, 0};
      const bool one = Tb == 2 && c < 8;
#pragma unroll
      for (int tg = 0; tg < 4; ++tg) d = mfma(A[tg], one ? ONES : B[tg], d);
      const int kc = 16 * Ta + 4 * g;
      const int vc = 16 * Tb + c - 40;
      if (kc < 40) {
        const int hk = kc >= 20;
        if (vc >= 0) { if (hk == (vc >= 20)) *(LAS f32x4*)(L + L_KVC + (vc * 20 + kc - 20 * hk) * 4) = d; }
        else if (c == 0) *(LAS f32x4*)(L + L_KVC + (800 + kc) * 4) = d;
      }
    }
    LBAR();
    STAMP(19);
    if (w == 0) {
      v4u v[4];
#pragma unroll
      for (int i = 0; i < 4; ++i) v[i] = lds128(L, L_KVC + min(i * 64 + l, 209) * 16);
#pragma unroll
      for (int i = 0; i < 4; ++i) if (i * 64 + l < 210) stg_sc1(rs, WS_KVP + (p * 16 + r) * KVP_STRIDE + (i * 64 + l) * 16, v[i]);
      STAMP(20);
      DRAIN();
      if (l == 0) set_flag(ws, WS_FKV + p * 256 + r * 4, tag);
    }
    __builtin_amdgcn_s_setprio(0);
    STAMP(3);
  }

  v4u pqA[2], pqB[2];
  pqA[0] = pqA[1] = pqB[0] = pqB[1] = v4u{0, 0, 0, 0};
  if (w >= 4) {
    const int tw = w - 4;
#pragma unroll
    for (int s = 0; s < 2; ++s) {
      const float sm = *(const LAS float*)(L + L_STAT + (32 * tw + 16 * s + c) * 4);
      const float sq2 = *(const LAS float*)(L + L_STAT + 512 + (32 * tw + 16 * s + c) * 4);
      const float m = sm * (1.0f / 640.0f);
      const float rs_ = __builtin_amdgcn_rsqf(fmaxf(sq2 * (1.0f / 640.0f) - m * m + 1e-5f, 1e-5f));
      const float mr = -m * rs_;
      unsigned pq[6];
#pragma unroll
      for (int t = 0; t < 3; ++t) {
        const f32x4 cw = *(const LAS f32x4*)(L + L_CSUM + (80 + 16 * t + 4 * g) * 4);
        pq[2 * t] = pk2v(phi2(pfma(lo2(qacc[t][s]), sp2(rs_), pfma(lo2(cw), sp2(mr), sp2(1e-6f)))));
        pq[2 * t + 1] = pk2v(phi2(pfma(hi2(qacc[t][s]), sp2(rs_), pfma(hi2(cw), sp2(mr), sp2(1e-6f)))));
        if (t == 2 && g >= 2) { pq[4] = 0u; pq[5] = 0u; }
      }
      pqA[s] = v4u{pq[0], pq[1], pq[2], pq[3]};
      pqB[s] = v4u{pq[4], pq[5], 0u, 0u};
    }
  } else if (w > 0) {
    const int f0 = min(7 * p, 101);
    const int u3 = min(r + 16 * (w - 1), 39), ks3 = u3 >> 1, mat = u3 & 1;
    const int ks4 = min(r2 + 32 * (w - 1), 53);
    const unsigned area = L_KVF + (w - 1) * 12288;
    const char* w3 = (const char*)(mat ? wu : wg);
#pragma unroll
    for (int i = 0; i < 7; ++i) {
      const int n = 64 * i + l, pos = n / 14, ch = n - pos * 14;
      const int feat = ks3 < 16 ? 80 * (ks3 >> 1) + 32 * (ks3 & 1) + pos : 160 * (ks3 - 16) + 64 + (pos & 15) + 80 * (pos >> 4);
      glds(feat * 3456 + (f0 * 16 + 8 * ch) * 2, w3, area + i * 1024);
    }
    if (w < 3) {
#pragma unroll
      for (int i = 0; i < 5; ++i) {
        const int n = 64 * i + l, pos = n / 10, ch = n - pos * 10, t = pos >> 4;
        const int sp = ks4 < 45 ? ks4 / 3 : ks4 == 45 ? 15 : 2 * (ks4 - 46) + t;
        const int lt = ks4 < 45 ? 2 * (ks4 % 3) + t : ks4 == 45 ? 4 + t : 6;
        const int row = 16 * (min(7 * sp, 101) + lt) + (pos & 15);
        glds(row * 1280 + (80 * cs + 8 * ch) * 2, wd, area + 7168 + i * 1024);
      }
    }
#pragma unroll 1
    for (int i = w - 1; i < 100; i += 3) {
      const int n = 64 * i + l, pos = n / 10, ch2 = n - pos * 10;
      const int ks = pos >> 5, p5 = pos & 31;
      int feat;
      if (ks < 16) feat = 40 * ks + p5;
      else { const int t = p5 >> 4, gg = (p5 >> 2) & 3, qq = p5 & 3; feat = 40 * (4 * (ks - 16) + 2 * (gg >> 1) + t) + 32 + 4 * (gg & 1) + qq; }
      glds(feat * 1280 + (80 * cs + 8 * ch2) * 2, wo, i * 1024);
    }
    if (w < 3) waitvm<33 + 5>(); else waitvm<33>();
    {
      const int tb = area + (4 * g + q4) * 224 + 8 * pp;
      v4u fr[7];
#pragma unroll
      for (int t = 0; t < 7; ++t) fr[t] = ldtr(L, tb + t * 32, 16 * 224);
      const bool ok = r + 16 * (w - 1) < 40;
      float csel0 = 0.f, csel1 = 0.f;
#pragma unroll
      for (int t = 0; t < 7; ++t) {
        if (ok) stg_sc1(rs, WS_W3 + ((p * 20 + ks3) * 14 + mat * 7 + t) * 1024 + l * 16, fr[t]);
        const f32x4 cs_ = mfma(ONES, fr[t], f32x4{0, 0, 0, 0});
        if (t < 4) csel0 = (g == t) ? cs_[0] : csel0; else csel1 = (g == t - 4) ? cs_[0] : csel1;
      }
      if (ok) {
        stg1_sc1(rs, WS_CS + (p * 40 + u3) * 448 + l * 4, __float_as_uint(csel0));
        if (l < 48) stg1_sc1(rs, WS_CS + (p * 40 + u3) * 448 + 256 + l * 4, __float_as_uint(csel1));
      }
    }
    if (w < 3) {
      waitvm<33>();
      const int tb = area + 7168 + (4 * g + q4) * 160 + 8 * pp;
      v4u fr[5];
#pragma unroll
      for (int t = 0; t < 5; ++t) fr[t] = ldtr(L, tb + t * 32, 16 * 160);
      if (r2 + 32 * (w - 1) < 54) {
#pragma unroll
        for (int t = 0; t < 5; ++t) stg_sc1(rs, WS_W4 + ((cs * 54 + ks4) * 5 + t) * 1024 + l * 16, fr[t]);
      }
    }
  } else {
    wait16(ws, WS_FKV + p * 256, tag, l);
    STAMP(21);
  }
  STAMPW(22, 4); STAMPW(23, 1);
  LBAR();
  STAMP(4);
  {
    if (w >= 4 && tid - 256 < 210) {
      const int t = tid - 256;
      v4u v[16];
#pragma unroll
      for (int i = 0; i < 16; ++i) v[i] = ldg_sc1(rs, WS_KVP + (p * 16 + i) * KVP_STRIDE + t * 16);
      f32x4 s = {0, 0, 0, 0};
#pragma unroll
      for (int i = 0; i < 16; ++i) s += __builtin_bit_cast(f32x4, v[i]);
      const v2u pk = {pk2(s[0], s[1]), pk2(s[2], s[3])};
      if (t < 200) {
        const int fo = t / 5, f = 4 * (t - fo * 5) + (fo >= 20 ? 20 : 0);
        const int kst = f >> 5, tt = (f >> 4) & 1, gg = (f >> 2) & 3;
        *(LAS v2u*)(L + L_BD + ((fo >> 4) * 2 + kst) * 1024 + (gg * 16 + (fo & 15)) * 16 + tt * 8) = pk;
      } else {
        const int f = 4 * (t - 200), hd = f >= 20;
        const int kst = f >> 5, tt = (f >> 4) & 1, gg = (f >> 2) & 3;
#pragma unroll
        for (int i = 0; i < 8; ++i) *(LAS v2u*)(L + L_BD + (6 + kst) * 1024 + (gg * 16 + 2 * i + hd) * 16 + tt * 8) = pk;
      }
    }
    STAMPW(24, 4);
    LBAR();
    STAMP(25);
    if (w >= 4) {
      const int tw = w - 4;
      v4u bd[8];
#pragma unroll
      for (int i = 0; i < 8; ++i) bd[i] = lds128(L, L_BD + i * 1024 + l * 16);
#pragma unroll
      for (int s = 0; s < 2; ++s) {
        f32x4 den = mfma(bd[6], pqA[s], f32x4{0, 0, 0, 0}); den = mfma(bd[7], pqB[s], den);
        f32x4 num[3];
#pragma unroll
        for (int ft = 0; ft < 3; ++ft) { num[ft] = mfma(bd[2 * ft], pqA[s], f32x4{0, 0, 0, 0}); num[ft] = mfma(bd[2 * ft + 1], pqB[s], num[ft]); }
        const float r0 = __builtin_amdgcn_rcpf(den[0] + 1e-6f), r1 = __builtin_amdgcn_rcpf(den[1] + 1e-6f);
        const float rm = g == 0 ? r0 : r1;
        const unsigned o0 = pk2(num[0][0] * r0, num[0][1] * r0), o1 = pk2(num[0][2] * r0, num[0][3] * r0);
        const unsigned o2 = pk2(num[1][0] * rm, num[1][1] * rm), o3 = pk2(num[1][2] * rm, num[1][3] * rm);
        const unsigned o4 = pk2(num[2][0] * r1, num[2][1] * r1), o5 = pk2(num[2][2] * r1, num[2][3] * r1);
        const unsigned base = WS_O + (8 * r + 2 * tw + s) * 20 * 1024;
        stg_sc1(rs, base + p * 1024 + l * 16, v4u{o0, o1, o2, o3});
        if (g < 2) stg2_sc1(rs, base + (16 + (p >> 2)) * 1024 + ((2 * ((p & 3) >> 1) + g) * 16 + c) * 16 + (p & 1) * 8, v2u{o4, o5});
      }
    }
    STAMPW(26, 4);
    DRAIN();
    STAMPW(27, 4);
    LBAR();
    if (tid == 0) { set_flag(ws, WS_FO + r * 256 + p * 4, tag); set_flag(ws, WS_FC + p * 256 + r * 4, tag); set_flag(ws, WS_FD + cs * 256 + r2 * 4, tag); }
    STAMP(5);
  }

  const int th = w & 1, kq = w >> 1;
  const int sl = 2 * th + (kq & 1);
  const int tok = 16 * (4 * r2 + sl) + c;
  {
    v2u xr[3];
#pragma unroll
    for (int i = 0; i < 3; ++i) {
      const int T = kq < 2 ? (i == 2 ? 4 : i) : (i == 2 ? 2 : 2 + i);
      xr[i] = *(const v2u*)(x + (size_t)tok * 640 + 80 * cs + 16 * T + 4 * g);
    }
    v4u wf[5][5];
    const int wb = (5 * kq * 32 + 4 * g + q4) * 160 + 8 * pp;
#pragma unroll
    for (int k = 0; k < 5; ++k)
#pragma unroll
      for (int T = 0; T < 5; ++T) wf[k][T] = ldtr(L, wb + k * 32 * 160 + T * 32, 16 * 160);
    STAMP(28);
    if (w == 0) wait16(ws, WS_FO + r * 256, tag, l);
    if (w == 1) wait16(ws, WS_FC + p * 256, tag, l);
    if (w == 2) wait32(ws, WS_FD + cs * 256, tag, l);
    LBAR();
    STAMP(6);
    if (b == 0 && tid == 0) set_flag(ws, WS_EPOCH, tag);
    v4u xf[5][2];
#pragma unroll
    for (int k = 0; k < 5; ++k)
#pragma unroll
      for (int s = 0; s < 2; ++s) xf[k][s] = ldg_sc1(rs, WS_O + ((4 * r2 + 2 * th + s) * 20 + 5 * kq + k) * 1024 + l * 16);
    unsigned csv[20];
    const int ct = min(tid, 223), cm = ct >= 112;
    if (w < 4) {
#pragma unroll
      for (int i = 0; i < 20; ++i) csv[i] = ldg1_sc1(rs, WS_CS + (p * 40 + 2 * i + cm) * 448 + (ct - 112 * cm) * 4);
    } else {
#pragma unroll
      for (int i = 0; i < 20; ++i) csv[i] = 0;
    }
    f32x4 acc[5][2];
#pragma unroll
    for (int T = 0; T < 5; ++T) { acc[T][0] = f32x4{0, 0, 0, 0}; acc[T][1] = f32x4{0, 0, 0, 0}; }
    SB();
#pragma unroll
    for (int k = 0; k < 5; ++k)
#pragma unroll
      for (int T = 0; T < 5; ++T)
#pragma unroll
        for (int s = 0; s < 2; ++s) acc[T][s] = mfma(wf[k][T], xf[k][s], acc[T][s]);
    STAMP(7);
    f32x4 own[3];
    splitk_exchange(L, acc, own, th, kq, l);
    STAMP(29);
    float sm = 0.f, sq = 0.f;
    unsigned hp[3][2];
#pragma unroll
    for (int i = 0; i < 3; ++i) {
      hp[i][0] = pk2(bflo(xr[i].x) + own[i][0], bfhi(xr[i].x) + own[i][1]);
      hp[i][1] = pk2(bflo(xr[i].y) + own[i][2], bfhi(xr[i].y) + own[i][3]);
      if (i < 2 || kq < 2) {
        const float a0 = bflo(hp[i][0]), a1 = bfhi(hp[i][0]), a2 = bflo(hp[i][1]), a3 = bfhi(hp[i][1]);
        sm += (a0 + a1) + (a2 + a3);
        sq += (a0 * a0 + a1 * a1) + (a2 * a2 + a3 * a3);
      }
    }
    const unsigned hb = WS_H + (4 * r2 + sl) * 20 * 1024;
    stg_sc1(rs, hb + (2 * cs + (kq >> 1)) * 1024 + l * 16, v4u{hp[0][0], hp[0][1], hp[1][0], hp[1][1]});
    if (kq < 2) stg2_sc1(rs, hb + (16 + (cs >> 1)) * 1024 + l * 16 + (cs & 1) * 8, v2u{hp[2][0], hp[2][1]});
    sm += __shfl_xor(sm, 16); sq += __shfl_xor(sq, 16);
    sm += __shfl_xor(sm, 32); sq += __shfl_xor(sq, 32);
    if (g == 0) stg2_sc1(rs, WS_HS + ((4 * r2 + sl) * 16 + cs * 2 + (kq >> 1)) * 128 + c * 8, v2u{__float_as_uint(sm), __float_as_uint(sq)});
#pragma unroll
    for (int i = 0; i < 3; ++i)
      if (i < 2 || kq < 2) {
        const int T = kq < 2 ? (i == 2 ? 4 : i) : 2 + i;
        *(LAS v2u*)(L + L_HT + (sl * 5 + T) * 512 + l * 8) = v2u{hp[i][0], hp[i][1]};
      }
    if (tid < 224) {
      float s = 0.f;
#pragma unroll
      for (int i = 0; i < 20; ++i) s += __uint_as_float(csv[i]);
      *(LAS float*)(L + L_CS3 + tid * 4) = s;
    }
    STAMP(30);
    DRAIN();
    STAMP(31);
    LBAR();
    if (tid == 0) set_flag(ws, WS_FH + r * 256 + ((r2 & 1) * 8 + cs) * 4, tag);
    STAMP(8);
  }
  {
    if (w < 4) p3_body<4, 0, 3>(L, ws, rs, r, p, w, l, tag, 3 * w);
    else       p3_body<3, 4, 4>(L, ws, rs, r, p, w, l, tag, 12 + 4 * (w - 4));
    STAMP(36);
    DRAIN();
    STAMP(37);
    LBAR();
    if (tid == 0) set_flag(ws, WS_FA + r * 256 + p * 4, tag);
    STAMP(12);
  }
  {
    const int kb = kq < 2 ? 14 * kq : 28 + 13 * (kq - 2);
    const int nk = kq < 2 ? 14 : 13;
    const unsigned l16 = l * 16;
    const char* w4 = ws + WS_W4 + (size_t)cs * 270 * 1024 + (th ? 3072 : 0);
    const unsigned lw4 = kq * 5120 + (th ? 3072 : 0);
    auto issue = [&](auto STG) {
      constexpr int stg = STG;
      const char* base = w4 + (size_t)min(kb + stg, 53) * 5120;
      if (th == 0) gldsn_sc1<3, (stg % 7) * 20480>(l16, base, lw4); else gldsn_sc1<2, (stg % 7) * 20480>(l16, base, lw4);
    };
    static_for<0, 7>([&](auto ST) { issue(ST); });
    STAMP(38);
    if (w == 0) wait16(ws, WS_FA + r * 256, tag, l);
    STAMP(39);
    DRAIN();
    LBAR();
    STAMP(13);
    v4u xf[14][2];
    const unsigned xa = WS_ACT + (4 * r2 + 2 * th) * 54 * 1024 + l16;
    auto ldx = [&](auto K, auto S) { constexpr int k = K, s = S; xf[k][s] = ldg_sc1(rs, xa + (s * 54 + min(kb + k, 53)) * 1024); };
    constexpr int XW = 4;
    static_for<0, XW>([&](auto K) { ldx(K, IC<0>{}); ldx(K, IC<1>{}); });
    f32x4 acc[5][2];
#pragma unroll
    for (int T = 0; T < 5; ++T) { acc[T][0] = f32x4{0, 0, 0, 0}; acc[T][1] = f32x4{0, 0, 0, 0}; }
    int wbs[3];
    wbs[0] = kq * 5120 + l16; wbs[1] = wbs[0] + 3 * 20480; wbs[2] = wbs[0] + 6 * 20480;
    asm volatile("" : "+v"(wbs[0]), "+v"(wbs[1]), "+v"(wbs[2]));
    v4u wf[2][5];
    auto rd1 = [&](auto STG, auto I) {
      constexpr int stg = STG, T = I, sl7 = stg % 7;
      wf[stg & 1][T] = lds128(L, wbs[sl7 / 3] + (sl7 % 3) * 20480 + T * 1024);
    };
    static_for<0, 5>([&](auto I) { rd1(IC<0>{}, I); });
    static_for<0, 14>([&](auto ST) {
      constexpr int st = ST;
      if constexpr (st <= 12 && (st & 1) == 0) {
        if constexpr (st <= 4) LBAR();
        else if (th == 0) { if constexpr (st <= 8) waitbar<9>(); else if constexpr (st == 10) waitbar<3>(); else waitbar<0>(); }
        else { if constexpr (st <= 8) waitbar<6>(); else if constexpr (st == 10) waitbar<2>(); else waitbar<0>(); }
      }
      if constexpr (st == 1) STAMP(40);
      if constexpr (st == 4) STAMP(41);
      if constexpr (st == 7) STAMP(42);
      if constexpr (st == 10) STAMP(43);
      if constexpr (st == 13) STAMP(44);
      if (st < nk) {
        static_for<0, 10>([&](auto I) {
          constexpr int i = I;
          acc[i >> 1][i & 1] = mfma(wf[st & 1][i >> 1], xf[st][i & 1], acc[i >> 1][i & 1]);
          if constexpr (st + XW < 14 && (i == 0 || i == 2)) ldx(IC<st + XW>{}, IC<(i >> 1)>{});
          if constexpr (st == 0 && i == 4) issue(IC<7>{});
          if constexpr ((st == 2 || st == 4 || st == 6) && (i == 4 || i == 7)) issue(IC<st + 6 + (i == 7)>{});
          if constexpr (st < 13 && i >= 2 && i < 7) rd1(IC<st + 1>{}, IC<i - 2>{});
          SB();
        });
      }
    });
    LBAR();
    STAMP(14);
    f32x4 own[3];
    splitk_exchange(L, acc, own, th, kq, l);
#pragma unroll
    for (int i = 0; i < 3; ++i) {
      if (i < 2 || kq < 2) {
        const int T = kq < 2 ? (i == 2 ? 4 : i) : 2 + i;
        const v2u hh = *(const LAS v2u*)(L + L_HT + (sl * 5 + T) * 512 + l * 8);
        const v2u o = {pk2(bflo(hh.x) + own[i][0], bfhi(hh.x) + own[i][1]), pk2(bflo(hh.y) + own[i][2], bfhi(hh.y) + own[i][3])};
        *(v2u*)(out + (size_t)tok * 640 + 80 * cs + 16 * T + 4 * g) = o;
      }
    }
    STAMP(15);
  }
}

extern "C" void kernel_launch(void* const* d_in, const int* in_sizes, int n_in, void* d_out, int out_size, void* d_ws, size_t ws_size,
                              hipStream_t stream) {
  (void)in_sizes; (void)n_in; (void)out_size; (void)ws_size;
  (void)hipFuncSetAttribute((const void*)fused_block, hipFuncAttributeMaxDynamicSharedMemorySize, LDS_BYTES);
  fused_block<<<256, 512, LDS_BYTES, stream>>>((const u16*)d_in[0], (const u16*)d_in[1], (const u16*)d_in[2], (const u16*)d_in[4],
                                                (const u16*)d_in[5], (const u16*)d_in[6], (u16*)d_out, (char*)d_ws);
}
```

```cpp
#include <hip/hip_runtime.h>
#include <hip/hip_bf16.h>
#include <stdint.h>
#include <type_traits>

#define LAS __attribute__((address_space(3)))
#define GAS __attribute__((address_space(1)))
typedef __bf16 bf16x8 __attribute__((ext_vector_type(8)));
typedef __bf16 bf16x2 __attribute__((ext_vector_type(2)));
typedef short s16x4 __attribute__((ext_vector_type(4)));
typedef float f32x4 __attribute__((ext_vector_type(4)));
typedef float f32x2 __attribute__((ext_vector_type(2)));
typedef unsigned v4u __attribute__((ext_vector_type(4)));
typedef unsigned v2u __attribute__((ext_vector_type(2)));
typedef GAS unsigned gu32;
typedef unsigned short u16;
#define RLX_AGENT __ATOMIC_RELAXED, __HIP_MEMORY_SCOPE_AGENT

constexpr unsigned WS_EPOCH = 0, WS_TMO = 256;
constexpr unsigned WS_FKV = 4096, WS_FO = 8192, WS_FH = 12288, WS_FA = 16384, WS_FC = 20480, WS_FD = 24576;
constexpr unsigned WS_KVP = 32768, KVP_STRIDE = 3456;
constexpr unsigned WS_O = WS_KVP + 256 * KVP_STRIDE;
constexpr unsigned WS_H = WS_O + 2621440;
constexpr unsigned WS_HS = WS_H + 2621440;
constexpr unsigned WS_ACT = WS_HS + 262144;
constexpr unsigned WS_CS = WS_ACT + 7077888;
constexpr unsigned WS_W3 = WS_CS + 16 * 40 * 448;
constexpr unsigned WS_W4 = WS_W3 + 16 * 280 * 1024;
constexpr unsigned WS_STAMP = WS_W4 + 8 * 270 * 1024;
constexpr unsigned WS_END = WS_STAMP + 256 * 64 * 8;

constexpr int LDS_BYTES = 163840;
constexpr int L_KVF = 102400;
constexpr int L_KVC = 135168;
constexpr int L_BD = 138752;
constexpr int L_STAT = 146944;
constexpr int L_CSUM = 147968;
constexpr int L_SIDE = 148480;
constexpr int L_CS3 = 151168;
constexpr int L_SYNC = 152064;
constexpr int L_HT = 153600;
constexpr int L_RED = 0;
constexpr int L_X3 = 86016;

#define STAMPS 0

__device__ __forceinline__ void glds(unsigned voff, const void* sbase, unsigned ldsaddr) {
  asm volatile("s_mov_b32 m0, %2\n\ts_nop 0\n\tglobal_load_lds_dwordx4 %0, %1" ::"v"(voff), "s"(sbase), "s"(ldsaddr) : "memory");
}
__device__ __forceinline__ void glds2_sc1(unsigned voff, const void* sbase, unsigned ldsaddr) {
  asm volatile("s_mov_b32 m0, %2\n\ts_nop 0\n\tglobal_load_lds_dwordx4 %0, %1 sc1\n\tglobal_load_lds_dwordx4 %0, %1 offset:1024 sc1" ::"v"(voff), "s"(sbase), "s"(ldsaddr) : "memory");
}
template <int IMM> __device__ __forceinline__ void gldsi(unsigned voff, const void* sbase, unsigned lbase) {
  asm volatile("s_add_u32 m0, %2, %3\n\ts_nop 0\n\tglobal_load_lds_dwordx4 %0, %1" ::"v"(voff), "s"(sbase), "s"(lbase), "i"(IMM) : "memory", "scc");
}
template <int IMM> __device__ __forceinline__ void glds2i_sc1(unsigned voff, const void* sbase, unsigned lbase) {
  asm volatile("s_add_u32 m0, %2, %3\n\ts_nop 0\n\tglobal_load_lds_dwordx4 %0, %1 sc1\n\tglobal_load_lds_dwordx4 %0, %1 offset:1024 sc1" ::"v"(voff), "s"(sbase), "s"(lbase), "i"(IMM) : "memory", "scc");
}
template <int N, int IMM> __device__ __forceinline__ void gldsn_sc1(unsigned voff, const void* sbase, unsigned lbase) {
  static_assert(N >= 1 && N <= 4, "");
  if constexpr (N == 1) asm volatile("s_add_u32 m0, %2, %3\n\ts_nop 0\n\tglobal_load_lds_dwordx4 %0, %1 sc1" ::"v"(voff), "s"(sbase), "s"(lbase), "i"(IMM) : "memory", "scc");
  if constexpr (N == 2) asm volatile("s_add_u32 m0, %2, %3\n\ts_nop 0\n\tglobal_load_lds_dwordx4 %0, %1 sc1\n\tglobal_load_lds_dwordx4 %0, %1 offset:1024 sc1" ::"v"(voff), "s"(sbase), "s"(lbase), "i"(IMM) : "memory", "scc");
  if constexpr (N == 3) asm volatile("s_add_u32 m0, %2, %3\n\ts_nop 0\n\tglobal_load_lds_dwordx4 %0, %1 sc1\n\tglobal_load_lds_dwordx4 %0, %1 offset:1024 sc1\n\tglobal_load_lds_dwordx4 %0, %1 offset:2048 sc1" ::"v"(voff), "s"(sbase), "s"(lbase), "i"(IMM) : "memory", "scc");
  if constexpr (N == 4) asm volatile("s_add_u32 m0, %2, %3\n\ts_nop 0\n\tglobal_load_lds_dwordx4 %0, %1 sc1\n\tglobal_load_lds_dwordx4 %0, %1 offset:1024 sc1\n\tglobal_load_lds_dwordx4 %0, %1 offset:2048 sc1\n\tglobal_load_lds_dwordx4 %0, %1 offset:3072 sc1" ::"v"(voff), "s"(sbase), "s"(lbase), "i"(IMM) : "memory", "scc");
}
template <int IMM, int OFF> __device__ __forceinline__ void gldso_sc1(unsigned voff, const void* sbase, unsigned lbase) {
  asm volatile("s_add_u32 m0, %2, %3\n\ts_nop 0\n\tglobal_load_lds_dwordx4 %0, %1 offset:%4 sc1" ::"v"(voff), "s"(sbase), "s"(lbase), "i"(IMM), "i"(OFF) : "memory", "scc");
}
__device__ __forceinline__ void ldg_asm_sc1(v4u& d, unsigned voff, const void* sbase) {
  asm volatile("global_load_dwordx4 %0, %1, %2 sc1" : "=v"(d) : "v"(voff), "s"(sbase) : "memory");
}
template <int N> __device__ __forceinline__ void wait_for(v4u& a, v4u& b) { asm volatile("s_waitcnt vmcnt(%2)" : "+v"(a), "+v"(b) : "i"(N) : "memory"); }
template <int N> __device__ __forceinline__ void waitvm() { asm volatile("s_waitcnt vmcnt(%0)" ::"i"(N) : "memory"); }
__device__ __forceinline__ f32x2 pfma(f32x2 a, f32x2 b, f32x2 c) { return __builtin_elementwise_fma(a, b, c); }
__device__ __forceinline__ f32x2 sp2(float a) { return f32x2{a, a}; }
__device__ __forceinline__ f32x2 lo2(f32x4 v) { return __builtin_shufflevector(v, v, 0, 1); }
__device__ __forceinline__ f32x2 hi2(f32x4 v) { return __builtin_shufflevector(v, v, 2, 3); }
__device__ __forceinline__ unsigned pk2v(f32x2 v) { return __builtin_bit_cast(unsigned, __builtin_convertvector(v, bf16x2)); }
__device__ __forceinline__ f32x2 phi2(f32x2 te) {
  const f32x2 y = pfma(te, sp2(1.44269504088896f), sp2(-1.44269504088896e-6f));
  const f32x2 e = {__builtin_amdgcn_fmed3f(__builtin_amdgcn_exp2f(y.x), 0.f, 1.f), __builtin_amdgcn_fmed3f(__builtin_amdgcn_exp2f(y.y), 0.f, 1.f)};
  const f32x2 m = {fmaxf(te.x, 1e-6f), fmaxf(te.y, 1e-6f)};
  return m + e;
}
__device__ __forceinline__ unsigned geglu2(f32x2 ag, f32x2 au, f32x2 cg, f32x2 cu, float rstd, float mr) {
  const f32x2 gv = pfma(ag, sp2(rstd), cg * sp2(mr)), uv = pfma(au, sp2(rstd), cu * sp2(mr));
  const f32x2 z = gv * pfma(gv * gv, sp2(-0.1029432397f), sp2(-2.302208198f));
  const f32x2 d = {1.0f + __builtin_amdgcn_exp2f(z.x), 1.0f + __builtin_amdgcn_exp2f(z.y)};
  const f32x2 rc = {__builtin_amdgcn_rcpf(d.x), __builtin_amdgcn_rcpf(d.y)};
  return pk2v(gv * uv * rc);
}
template <int V> using IC = std::integral_constant<int, V>;
template <int I, int N, class F> __device__ __forceinline__ void static_for(F&& f) {
  if constexpr (I < N) { f(std::integral_constant<int, I>{}); static_for<I + 1, N>(f); }
}
#define LBAR() asm volatile("s_waitcnt lgkmcnt(0)\n\ts_barrier" ::: "memory")
#define DRAIN() asm volatile("s_waitcnt vmcnt(0)" ::: "memory")
#define SB() __builtin_amdgcn_sched_barrier(0)
template <int N> __device__ __forceinline__ void waitbar() { asm volatile("s_waitcnt vmcnt(%0) lgkmcnt(0)\n\ts_barrier" ::"i"(N) : "memory"); }

__device__ __forceinline__ unsigned pk2(float a, float b) {
  bf16x2 v = {(__bf16)a, (__bf16)b};
  return __builtin_bit_cast(unsigned, v);
}
__device__ __forceinline__ float bflo(unsigned u) { return __uint_as_float(u << 16); }
__device__ __forceinline__ float bfhi(unsigned u) { return __uint_as_float(u & 0xffff0000u); }
__device__ __forceinline__ f32x4 mfma(v4u a, v4u b, f32x4 c) {
  return __builtin_amdgcn_mfma_f32_16x16x32_bf16(__builtin_bit_cast(bf16x8, a), __builtin_bit_cast(bf16x8, b), c, 0, 0, 0);
}
__device__ __forceinline__ void mfma_acc(f32x4& acc, v4u a, v4u b) {
  asm volatile("v_mfma_f32_16x16x32_bf16 %0, %1, %2, %0" : "+v"(acc) : "v"(a), "v"(b));
}
__device__ __forceinline__ void mfma_first(f32x4& acc, v4u a, v4u b) {
  asm volatile("v_mfma_f32_16x16x32_bf16 %0, %1, %2, 0" : "=&v"(acc) : "v"(a), "v"(b));
}
template <bool FIRST> __device__ __forceinline__ void mfma_a(f32x4& acc, v4u a, v4u b) { if constexpr (FIRST) mfma_first(acc, a, b); else mfma_acc(acc, a, b); }
#define MFMA_FENCE() asm volatile("s_nop 7\n\ts_nop 1" ::: "memory")
__device__ __forceinline__ v4u lds128(LAS char* L, int off) { return *(const LAS v4u*)(L + off); }
__device__ __forceinline__ v4u ldtr(LAS char* L, int off, int d) {
  s16x4 a = __builtin_amdgcn_ds_read_tr16_b64_v4i16((LAS s16x4*)(L + off));
  s16x4 b = __builtin_amdgcn_ds_read_tr16_b64_v4i16((LAS s16x4*)(L + off + d));
  v2u x = __builtin_bit_cast(v2u, a), y = __builtin_bit_cast(v2u, b);
  return v4u{x.x, x.y, y.x, y.y};
}
__device__ __forceinline__ float phi_eps(float te) {
  const float e = __builtin_amdgcn_fmed3f(__builtin_amdgcn_exp2f((te - 1e-6f) * 1.44269504088896f), 0.f, 1.f);
  return fmaxf(te, 1e-6f) + e;
}
__device__ __forceinline__ v4u ldg_sc1(__amdgpu_buffer_rsrc_t rs, unsigned off) { return __builtin_amdgcn_raw_buffer_load_b128(rs, off, 0, 16); }
__device__ __forceinline__ v2u ldg2_sc1(__amdgpu_buffer_rsrc_t rs, unsigned off) { return __builtin_amdgcn_raw_buffer_load_b64(rs, off, 0, 16); }
__device__ __forceinline__ unsigned ldg1_sc1(__amdgpu_buffer_rsrc_t rs, unsigned off) { return __builtin_amdgcn_raw_buffer_load_b32(rs, off, 0, 16); }
__device__ __forceinline__ void stg_sc1(__amdgpu_buffer_rsrc_t rs, unsigned off, v4u v) { __builtin_amdgcn_raw_buffer_store_b128(v, rs, off, 0, 16); }
__device__ __forceinline__ void stg2_sc1(__amdgpu_buffer_rsrc_t rs, unsigned off, v2u v) { __builtin_amdgcn_raw_buffer_store_b64(v, rs, off, 0, 16); }
__device__ __forceinline__ void stg1_sc1(__amdgpu_buffer_rsrc_t rs, unsigned off, unsigned v) { __builtin_amdgcn_raw_buffer_store_b32(v, rs, off, 0, 16); }

__device__ __forceinline__ void wait16(char* ws, unsigned off, unsigned tag, int lane) {
  gu32* f = (gu32*)(ws + off) + (lane & 15);
  for (unsigned sp = 0; sp < (1u << 15); ++sp) {
    unsigned v = __hip_atomic_load(f, RLX_AGENT);
    if (__all(v == tag)) return;
    __builtin_amdgcn_s_sleep(1);
  }
  if (lane == 0) __hip_atomic_store((gu32*)(ws + WS_TMO), 1u, RLX_AGENT);
}
__device__ __forceinline__ void wait32(char* ws, unsigned off, unsigned tag, int lane) {
  gu32* f = (gu32*)(ws + off) + (lane & 31);
  for (unsigned sp = 0; sp < (1u << 15); ++sp) {
    unsigned v = __hip_atomic_load(f, RLX_AGENT);
    if (__all(v == tag)) return;
    __builtin_amdgcn_s_sleep(1);
  }
  if (lane == 0) __hip_atomic_store((gu32*)(ws + WS_TMO), 1u, RLX_AGENT);
}
typedef LAS unsigned lu32;
__device__ __forceinline__ unsigned lds_add(LAS char* L, int word) { return __hip_atomic_fetch_add((lu32*)(L + L_SYNC) + word, 1u, __ATOMIC_RELAXED, __HIP_MEMORY_SCOPE_WORKGROUP); }
__device__ __forceinline__ void lds_spin(LAS char* L, int word, unsigned want) {
  for (unsigned sp = 0; sp < (1u << 20); ++sp) {
    if (__builtin_amdgcn_readfirstlane(__hip_atomic_load((lu32*)(L + L_SYNC) + word, __ATOMIC_RELAXED, __HIP_MEMORY_SCOPE_WORKGROUP)) >= want) break;
  }
  asm volatile("" ::: "memory");
}
__device__ __forceinline__ void touch(unsigned voff, const void* sbase) {
  asm volatile("s_mov_b32 m0, %2\n\ts_nop 0\n\tglobal_load_lds_dword %0, %1" ::"v"(voff), "s"(sbase), "s"(L_SYNC + 64) : "memory");
}
__device__ __forceinline__ void set_flag(char* ws, unsigned off, unsigned tag) { __hip_atomic_store((gu32*)(ws + off), tag, RLX_AGENT); }
#define STAMPW(i, wv) do {} while (0)
#define STAMP(i) STAMPW(i, 0)

__device__ __forceinline__ void splitk_exchange(LAS char* L, f32x4 (&acc)[5][2], f32x4 (&own)[3], int th, int kq, int l) {
  const int wb = L_RED + (th * 40 + kq) * 1024 + l * 16;
#pragma unroll
  for (int s = 0; s < 2; ++s)
#pragma unroll
    for (int T = 0; T < 5; ++T) *(LAS f32x4*)(L + wb + (s * 5 + T) * 4096) = acc[T][s];
  LBAR();
  const int rb = L_RED + (th * 2 + (kq & 1)) * 5 * 4096 + l * 16, t0 = rb + (kq >> 1) * 8192;
  f32x4 v[3][4];
#pragma unroll
  for (int k = 0; k < 4; ++k) {
    v[0][k] = *(const LAS f32x4*)(L + t0 + k * 1024);
    v[1][k] = *(const LAS f32x4*)(L + t0 + 4096 + k * 1024);
    v[2][k] = *(const LAS f32x4*)(L + rb + 4 * 4096 + k * 1024);
  }
#pragma unroll
  for (int i = 0; i < 3; ++i) own[i] = (v[i][0] + v[i][1]) + (v[i][2] + v[i][3]);
}

template <int NT> struct Frag3 { v4u x[2], g[NT], u[NT]; };

template <int NT, int TILE0, bool LOADER>
__device__ __forceinline__ void p3_body(LAS char* L, char* ws, __amdgpu_buffer_rsrc_t rs, int r, int p, int cs, int w, int l, unsigned tag) {
  const int c = l & 15, g = l >> 4, tg = w & 3;
  const unsigned l16 = l * 16;
  constexpr int NWP = LOADER ? 3 : 4, NP = LOADER ? 7 : 4;
  const int wp0 = LOADER ? 16 + 3 * tg : 4 * tg;
  const char* wsrc = ws + WS_W3 + (size_t)p * 280 * 1024 + wp0 * 1024;
  const char* hx = ws + WS_H + (size_t)(8 * r + 2 * tg) * 20 * 1024;
  const unsigned lw = wp0 * 1024, lx = L_X3 + tg * 4096;
  auto piece = [&](auto STG, auto Q) {
    constexpr int stg = STG, q = Q, slot = stg % 3;
    if constexpr (q < NWP) gldso_sc1<slot * 28672, q * 1024>(l16, wsrc + stg * 28672, lw);
    else gldso_sc1<slot * 16384 + ((q - NWP) >> 1) * 2048, ((q - NWP) & 1) * 1024>(l16, hx + stg * 2048 + ((q - NWP) >> 1) * 20480, lx);
  };
  static_for<0, 3>([&](auto ST) { static_for<0, NWP>([&](auto Q) { piece(ST, Q); }); });
  STAMP(32);
  if (w == 0) { wait16(ws, WS_FH + r * 256, tag, l); STAMP(33); }
  DRAIN();
  LBAR();
  STAMP(10);
  if constexpr (LOADER) static_for<0, 3>([&](auto ST) { static_for<NWP, NP>([&](auto Q) { piece(ST, Q); }); });
  v2u stp[2][4];
#pragma unroll
  for (int s = 0; s < 2; ++s)
#pragma unroll
    for (int i = 0; i < 4; ++i) stp[s][i] = ldg2_sc1(rs, WS_HS + ((8 * r + 2 * tg + s) * 16 + 4 * g + i) * 128 + c * 8);

  f32x4 ag[NT][2], au[NT][2];
  const int xo = L_X3 + tg * 4096 + l16;
  int wb0 = TILE0 * 1024 + l16, wb2 = wb0 + 2 * 28672;
  asm volatile("" : "+v"(wb0), "+v"(wb2));
  auto rd1 = [&](Frag3<NT>& f, auto SLOT, auto KS, auto I) {
    constexpr int slot = SLOT, ks = KS, i = I;
    if constexpr (i < 2) f.x[i] = lds128(L, xo + slot * 16384 + ks * 1024 + i * 2048);
    else {
      constexpr int j = i - 2, t = j >> 1, u = j & 1, off = ks * 14336 + u * 7168 + t * 1024;
      const v4u v = slot < 2 ? lds128(L, wb0 + slot * 28672 + off) : lds128(L, wb2 + off);
      if constexpr (u == 0) f.g[t] = v; else f.u[t] = v;
    }
  };
  auto mma1 = [&](const Frag3<NT>& f, auto I, auto FIRST) {
    constexpr int i = I, t = i >> 2, u = (i >> 1) & 1, s = i & 1;
    if constexpr (u == 0) mfma_a<FIRST>(ag[t][s], f.g[t], f.x[s]); else mfma_a<FIRST>(au[t][s], f.u[t], f.x[s]);
  };
  constexpr int NM = 4 * NT, NR = 2 + 2 * NT;
  Frag3<NT> A, B;
  if constexpr (LOADER) waitbar<8 + 8>(); else LBAR();
  STAMP(35);
  static_for<0, NR>([&](auto I) { rd1(A, IC<0>{}, IC<0>{}, I); });
  static_for<0, 10>([&](auto ST) {
    constexpr int st = ST, slot = st % 3, nslot = (st + 1) % 3;
    static_for<0, NM>([&](auto I) {
      constexpr int i = I;
      mma1(A, I, IC<(st == 0)>{});
      if constexpr (i < NR) rd1(B, IC<slot>{}, IC<1>{}, I);
      SB();
    });
    if constexpr (st < 9) {
      if constexpr (st == 0) { if constexpr (LOADER) waitbar<4 + 8>(); else LBAR(); }
      else if constexpr (st == 1) { if constexpr (LOADER) waitbar<NP>(); else LBAR(); }
      else if constexpr (st < 8) waitbar<NP>(); else waitbar<0>();
    }
    if constexpr (st == 2) STAMP(45);
    if constexpr (st == 5) STAMP(46);
    if constexpr (st == 8) STAMP(47);
    static_for<0, NM>([&](auto I) {
      constexpr int i = I;
      mma1(B, I, IC<0>{});
      if constexpr (LOADER) { if constexpr (st <= 6 && i < NP) piece(IC<st + 3>{}, I); }
      else { if constexpr (st <= 6 && (i % 3) == 0 && i / 3 < NP) piece(IC<st + 3>{}, IC<i / 3>{}); }
      if constexpr (st < 9 && i < NR) rd1(A, IC<nslot>{}, IC<0>{}, I);
      SB();
    });
  });
  MFMA_FENCE();
  STAMP(11);
  f32x4 cgv[NT], cuv[NT];
#pragma unroll
  for (int t = 0; t < NT; ++t) {
    cgv[t] = *(const LAS f32x4*)(L + L_CS3 + ((TILE0 + t) * 16 + 4 * g) * 4);
    cuv[t] = *(const LAS f32x4*)(L + L_CS3 + (112 + (TILE0 + t) * 16 + 4 * g) * 4);
  }
#pragma unroll
  for (int s = 0; s < 2; ++s) {
    float sm = 0.f, sq = 0.f;
#pragma unroll
    for (int i = 0; i < 4; ++i) { sm += __uint_as_float(stp[s][i].x); sq += __uint_as_float(stp[s][i].y); }
    sm += __shfl_xor(sm, 16); sq += __shfl_xor(sq, 16);
    sm += __shfl_xor(sm, 32); sq += __shfl_xor(sq, 32);
    const float mean = sm * (1.0f / 640.0f);
    const float rstd = __builtin_amdgcn_rsqf(fmaxf(sq * (1.0f / 640.0f) - mean * mean, 0.f) + 1e-5f);
    const float mr = -mean * rstd;
    unsigned y[NT][2];
    const unsigned ab = WS_ACT + (8 * r + 2 * tg + s) * 54 * 1024 + l16;
#pragma unroll
    for (int t = 0; t < NT; ++t) {
      y[t][0] = geglu2(lo2(ag[t][s]), lo2(au[t][s]), lo2(cgv[t]), lo2(cuv[t]), rstd, mr);
      y[t][1] = geglu2(hi2(ag[t][s]), hi2(au[t][s]), hi2(cgv[t]), hi2(cuv[t]), rstd, mr);
      if constexpr (true) {
        if (t & 1) {
          if (TILE0 == 0) { if (p < 15) stg_sc1(rs, ab + (3 * p + (t >> 1)) * 1024, v4u{y[t - 1][0], y[t - 1][1], y[t][0], y[t][1]}); }
          else stg_sc1(rs, ab + (p < 15 ? 3 * p + 2 : 45) * 1024, v4u{y[t - 1][0], y[t - 1][1], y[t][0], y[t][1]});
        }
      }
    }
    if (TILE0 != 0) stg2_sc1(rs, ab + (46 + (p >> 1)) * 1024 + (p & 1) * 8, v2u{y[NT - 1][0], y[NT - 1][1]});
  }
}

constexpr int P4_XW = 4;
constexpr int p4_g(int it, int n) { return it == 0 ? n : (it == 2 || it == 4 || it == 6) ? 2 * n : 0; }
constexpr int p4_x(int it) { return it + P4_XW < 14 ? 2 : 0; }
constexpr int p4_xwait(int st, int n) {
  int y = 0;
  if (st < P4_XW) { y = 2 * (P4_XW - 1 - st); for (int j = 0; j < st; ++j) y += p4_x(j) + p4_g(j, n); }
  else { y = p4_g(st - P4_XW, n); for (int j = st - P4_XW + 1; j < st; ++j) y += p4_x(j) + p4_g(j, n); }
  return y;
}
template <int TH>
__device__ __forceinline__ void p4_main(LAS char* L, char* ws, int r, int r2, int cs, int kq, int w, int l, unsigned tag, f32x4 (&acc)[5][2]) {
  constexpr int N = TH == 0 ? 3 : 2;
  const int kb = kq < 2 ? 14 * kq : 28 + 13 * (kq - 2);
  const int nk = kq < 2 ? 14 : 13;
  const unsigned l16 = l * 16;
  const char* w4 = ws + WS_W4 + (size_t)cs * 270 * 1024 + TH * 3072;
  const unsigned lw4 = kq * 5120 + TH * 3072;
  auto issue = [&](auto STG) { constexpr int stg = STG; gldsn_sc1<N, (stg % 7) * 20480>(l16, w4 + (size_t)min(kb + stg, 53) * 5120, lw4); };
  static_for<0, 7>([&](auto ST) { issue(ST); });
  STAMP(38);
  if (w == 0) wait16(ws, WS_FA + r * 256, tag, l);
  STAMP(39);
  DRAIN();
  LBAR();
  STAMP(13);
  v4u xf[14][2];
  const char* xa = ws + WS_ACT + (size_t)(4 * r2 + 2 * TH) * 54 * 1024;
  auto ldx = [&](auto K, auto S) {
    constexpr int k = K, s = S;
    ldg_asm_sc1(xf[k][s], l16, xa + (size_t)(s * 54 + min(kb + k, 53)) * 1024);
  };
  static_for<0, P4_XW>([&](auto K) { ldx(K, IC<0>{}); ldx(K, IC<1>{}); });
  int wbs[3];
  wbs[0] = kq * 5120 + l16; wbs[1] = wbs[0] + 3 * 20480; wbs[2] = wbs[0] + 6 * 20480;
  asm volatile("" : "+v"(wbs[0]), "+v"(wbs[1]), "+v"(wbs[2]));
  v4u wf[2][5];
  auto rd1 = [&](auto STG, auto I) {
    constexpr int stg = STG, T = I, sl7 = stg % 7;
    wf[stg & 1][T] = lds128(L, wbs[sl7 / 3] + (sl7 % 3) * 20480 + T * 1024);
  };
  static_for<0, 5>([&](auto I) { rd1(IC<0>{}, I); });
  static_for<0, 14>([&](auto ST) {
    constexpr int st = ST;
    if constexpr (st <= 12 && (st & 1) == 0) {
      if constexpr (st <= 4) LBAR();
      else if constexpr (st <= 8) waitbar<3 * N + 6>(); else if constexpr (st == 10) waitbar<N + 6>(); else waitbar<6>();
    }
    if constexpr (st == 1) STAMP(40);
    if constexpr (st == 4) STAMP(41);
    if constexpr (st == 7) STAMP(42);
    if constexpr (st == 10) STAMP(43);
    if constexpr (st == 13) STAMP(44);
    wait_for<p4_xwait(st, N)>(xf[st][0], xf[st][1]);
    SB();
    if (st < 13 || st < nk) {
      static_for<0, 10>([&](auto I) {
        constexpr int i = I;
        mfma_a<(st == 0)>(acc[i >> 1][i & 1], wf[st & 1][i >> 1], xf[st][i & 1]);
        if constexpr (st + P4_XW < 14 && (i == 0 || i == 2)) ldx(IC<st + P4_XW>{}, IC<(i >> 1)>{});
        if constexpr (st == 0 && i == 4) issue(IC<7>{});
        if constexpr ((st == 2 || st == 4 || st == 6) && (i == 4 || i == 7)) issue(IC<st + 6 + (i == 7)>{});
        if constexpr (st < 13 && i >= 2 && i < 7) rd1(IC<st + 1>{}, IC<i - 2>{});
        SB();
      });
    }
  });
  MFMA_FENCE();
}

struct FragKV { v4u x[2], w[5]; };
struct FragQ { v4u x[2], w[3], e0, e1; };

__global__ __launch_bounds__(512, 2) void fused_block(const u16* __restrict__ x, const u16* __restrict__ wqkv, const u16* __restrict__ wo,
                                                      const u16* __restrict__ wg, const u16* __restrict__ wu, const u16* __restrict__ wd,
                                                      u16* __restrict__ out, char* __restrict__ ws) {
  extern __shared__ __attribute__((aligned(16))) char smem_[];
  LAS char* L = (LAS char*)smem_;
  const int tid = threadIdx.x, l = tid & 63, c = l & 15, g = l >> 4;
  const int w = __builtin_amdgcn_readfirstlane(tid >> 6);
  const int b = blockIdx.x, xcd = b & 7, jj = b >> 3, rg = xcd >> 1, cgp = xcd & 1;
  const int r = 4 * rg + (jj >> 3), p = 8 * cgp + (jj & 7);
  const int r2 = 4 * xcd + (jj >> 3), cs = jj & 7;
  const int r3 = 8 * (xcd >> 2) + (jj >> 2), c3 = 4 * (xcd & 3) + (jj & 3);
  const __amdgpu_buffer_rsrc_t rs = __builtin_amdgcn_make_buffer_rsrc(ws, 0, WS_END, 0x00020000);
  const v4u ONES = {0x3F803F80u, 0x3F803F80u, 0x3F803F80u, 0x3F803F80u};
  const int q4 = (l & 15) >> 2, pp = l & 3;
  STAMP(0);

  unsigned tag;
  f32x4 qacc[3][2];

  {
    unsigned vx[2], vw[2];
    bool aw[2];
#pragma unroll
    for (int i = 0; i < 2; ++i) {
      const int tok = 8 * (2 * w + i) + (l >> 3);
      vx[i] = tok * 1280 + (((l & 7) ^ (tok & 7)) << 4);
      const int a = 2 * w + i;
      int pos, col;
      if (a < 10) {
        const int n = 64 * a + l; pos = n / 10; const int ch = n - pos * 10;
        col = (ch < 5) ? 640 + 40 * p + 8 * ch : 1280 + 40 * p + 8 * (ch - 5); aw[i] = true;
      } else {
        const int n = 64 * (a - 10) + l; pos = n / 6; const int ch = n - pos * 6;
        col = 40 * p + 8 * ch; aw[i] = ch < 5;
      }
      const int k = (pos & ~12) | ((pos & 4) << 1) | ((pos & 8) >> 1);
      vw[i] = k * 3840 + col * 2;
    }
    const char* xb = (const char*)x + (size_t)r * 128 * 1280;
    const unsigned lxw = 2 * w * 1024;
    auto issue1 = [&](auto ST, auto I) {
      constexpr int st = ST, i = I;
      if constexpr (i < 2) gldsi<(st & 3) * 32768 + i * 1024>(vx[i], xb + st * 128, lxw);
      else { if (aw[i - 2]) gldsi<(st & 3) * 32768 + 16384 + (i - 2) * 1024>(vw[i - 2], (const char*)wqkv + (size_t)st * 64 * 3840, lxw); }
    };
    static_for<0, 4>([&](auto ST) { static_for<0, 4>([&](auto I) { issue1(ST, I); }); });
    unsigned tvo; const char* tvb;
    {
      const int li = b * 64 + l;
      const int k3 = li >> 10;
      tvb = w == 0 ? (const char*)wo : w == 1 ? (const char*)wg : w == 2 ? (const char*)wu : w == 3 ? (const char*)wd
          : w == 4 ? (const char*)((b >> 4) % 3 == 0 ? wg : (b >> 4) % 3 == 1 ? wu : wd) : (const char*)ws;
      (void)k3;
      tvo = w == 0 ? min(li * 128, 819200 - 4) : w < 4 ? min(li * 128, 2211840 - 4)
          : w == 4 ? min((16384 + (li & 1023)) * 128, 2211840 - 4)
          : w == 5 ? (l < 32 ? WS_O + ((8 * r + (l >> 2)) * 20 + 16 + (p >> 2)) * 1024 + ((p & 3) >> 1) * 512 + (l & 3) * 128
                             : WS_H + ((4 * r2 + ((l - 32) >> 3)) * 20 + 16 + (cs >> 1)) * 1024 + (l & 7) * 128)
          : w == 6 ? WS_ACT + ((8 * r3 + (l >> 3)) * 54 + 46 + (c3 >> 1)) * 1024 + (l & 7) * 128
                   : WS_FKV + (l >> 4) * 4096 + (l & 15) * 256;
    }
    if (tid < 256) {
      const int sl = tid >> 6, row = tid & 63;
      *(LAS v4u*)(L + sl * 32768 + 26624 + row * 96 + 80) = v4u{0x00003F80u, 0u, 0u, 0u};
    }
    *(LAS v4u*)(L + L_BD + tid * 16) = v4u{0, 0, 0, 0};
    if (tid == 0) *(LAS v4u*)(L + L_SYNC) = v4u{0, 0, 0, 0};
    { unsigned e; asm volatile("s_load_dword %0, %1, 0x0\n\ts_waitcnt lgkmcnt(0)" : "=s"(e) : "s"(ws + WS_EPOCH) : "memory"); tag = e + 1u; }
    STAMP(1);

    const int xo0 = c * 128 + ((g ^ (c & 7)) << 4);
    const int wrow = 16 * (g >> 1) + 4 * (g & 1) + q4;
    const int kvo = 16384 + wrow * 160 + 8 * pp;
    const int qo = 16384 + 10240 + wrow * 96 + 8 * pp;

    if (w < 4) {
      f32x4 acc[2][5];
      const int xw = w * 32 * 128;
      int xbs[2][2], kbs[4];
      xbs[0][0] = xw + xo0; xbs[0][1] = xw + (xo0 ^ 64); xbs[1][0] = xbs[0][0] + 65536; xbs[1][1] = xbs[0][1] + 65536;
      kbs[0] = kvo; kbs[1] = kvo + 32768; kbs[2] = kvo + 65536; kbs[3] = kvo + 98304;
      asm volatile("" : "+v"(xbs[0][0]), "+v"(xbs[0][1]), "+v"(xbs[1][0]), "+v"(xbs[1][1]), "+v"(kbs[0]), "+v"(kbs[1]), "+v"(kbs[2]), "+v"(kbs[3]));
      auto rd1 = [&](FragKV& f, auto SLOT, auto KS, auto I) {
        constexpr int slot = SLOT, ks = KS, i = I;
        if constexpr (i < 2) f.x[i] = lds128(L, xbs[slot >> 1][ks] + (slot & 1) * 32768 + i * 2048);
        else {
          constexpr int j = i - 2, t = j >> 1, h = j & 1;
          const v2u v = __builtin_bit_cast(v2u, __builtin_amdgcn_ds_read_tr16_b64_v4i16((LAS s16x4*)(L + kbs[slot] + ks * 32 * 160 + t * 32 + h * 8 * 160)));
          if constexpr (h == 0) { f.w[t].x = v.x; f.w[t].y = v.y; } else { f.w[t].z = v.x; f.w[t].w = v.y; }
        }
      };
      auto mma1 = [&](const FragKV& f, auto I, auto FIRST) { constexpr int i = I, t = i >> 1, s = i & 1; mfma_a<FIRST>(acc[s][t], f.x[s], f.w[t]); };
      FragKV A, B;
      waitbar<12>();
      static_for<0, 12>([&](auto I) { rd1(A, IC<0>{}, IC<0>{}, I); });
      static_for<0, 10>([&](auto ST) {
        constexpr int st = ST;
        static_for<0, 10>([&](auto I) {
          constexpr int i = I;
          mma1(A, I, IC<(st == 0)>{});
          if constexpr (i < 6) { rd1(B, IC<(st & 3)>{}, IC<1>{}, IC<2 * i>{}); rd1(B, IC<(st & 3)>{}, IC<1>{}, IC<2 * i + 1>{}); }
          SB();
        });
        if constexpr (st < 9) { if constexpr (st < 6) waitbar<8>(); else if constexpr (st == 6) waitbar<9>(); else if constexpr (st == 7) waitbar<5>(); else waitbar<1>(); }
        static_for<0, 10>([&](auto I) {
          constexpr int i = I;
          mma1(B, I, IC<0>{});
          if constexpr (st + 4 < 10 && i < 4) issue1(IC<st + 4>{}, I);
          if constexpr (st == 5 && i == 4) touch(tvo, tvb);
          if constexpr (st < 9 && i >= 2 && i < 8) { rd1(A, IC<((st + 1) & 3)>{}, IC<0>{}, IC<2 * (i - 2)>{}); rd1(A, IC<((st + 1) & 3)>{}, IC<0>{}, IC<2 * (i - 2) + 1>{}); }
          SB();
        });
      });
      MFMA_FENCE();
      STAMP(2);
      __builtin_amdgcn_s_setprio(2);
      LBAR();
      STAMP(16);
      f32x4 rstd[2], mr[2];
#pragma unroll
      for (int s = 0; s < 2; ++s) {
        const f32x4 sm = *(const LAS f32x4*)(L + L_STAT + (32 * w + 16 * s + 4 * g) * 4) * (1.0f / 640.0f);
        const f32x4 sq = *(const LAS f32x4*)(L + L_STAT + 512 + (32 * w + 16 * s + 4 * g) * 4);
        const f32x2 v0 = pfma(lo2(sq), sp2(1.0f / 640.0f), pfma(-lo2(sm), lo2(sm), sp2(1e-5f)));
        const f32x2 v1 = pfma(hi2(sq), sp2(1.0f / 640.0f), pfma(-hi2(sm), hi2(sm), sp2(1e-5f)));
        rstd[s] = f32x4{__builtin_amdgcn_rsqf(fmaxf(v0.x, 1e-5f)), __builtin_amdgcn_rsqf(fmaxf(v0.y, 1e-5f)), __builtin_amdgcn_rsqf(fmaxf(v1.x, 1e-5f)), __builtin_amdgcn_rsqf(fmaxf(v1.y, 1e-5f))};
        mr[s] = -sm * rstd[s];
      }
      float cw[5];
#pragma unroll
      for (int t = 0; t < 5; ++t) cw[t] = *(const LAS float*)(L + L_CSUM + (16 * t + c) * 4);
#pragma unroll
      for (int t = 0; t < 5; ++t) {
        unsigned o[4];
#pragma unroll
        for (int s = 0; s < 2; ++s)
#pragma unroll
          for (int h = 0; h < 2; ++h) {
            const f32x2 a2 = h ? hi2(acc[s][t]) : lo2(acc[s][t]), r2_ = h ? hi2(rstd[s]) : lo2(rstd[s]), m2 = h ? hi2(mr[s]) : lo2(mr[s]);
            f32x2 y;
            if (t < 2) y = phi2(pfma(a2, r2_, pfma(m2, sp2(cw[t]), sp2(1e-6f))));
            else if (t == 2) {
              const f32x2 te = pfma(a2, r2_, pfma(m2, sp2(cw[t]), sp2(1e-6f))), ph = phi2(te), v = te - sp2(1e-6f);
              y = c < 8 ? ph : v;
            } else y = pfma(a2, r2_, m2 * sp2(cw[t]));
            o[2 * s + h] = pk2v(y);
          }
        *(LAS v4u*)(L + L_KVF + (w * 5 + t) * 1024 + l * 16) = v4u{o[0], o[1], o[2], o[3]};
      }
    } else {
      const int tw = w - 4;
      f32x4 sq[2], cs0, cs1;
      const int xw = tw * 32 * 128;
      const int c1o = kvo + (tw < 3 ? tw : 4) * 32;
      const int c0o = (tw < 3) ? qo + tw * 32 : kvo + 3 * 32;
      const int c0d = (tw < 3) ? 8 * 96 : 8 * 160, c0k = (tw < 3) ? 32 * 96 : 32 * 160;
      int xbs[2][2], qbs[4], e0b[4][2], e1b[4];
      xbs[0][0] = xw + xo0; xbs[0][1] = xw + (xo0 ^ 64); xbs[1][0] = xbs[0][0] + 65536; xbs[1][1] = xbs[0][1] + 65536;
#pragma unroll
      for (int q = 0; q < 4; ++q) { qbs[q] = qo + q * 32768; e1b[q] = c1o + q * 32768; e0b[q][0] = c0o + q * 32768; e0b[q][1] = c0o + c0k + q * 32768; }
      asm volatile("" : "+v"(xbs[0][0]), "+v"(xbs[0][1]), "+v"(xbs[1][0]), "+v"(xbs[1][1]), "+v"(qbs[0]), "+v"(qbs[1]), "+v"(qbs[2]), "+v"(qbs[3]));
      asm volatile("" : "+v"(e1b[0]), "+v"(e1b[1]), "+v"(e1b[2]), "+v"(e1b[3]), "+v"(e0b[0][0]), "+v"(e0b[1][0]), "+v"(e0b[2][0]), "+v"(e0b[3][0]),
                        "+v"(e0b[0][1]), "+v"(e0b[1][1]), "+v"(e0b[2][1]), "+v"(e0b[3][1]));
      int c0dv = c0d; asm volatile("" : "+v"(c0dv));
      int e0c[4][2];
#pragma unroll
      for (int q = 0; q < 4; ++q) { e0c[q][0] = e0b[q][0] + c0dv; e0c[q][1] = e0b[q][1] + c0dv; }
      auto rd1 = [&](FragQ& f, auto SLOT, auto KS, auto I) {
        constexpr int slot = SLOT, ks = KS, i = I;
        if constexpr (i < 2) f.x[i] = lds128(L, xbs[slot >> 1][ks] + (slot & 1) * 32768 + i * 2048);
        else {
          constexpr int j = i - 2, t = j >> 1, h = j & 1;
          int a;
          if constexpr (t < 3) a = qbs[slot] + ks * 32 * 96 + t * 32 + h * 8 * 96;
          else if constexpr (t == 3) a = e1b[slot] + ks * 32 * 160 + h * 8 * 160;
          else a = h ? e0c[slot][ks] : e0b[slot][ks];
          const v2u v = __builtin_bit_cast(v2u, __builtin_amdgcn_ds_read_tr16_b64_v4i16((LAS s16x4*)(L + a)));
          v4u& d = *(t < 3 ? &f.w[t < 3 ? t : 0] : t == 3 ? &f.e1 : &f.e0);
          if constexpr (h == 0) { d.x = v.x; d.y = v.y; } else { d.z = v.x; d.w = v.y; }
        }
      };
      v4u ones_ = ONES;
      asm volatile("" : "+v"(ones_));
      auto mma1 = [&](const FragQ& f, auto I, auto FIRST) {
        constexpr int i = I;
        if constexpr (i < 6) mfma_a<FIRST>(qacc[i >> 1][i & 1], f.w[i >> 1], f.x[i & 1]);
        else if constexpr (i < 8) mfma_a<FIRST>(sq[i - 6], f.x[i - 6], f.x[i - 6]);
        else if constexpr (i == 8) mfma_a<FIRST>(cs1, ones_, f.e1);
        else mfma_a<FIRST>(cs0, ones_, f.e0);
      };
      FragQ A, B;
      waitbar<12>();
      static_for<0, 12>([&](auto I) { rd1(A, IC<0>{}, IC<0>{}, I); });
      static_for<0, 10>([&](auto ST) {
        constexpr int st = ST;
        static_for<0, 10>([&](auto I) {
          constexpr int i = I;
          mma1(A, I, IC<(st == 0)>{});
          if constexpr (i < 6) { rd1(B, IC<(st & 3)>{}, IC<1>{}, IC<2 * i>{}); rd1(B, IC<(st & 3)>{}, IC<1>{}, IC<2 * i + 1>{}); }
          SB();
        });
        if constexpr (st < 9) { if constexpr (st < 6) waitbar<8>(); else if constexpr (st == 6) waitbar<9>(); else if constexpr (st == 7) waitbar<5>(); else waitbar<1>(); }
        static_for<0, 10>([&](auto I) {
          constexpr int i = I;
          mma1(B, I, IC<0>{});
          if constexpr (st + 4 < 10 && i < 4) issue1(IC<st + 4>{}, I);
          if constexpr (st == 5 && i == 4) touch(tvo, tvb);
          if constexpr (st < 9 && i >= 2 && i < 8) { rd1(A, IC<((st + 1) & 3)>{}, IC<0>{}, IC<2 * (i - 2)>{}); rd1(A, IC<((st + 1) & 3)>{}, IC<0>{}, IC<2 * (i - 2) + 1>{}); }
          SB();
        });
      });
      MFMA_FENCE();
#pragma unroll
      for (int s = 0; s < 2; ++s) {
        if (g == 2) *(LAS float*)(L + L_STAT + (32 * tw + 16 * s + c) * 4) = qacc[2][s][0];
        if (g == (c >> 2)) {
          const int e = c & 3;
          const float v = e == 0 ? sq[s][0] : e == 1 ? sq[s][1] : e == 2 ? sq[s][2] : sq[s][3];
          *(LAS float*)(L + L_STAT + 512 + (32 * tw + 16 * s + c) * 4) = v;
        }
      }
      if (g == 0) {
        if (tw < 3) { *(LAS float*)(L + L_CSUM + (80 + 16 * tw + c) * 4) = cs0[0]; *(LAS float*)(L + L_CSUM + (16 * tw + c) * 4) = cs1[0]; }
        else { *(LAS float*)(L + L_CSUM + (48 + c) * 4) = cs0[0]; *(LAS float*)(L + L_CSUM + (64 + c) * 4) = cs1[0]; }
      }
      LBAR();
    }
    STAMP(17);
    LBAR();
    STAMP(18);
    {
      const int Ta = w < 2 ? 0 : w < 5 ? 1 : 2;
      const int Tb = (w == 0 || w == 2 || w == 7) ? 2 : (w == 4 || w == 6) ? 4 : 3;
      v4u A[4], B[4];
#pragma unroll
      for (int tg = 0; tg < 4; ++tg) {
        A[tg] = lds128(L, L_KVF + (tg * 5 + Ta) * 1024 + l * 16);
        B[tg] = lds128(L, L_KVF + (tg * 5 + Tb) * 1024 + l * 16);
      }
      f32x4 d = {0, 0, 0, 0};
      const bool one = Tb == 2 && c < 8;
#pragma unroll
      for (int tg = 0; tg < 4; ++tg) d = mfma(A[tg], one ? ONES : B[tg], d);
      const int kc = 16 * Ta + 4 * g;
      const int vc = 16 * Tb + c - 40;
      if (kc < 40) {
        const int hk = kc >= 20;
        if (vc >= 0) { if (hk == (vc >= 20)) *(LAS f32x4*)(L + L_KVC + (vc * 20 + kc - 20 * hk) * 4) = d; }
        else if (c == 0) *(LAS f32x4*)(L + L_KVC + (800 + kc) * 4) = d;
      }
    }
    LBAR();
    STAMP(19);
  }

  if (w < 4) {
    if (w == 0) {
      v4u v[4];
#pragma unroll
      for (int i = 0; i < 4; ++i) v[i] = lds128(L, L_KVC + min(i * 64 + l, 209) * 16);
#pragma unroll
      for (int i = 0; i < 4; ++i) stg_sc1(rs, i * 64 + l < 210 ? WS_KVP + (p * 16 + r) * KVP_STRIDE + (i * 64 + l) * 16 : 0x7FFFFFF0u, v[i]);
      STAMP(20);
      DRAIN();
      if (l == 0) set_flag(ws, WS_FKV + p * 256 + r * 4, tag);
      STAMP(3);
    }
    __builtin_amdgcn_s_setprio(0);
    const int f0 = min(7 * c3, 101);
    const int u3 = min(r3 + 16 * max(w - 1, 0), 39), ks3 = u3 >> 1, mat = u3 & 1;
    const int ks4 = min(r2 + 32 * max(w - 1, 0), 53);
    const unsigned area = L_KVF + max(w - 1, 0) * 12288;
    if (w > 0) {
      const char* w3 = (const char*)(mat ? wu : wg);
#pragma unroll
      for (int i = 0; i < 7; ++i) {
        const int n = 64 * i + l, pos = n / 14, ch = n - pos * 14;
        const int feat = ks3 < 16 ? 80 * (ks3 >> 1) + 32 * (ks3 & 1) + pos : 160 * (ks3 - 16) + 64 + (pos & 15) + 80 * (pos >> 4);
        glds(feat * 3456 + (f0 * 16 + 8 * ch) * 2, w3, area + i * 1024);
      }
      if (w < 3) {
#pragma unroll
        for (int i = 0; i < 5; ++i) {
          const int n = 64 * i + l, pos = n / 10, ch = n - pos * 10, t = pos >> 4;
          const int sp = ks4 < 45 ? ks4 / 3 : ks4 == 45 ? 15 : 2 * (ks4 - 46) + t;
          const int lt = ks4 < 45 ? 2 * (ks4 % 3) + t : ks4 == 45 ? 4 + t : 6;
          const int row = 16 * (min(7 * sp, 101) + lt) + (pos & 15);
          glds(row * 1280 + (80 * cs + 8 * ch) * 2, wd, area + 7168 + i * 1024);
        }
      }
    }
#pragma unroll 1
    for (int i = w; i < 100; i += 4) {
      const int n = 64 * i + l, pos = n / 10, ch2 = n - pos * 10;
      const int ks = pos >> 5, p5 = pos & 31;
      const int t = p5 >> 4, gg = (p5 >> 2) & 3, qq = p5 & 3;
      const int feat = ks < 16 ? 40 * ks + p5 : 40 * (4 * (ks - 16) + 2 * (gg >> 1) + t) + 32 + 4 * (gg & 1) + qq;
      glds(feat * 1280 + (80 * cs + 8 * ch2) * 2, wo, i * 1024);
    }
    if (w > 0) {
      if (w < 3) waitvm<25 + 5>(); else waitvm<25>();
      {
        const int tb = area + (4 * g + q4) * 224 + 8 * pp;
        v4u fr[7];
#pragma unroll
        for (int t = 0; t < 7; ++t) fr[t] = ldtr(L, tb + t * 32, 16 * 224);
        const bool ok = r3 + 16 * (w - 1) < 40;
        const unsigned ob = ok ? WS_W3 + ((c3 * 20 + ks3) * 14 + mat * 7) * 1024 + l * 16 : 0x7FFF0000u;
        float csel0 = 0.f, csel1 = 0.f;
#pragma unroll
        for (int t = 0; t < 7; ++t) {
          stg_sc1(rs, ob + t * 1024, fr[t]);
          const f32x4 cs_ = mfma(ONES, fr[t], f32x4{0, 0, 0, 0});
          if (t < 4) csel0 = (g == t) ? cs_[0] : csel0; else csel1 = (g == t - 4) ? cs_[0] : csel1;
        }
        const unsigned cb = ok ? WS_CS + (c3 * 40 + u3) * 448 + l * 4 : 0x7FFF0000u;
        stg1_sc1(rs, cb, __float_as_uint(csel0));
        stg1_sc1(rs, l < 48 ? cb + 256 : 0x7FFF0000u, __float_as_uint(csel1));
      }
      if (w < 3) {
        waitvm<25>();
        const int tb = area + 7168 + (4 * g + q4) * 160 + 8 * pp;
        v4u fr[5];
#pragma unroll
        for (int t = 0; t < 5; ++t) fr[t] = ldtr(L, tb + t * 32, 16 * 160);
        const unsigned ob = (r2 + 32 * (w - 1) < 54) ? WS_W4 + (cs * 54 + ks4) * 5 * 1024 + l * 16 : 0x7FFF0000u;
#pragma unroll
        for (int t = 0; t < 5; ++t) stg_sc1(rs, ob + t * 1024, fr[t]);
      }
    }
    DRAIN();
    if (l == 0 && lds_add(L, 3) == 3) { set_flag(ws, WS_FC + c3 * 256 + r3 * 4, tag); set_flag(ws, WS_FD + cs * 256 + r2 * 4, tag); }
    STAMPW(23, 1);
  } else {
    const int tw = w - 4;
    v4u pqA[2], pqB[2];
#pragma unroll
    for (int s = 0; s < 2; ++s) {
      const float sm = *(const LAS float*)(L + L_STAT + (32 * tw + 16 * s + c) * 4);
      const float sq2 = *(const LAS float*)(L + L_STAT + 512 + (32 * tw + 16 * s + c) * 4);
      const float m = sm * (1.0f / 640.0f);
      const float rs_ = __builtin_amdgcn_rsqf(fmaxf(sq2 * (1.0f / 640.0f) - m * m + 1e-5f, 1e-5f));
      const float mr = -m * rs_;
      unsigned pq[6];
#pragma unroll
      for (int t = 0; t < 3; ++t) {
        const f32x4 cw = *(const LAS f32x4*)(L + L_CSUM + (80 + 16 * t + 4 * g) * 4);
        pq[2 * t] = pk2v(phi2(pfma(lo2(qacc[t][s]), sp2(rs_), pfma(lo2(cw), sp2(mr), sp2(1e-6f)))));
        pq[2 * t + 1] = pk2v(phi2(pfma(hi2(qacc[t][s]), sp2(rs_), pfma(hi2(cw), sp2(mr), sp2(1e-6f)))));
      }
      pqA[s] = v4u{pq[0], pq[1], pq[2], pq[3]};
      pqB[s] = v4u{g < 2 ? pq[4] : 0u, g < 2 ? pq[5] : 0u, 0u, 0u};
    }
    STAMPW(22, 4);
    if (w == 4) { wait16(ws, WS_FKV + p * 256, tag, l); if (l == 0) lds_add(L, 0); } else lds_spin(L, 0, 1);
    STAMPW(21, 4);
    {
      const int t = min(tid - 256, 209);
      v4u v[16];
#pragma unroll
      for (int i = 0; i < 16; ++i) v[i] = ldg_sc1(rs, WS_KVP + (p * 16 + i) * KVP_STRIDE + t * 16);
      f32x4 s = {0, 0, 0, 0};
#pragma unroll
      for (int i = 0; i < 16; ++i) s += __builtin_bit_cast(f32x4, v[i]);
      const v2u pk = {pk2v(lo2(s)), pk2v(hi2(s))};
      const int fo = t / 5, f = t < 200 ? 4 * (t - fo * 5) + (fo >= 20 ? 20 : 0) : 4 * (t - 200);
      const int kst = f >> 5, tt = (f >> 4) & 1, gg = (f >> 2) & 3;
      if (t < 200) *(LAS v2u*)(L + L_BD + ((fo >> 4) * 2 + kst) * 1024 + (gg * 16 + (fo & 15)) * 16 + tt * 8) = pk;
      else {
#pragma unroll
        for (int i = 0; i < 8; ++i) *(LAS v2u*)(L + L_BD + (6 + kst) * 1024 + (gg * 16 + 2 * i + (f >= 20)) * 16 + tt * 8) = pk;
      }
    }
    asm volatile("s_waitcnt lgkmcnt(0)" ::: "memory");
    if (l == 0) lds_add(L, 1);
    lds_spin(L, 1, 4);
    STAMPW(24, 4);
    v4u bd[8];
#pragma unroll
    for (int i = 0; i < 8; ++i) bd[i] = lds128(L, L_BD + i * 1024 + l * 16);
#pragma unroll
    for (int s = 0; s < 2; ++s) {
      f32x4 den = mfma(bd[6], pqA[s], f32x4{0, 0, 0, 0}); den = mfma(bd[7], pqB[s], den);
      f32x4 num[3];
#pragma unroll
      for (int ft = 0; ft < 3; ++ft) { num[ft] = mfma(bd[2 * ft], pqA[s], f32x4{0, 0, 0, 0}); num[ft] = mfma(bd[2 * ft + 1], pqB[s], num[ft]); }
      const float r0 = __builtin_amdgcn_rcpf(den[0] + 1e-6f), r1 = __builtin_amdgcn_rcpf(den[1] + 1e-6f);
      const float rm = g == 0 ? r0 : r1;
      const unsigned base = WS_O + (8 * r + 2 * tw + s) * 20 * 1024;
      stg_sc1(rs, base + p * 1024 + l * 16, v4u{pk2v(lo2(num[0]) * r0), pk2v(hi2(num[0]) * r0), pk2v(lo2(num[1]) * rm), pk2v(hi2(num[1]) * rm)});
      stg2_sc1(rs, g < 2 ? base + (16 + (p >> 2)) * 1024 + ((2 * ((p & 3) >> 1) + g) * 16 + c) * 16 + (p & 1) * 8 : 0x7FFFFFF0u,
               v2u{pk2v(lo2(num[2]) * r1), pk2v(hi2(num[2]) * r1)});
    }
    STAMPW(26, 4);
    DRAIN();
    STAMPW(27, 4);
    if (l == 0 && lds_add(L, 2) == 3) set_flag(ws, WS_FO + r * 256 + p * 4, tag);
  }
  LBAR();
  STAMP(5);

  const int th = w & 1, kq = w >> 1;
  const int sl = 2 * th + (kq & 1);
  const int tok = 16 * (4 * r2 + sl) + c;
  {
    v2u xr[3];
#pragma unroll
    for (int i = 0; i < 3; ++i) {
      const int T = kq < 2 ? (i == 2 ? 4 : i) : (i == 2 ? 2 : 2 + i);
      xr[i] = *(const v2u*)(x + (size_t)tok * 640 + 80 * cs + 16 * T + 4 * g);
    }
    v4u wf[5][5];
    const int wb = (5 * kq * 32 + 4 * g + q4) * 160 + 8 * pp;
#pragma unroll
    for (int k = 0; k < 5; ++k)
#pragma unroll
      for (int T = 0; T < 5; ++T) wf[k][T] = ldtr(L, wb + k * 32 * 160 + T * 32, 16 * 160);
    STAMP(28);
    if (w == 0) wait16(ws, WS_FO + (r2 >> 1) * 256, tag, l);
    if (w == 1) wait16(ws, WS_FC + c3 * 256, tag, l);
    if (w == 2) wait32(ws, WS_FD + cs * 256, tag, l);
    LBAR();
    STAMP(6);
    if (b == 0 && tid == 0) set_flag(ws, WS_EPOCH, tag);
    v4u xf[5][2];
#pragma unroll
    for (int k = 0; k < 5; ++k)
#pragma unroll
      for (int s = 0; s < 2; ++s) xf[k][s] = ldg_sc1(rs, WS_O + ((4 * r2 + 2 * th + s) * 20 + 5 * kq + k) * 1024 + l * 16);
    unsigned csv[20];
    const int ct = min(tid, 223), cm = ct >= 112;
    if (w < 4) {
#pragma unroll
      for (int i = 0; i < 20; ++i) csv[i] = ldg1_sc1(rs, WS_CS + (c3 * 40 + 2 * i + cm) * 448 + (ct - 112 * cm) * 4);
    } else {
#pragma unroll
      for (int i = 0; i < 20; ++i) csv[i] = 0;
    }
    f32x4 acc[5][2];
#pragma unroll
    for (int T = 0; T < 5; ++T) { acc[T][0] = f32x4{0, 0, 0, 0}; acc[T][1] = f32x4{0, 0, 0, 0}; }
    SB();
#pragma unroll
    for (int k = 0; k < 5; ++k)
#pragma unroll
      for (int T = 0; T < 5; ++T)
#pragma unroll
        for (int s = 0; s < 2; ++s) acc[T][s] = mfma(wf[k][T], xf[k][s], acc[T][s]);
    STAMP(7);
    f32x4 own[3];
    splitk_exchange(L, acc, own, th, kq, l);
    STAMP(29);
    const bool hi = kq >= 2;
    unsigned hp[3][2];
    f32x2 sm2 = {0.f, 0.f}, sq2 = {0.f, 0.f};
#pragma unroll
    for (int i = 0; i < 3; ++i) {
      hp[i][0] = pk2v(f32x2{bflo(xr[i].x), bfhi(xr[i].x)} + lo2(own[i]));
      hp[i][1] = pk2v(f32x2{bflo(xr[i].y), bfhi(xr[i].y)} + hi2(own[i]));
      if (i == 2 && hi) { hp[2][0] = 0u; hp[2][1] = 0u; }
      const f32x2 a0 = {bflo(hp[i][0]), bfhi(hp[i][0])}, a1 = {bflo(hp[i][1]), bfhi(hp[i][1])};
      sm2 += a0 + a1;
      sq2 = pfma(a0, a0, pfma(a1, a1, sq2));
    }
    float sm = sm2.x + sm2.y, sq = sq2.x + sq2.y;
    const unsigned hb = WS_H + (4 * r2 + sl) * 20 * 1024;
    stg_sc1(rs, hb + (2 * cs + (kq >> 1)) * 1024 + l * 16, v4u{hp[0][0], hp[0][1], hp[1][0], hp[1][1]});
    stg2_sc1(rs, hi ? 0x7FFFFFF0u : hb + (16 + (cs >> 1)) * 1024 + l * 16 + (cs & 1) * 8, v2u{hp[2][0], hp[2][1]});
    sm += __shfl_xor(sm, 16); sq += __shfl_xor(sq, 16);
    sm += __shfl_xor(sm, 32); sq += __shfl_xor(sq, 32);
    stg2_sc1(rs, g == 0 ? WS_HS + ((4 * r2 + sl) * 16 + cs * 2 + (kq >> 1)) * 128 + c * 8 : 0x7FFFFFF0u, v2u{__float_as_uint(sm), __float_as_uint(sq)});
    *(LAS v2u*)(L + L_HT + (sl * 5 + (hi ? 2 : 0)) * 512 + l * 8) = v2u{hp[0][0], hp[0][1]};
    *(LAS v2u*)(L + L_HT + (sl * 5 + (hi ? 3 : 1)) * 512 + l * 8) = v2u{hp[1][0], hp[1][1]};
    *(LAS v2u*)(L + (hi ? L_SYNC + 512 : L_HT + (sl * 5 + 4) * 512) + l * 8) = v2u{hp[2][0], hp[2][1]};
    if (tid < 224) {
      float s = 0.f;
#pragma unroll
      for (int i = 0; i < 20; ++i) s += __uint_as_float(csv[i]);
      *(LAS float*)(L + L_CS3 + tid * 4) = s;
    }
    STAMP(30);
    DRAIN();
    STAMP(31);
    LBAR();
    if (tid == 0) set_flag(ws, WS_FH + (r2 >> 1) * 256 + ((r2 & 1) * 8 + cs) * 4, tag);
    STAMP(8);
  }
  {
    if (w < 4) p3_body<4, 0, false>(L, ws, rs, r3, c3, cs | ((r2 & 3) << 8), w, l, tag);
    else       p3_body<3, 4, true>(L, ws, rs, r3, c3, cs | ((r2 & 3) << 8), w, l, tag);
    STAMP(36);
    DRAIN();
    STAMP(37);
    LBAR();
    if (tid == 0) set_flag(ws, WS_FA + r3 * 256 + c3 * 4, tag);
    STAMP(12);
  }
  {
    f32x4 acc[5][2];
    if (th == 0) p4_main<0>(L, ws, r2 >> 1, r2, cs, kq, w, l, tag, acc); else p4_main<1>(L, ws, r2 >> 1, r2, cs, kq, w, l, tag, acc);
    LBAR();
    STAMP(14);
    f32x4 own[3];
    splitk_exchange(L, acc, own, th, kq, l);
    const bool hi = kq >= 2;
    u16* ob = out + (size_t)tok * 640 + 80 * cs + 4 * g;
#pragma unroll
    for (int i = 0; i < 3; ++i) {
      const int T = i == 2 ? 4 : (hi ? 2 : 0) + i;
      const v2u hh = *(const LAS v2u*)(L + L_HT + (sl * 5 + T) * 512 + l * 8);
      const v2u o = {pk2v(f32x2{bflo(hh.x), bfhi(hh.x)} + lo2(own[i])), pk2v(f32x2{bflo(hh.y), bfhi(hh.y)} + hi2(own[i]))};
      if (i < 2 || !hi) *(v2u*)(ob + 16 * T) = o;
    }
    STAMP(15);
  }
}

extern "C" void kernel_launch(void* const* d_in, const int* in_sizes, int n_in, void* d_out, int out_size, void* d_ws, size_t ws_size,
                              hipStream_t stream) {
  (void)in_sizes; (void)n_in; (void)out_size; (void)ws_size;
  (void)hipFuncSetAttribute((const void*)fused_block, hipFuncAttributeMaxDynamicSharedMemorySize, LDS_BYTES);
  fused_block<<<256, 512, LDS_BYTES, stream>>>((const u16*)d_in[0], (const u16*)d_in[1], (const u16*)d_in[2], (const u16*)d_in[4],
                                                (const u16*)d_in[5], (const u16*)d_in[6], (u16*)d_out, (char*)d_ws);
}
```

```cpp
#include <hip/hip_runtime.h>
#include <hip/hip_bf16.h>
#include <stdint.h>
#include <type_traits>

#define LAS __attribute__((address_space(3)))
#define GAS __attribute__((address_space(1)))
typedef __bf16 bf16x8 __attribute__((ext_vector_type(8)));
typedef __bf16 bf16x2 __attribute__((ext_vector_type(2)));
typedef short s16x4 __attribute__((ext_vector_type(4)));
typedef float f32x4 __attribute__((ext_vector_type(4)));
typedef float f32x2 __attribute__((ext_vector_type(2)));
typedef unsigned v4u __attribute__((ext_vector_type(4)));
typedef unsigned v2u __attribute__((ext_vector_type(2)));
typedef GAS unsigned gu32;
typedef unsigned short u16;
#define RLX_AGENT __ATOMIC_RELAXED, __HIP_MEMORY_SCOPE_AGENT

constexpr unsigned WS_EPOCH = 0, WS_TMO = 256;
constexpr unsigned WS_FKV = 4096, WS_FO = 8192, WS_FH = 12288, WS_FA = 16384, WS_FC = 20480, WS_FD = 24576;
constexpr unsigned WS_KVP = 32768, KVP_STRIDE = 3456;
constexpr unsigned WS_O = WS_KVP + 256 * KVP_STRIDE;
constexpr unsigned WS_H = WS_O + 2621440;
constexpr unsigned WS_HS = WS_H + 2621440;
constexpr unsigned WS_ACT = WS_HS + 262144;
constexpr unsigned WS_CS = WS_ACT + 7077888;
constexpr unsigned WS_W3 = WS_CS + 16 * 40 * 448;
constexpr unsigned WS_W4 = WS_W3 + 16 * 280 * 1024;
constexpr unsigned WS_STAMP = WS_W4 + 8 * 270 * 1024;
constexpr unsigned WS_END = WS_STAMP + 256 * 64 * 8;

constexpr int LDS_BYTES = 163840;
constexpr int L_KVF = 102400;
constexpr int L_KVC = 135168;
constexpr int L_BD = 138752;
constexpr int L_STAT = 146944;
constexpr int L_CSUM = 147968;
constexpr int L_SIDE = 148480;
constexpr int L_CS3 = 151168;
constexpr int L_SYNC = 152064;
constexpr int L_HT = 153600;
constexpr int L_RED = 0;
constexpr int L_X3 = 86016;

#define STAMPS 0
#define T2MODE 1
#define WOMODE 1
#define T0KB 52

__device__ __forceinline__ void glds(unsigned voff, const void* sbase, unsigned ldsaddr) {
  asm volatile("s_mov_b32 m0, %2\n\ts_nop 0\n\tglobal_load_lds_dwordx4 %0, %1" ::"v"(voff), "s"(sbase), "s"(ldsaddr) : "memory");
}
__device__ __forceinline__ void glds2_sc1(unsigned voff, const void* sbase, unsigned ldsaddr) {
  asm volatile("s_mov_b32 m0, %2\n\ts_nop 0\n\tglobal_load_lds_dwordx4 %0, %1 sc1\n\tglobal_load_lds_dwordx4 %0, %1 offset:1024 sc1" ::"v"(voff), "s"(sbase), "s"(ldsaddr) : "memory");
}
template <int IMM> __device__ __forceinline__ void gldsi(unsigned voff, const void* sbase, unsigned lbase) {
  asm volatile("s_add_u32 m0, %2, %3\n\ts_nop 0\n\tglobal_load_lds_dwordx4 %0, %1" ::"v"(voff), "s"(sbase), "s"(lbase), "i"(IMM) : "memory", "scc");
}
template <int IMM> __device__ __forceinline__ void glds2i_sc1(unsigned voff, const void* sbase, unsigned lbase) {
  asm volatile("s_add_u32 m0, %2, %3\n\ts_nop 0\n\tglobal_load_lds_dwordx4 %0, %1 sc1\n\tglobal_load_lds_dwordx4 %0, %1 offset:1024 sc1" ::"v"(voff), "s"(sbase), "s"(lbase), "i"(IMM) : "memory", "scc");
}
template <int N, int IMM> __device__ __forceinline__ void gldsn_sc1(unsigned voff, const void* sbase, unsigned lbase) {
  static_assert(N >= 1 && N <= 4, "");
  if constexpr (N == 1) asm volatile("s_add_u32 m0, %2, %3\n\ts_nop 0\n\tglobal_load_lds_dwordx4 %0, %1 sc1" ::"v"(voff), "s"(sbase), "s"(lbase), "i"(IMM) : "memory", "scc");
  if constexpr (N == 2) asm volatile("s_add_u32 m0, %2, %3\n\ts_nop 0\n\tglobal_load_lds_dwordx4 %0, %1 sc1\n\tglobal_load_lds_dwordx4 %0, %1 offset:1024 sc1" ::"v"(voff), "s"(sbase), "s"(lbase), "i"(IMM) : "memory", "scc");
  if constexpr (N == 3) asm volatile("s_add_u32 m0, %2, %3\n\ts_nop 0\n\tglobal_load_lds_dwordx4 %0, %1 sc1\n\tglobal_load_lds_dwordx4 %0, %1 offset:1024 sc1\n\tglobal_load_lds_dwordx4 %0, %1 offset:2048 sc1" ::"v"(voff), "s"(sbase), "s"(lbase), "i"(IMM) : "memory", "scc");
  if constexpr (N == 4) asm volatile("s_add_u32 m0, %2, %3\n\ts_nop 0\n\tglobal_load_lds_dwordx4 %0, %1 sc1\n\tglobal_load_lds_dwordx4 %0, %1 offset:1024 sc1\n\tglobal_load_lds_dwordx4 %0, %1 offset:2048 sc1\n\tglobal_load_lds_dwordx4 %0, %1 offset:3072 sc1" ::"v"(voff), "s"(sbase), "s"(lbase), "i"(IMM) : "memory", "scc");
}
template <int IMM, int OFF> __device__ __forceinline__ void gldso_sc1(unsigned voff, const void* sbase, unsigned lbase) {
  asm volatile("s_add_u32 m0, %2, %3\n\ts_nop 0\n\tglobal_load_lds_dwordx4 %0, %1 offset:%4 sc1" ::"v"(voff), "s"(sbase), "s"(lbase), "i"(IMM), "i"(OFF) : "memory", "scc");
}
__device__ __forceinline__ void ldg_asm_sc1(v4u& d, unsigned voff, const void* sbase) {
  asm volatile("global_load_dwordx4 %0, %1, %2 sc1" : "=v"(d) : "v"(voff), "s"(sbase) : "memory");
}
__device__ __forceinline__ void ldg2_asm_sc1(v2u& d, unsigned voff, const void* sbase) {
  asm volatile("global_load_dwordx2 %0, %1, %2 sc1" : "=v"(d) : "v"(voff), "s"(sbase) : "memory");
}
template <int N> __device__ __forceinline__ void wait_for(v4u& a, v4u& b) { asm volatile("s_waitcnt vmcnt(%2)" : "+v"(a), "+v"(b) : "i"(N) : "memory"); }
template <int N> __device__ __forceinline__ void waitvm() { asm volatile("s_waitcnt vmcnt(%0)" ::"i"(N) : "memory"); }
__device__ __forceinline__ f32x2 pfma(f32x2 a, f32x2 b, f32x2 c) { return __builtin_elementwise_fma(a, b, c); }
__device__ __forceinline__ f32x2 sp2(float a) { return f32x2{a, a}; }
__device__ __forceinline__ f32x2 lo2(f32x4 v) { return __builtin_shufflevector(v, v, 0, 1); }
__device__ __forceinline__ f32x2 hi2(f32x4 v) { return __builtin_shufflevector(v, v, 2, 3); }
__device__ __forceinline__ unsigned pk2v(f32x2 v) { return __builtin_bit_cast(unsigned, __builtin_convertvector(v, bf16x2)); }
__device__ __forceinline__ f32x2 phi2(f32x2 te) {
  const f32x2 y = pfma(te, sp2(1.44269504088896f), sp2(-1.44269504088896e-6f));
  const f32x2 e = {__builtin_amdgcn_fmed3f(__builtin_amdgcn_exp2f(y.x), 0.f, 1.f), __builtin_amdgcn_fmed3f(__builtin_amdgcn_exp2f(y.y), 0.f, 1.f)};
  const f32x2 m = {fmaxf(te.x, 1e-6f), fmaxf(te.y, 1e-6f)};
  return m + e;
}
__device__ __forceinline__ unsigned geglu2(f32x2 ag, f32x2 au, f32x2 cg, f32x2 cu, float rstd, float mr) {
  const f32x2 gv = pfma(ag, sp2(rstd), cg * sp2(mr)), uv = pfma(au, sp2(rstd), cu * sp2(mr));
  const f32x2 z = gv * pfma(gv * gv, sp2(-0.1029432397f), sp2(-2.302208198f));
  const f32x2 d = {1.0f + __builtin_amdgcn_exp2f(z.x), 1.0f + __builtin_amdgcn_exp2f(z.y)};
  const f32x2 rc = {__builtin_amdgcn_rcpf(d.x), __builtin_amdgcn_rcpf(d.y)};
  return pk2v(gv * uv * rc);
}
template <int V> using IC = std::integral_constant<int, V>;
template <int I, int N, class F> __device__ __forceinline__ void static_for(F&& f) {
  if constexpr (I < N) { f(std::integral_constant<int, I>{}); static_for<I + 1, N>(f); }
}
#define LBAR() asm volatile("s_waitcnt lgkmcnt(0)\n\ts_barrier" ::: "memory")
#define DRAIN() asm volatile("s_waitcnt vmcnt(0)" ::: "memory")
#define SB() __builtin_amdgcn_sched_barrier(0)
template <int N> __device__ __forceinline__ void waitbar() { asm volatile("s_waitcnt vmcnt(%0) lgkmcnt(0)\n\ts_barrier" ::"i"(N) : "memory"); }

__device__ __forceinline__ unsigned pk2(float a, float b) {
  bf16x2 v = {(__bf16)a, (__bf16)b};
  return __builtin_bit_cast(unsigned, v);
}
__device__ __forceinline__ float bflo(unsigned u) { return __uint_as_float(u << 16); }
__device__ __forceinline__ float bfhi(unsigned u) { return __uint_as_float(u & 0xffff0000u); }
__device__ __forceinline__ f32x4 mfma(v4u a, v4u b, f32x4 c) {
  return __builtin_amdgcn_mfma_f32_16x16x32_bf16(__builtin_bit_cast(bf16x8, a), __builtin_bit_cast(bf16x8, b), c, 0, 0, 0);
}
__device__ __forceinline__ void mfma_acc(f32x4& acc, v4u a, v4u b) {
  asm volatile("v_mfma_f32_16x16x32_bf16 %0, %1, %2, %0" : "+v"(acc) : "v"(a), "v"(b));
}
__device__ __forceinline__ void mfma_first(f32x4& acc, v4u a, v4u b) {
  asm volatile("v_mfma_f32_16x16x32_bf16 %0, %1, %2, 0" : "=&v"(acc) : "v"(a), "v"(b));
}
template <bool FIRST> __device__ __forceinline__ void mfma_a(f32x4& acc, v4u a, v4u b) { if constexpr (FIRST) mfma_first(acc, a, b); else mfma_acc(acc, a, b); }
#define MFMA_FENCE() asm volatile("s_nop 7\n\ts_nop 1" ::: "memory")
__device__ __forceinline__ v4u lds128(LAS char* L, int off) { return *(const LAS v4u*)(L + off); }
__device__ __forceinline__ v4u ldtr(LAS char* L, int off, int d) {
  s16x4 a = __builtin_amdgcn_ds_read_tr16_b64_v4i16((LAS s16x4*)(L + off));
  s16x4 b = __builtin_amdgcn_ds_read_tr16_b64_v4i16((LAS s16x4*)(L + off + d));
  v2u x = __builtin_bit_cast(v2u, a), y = __builtin_bit_cast(v2u, b);
  return v4u{x.x, x.y, y.x, y.y};
}
__device__ __forceinline__ float phi_eps(float te) {
  const float e = __builtin_amdgcn_fmed3f(__builtin_amdgcn_exp2f((te - 1e-6f) * 1.44269504088896f), 0.f, 1.f);
  return fmaxf(te, 1e-6f) + e;
}
__device__ __forceinline__ float rowsum4(float v) {
  auto a = __builtin_amdgcn_permlane16_swap(__float_as_uint(v), __float_as_uint(v), false, false);
  const float u = __uint_as_float(a[0]) + __uint_as_float(a[1]);
  auto b = __builtin_amdgcn_permlane32_swap(__float_as_uint(u), __float_as_uint(u), false, false);
  return __uint_as_float(b[0]) + __uint_as_float(b[1]);
}
__device__ __forceinline__ v4u ldg_sc1(__amdgpu_buffer_rsrc_t rs, unsigned off) { return __builtin_amdgcn_raw_buffer_load_b128(rs, off, 0, 16); }
__device__ __forceinline__ v2u ldg2_sc1(__amdgpu_buffer_rsrc_t rs, unsigned off) { return __builtin_amdgcn_raw_buffer_load_b64(rs, off, 0, 16); }
__device__ __forceinline__ unsigned ldg1_sc1(__amdgpu_buffer_rsrc_t rs, unsigned off) { return __builtin_amdgcn_raw_buffer_load_b32(rs, off, 0, 16); }
__device__ __forceinline__ void stg_sc1(__amdgpu_buffer_rsrc_t rs, unsigned off, v4u v) { __builtin_amdgcn_raw_buffer_store_b128(v, rs, off, 0, 16); }
__device__ __forceinline__ void stg2_sc1(__amdgpu_buffer_rsrc_t rs, unsigned off, v2u v) { __builtin_amdgcn_raw_buffer_store_b64(v, rs, off, 0, 16); }
__device__ __forceinline__ void stg1_sc1(__amdgpu_buffer_rsrc_t rs, unsigned off, unsigned v) { __builtin_amdgcn_raw_buffer_store_b32(v, rs, off, 0, 16); }

__device__ __forceinline__ void wait16(char* ws, unsigned off, unsigned tag, int lane) {
  gu32* f = (gu32*)(ws + off) + (lane & 15);
  for (unsigned sp = 0; sp < (1u << 15); ++sp) {
    unsigned v = __hip_atomic_load(f, RLX_AGENT);
    if (__all(v == tag)) return;
    __builtin_amdgcn_s_sleep(1);
  }
  if (lane == 0) __hip_atomic_store((gu32*)(ws + WS_TMO), 1u, RLX_AGENT);
}
__device__ __forceinline__ void wait32(char* ws, unsigned off, unsigned tag, int lane) {
  gu32* f = (gu32*)(ws + off) + (lane & 31);
  for (unsigned sp = 0; sp < (1u << 15); ++sp) {
    unsigned v = __hip_atomic_load(f, RLX_AGENT);
    if (__all(v == tag)) return;
    __builtin_amdgcn_s_sleep(1);
  }
  if (lane == 0) __hip_atomic_store((gu32*)(ws + WS_TMO), 1u, RLX_AGENT);
}
typedef LAS unsigned lu32;
__device__ __forceinline__ unsigned lds_add(LAS char* L, int word) { return __hip_atomic_fetch_add((lu32*)(L + L_SYNC) + word, 1u, __ATOMIC_RELAXED, __HIP_MEMORY_SCOPE_WORKGROUP); }
__device__ __forceinline__ void lds_spin(LAS char* L, int word, unsigned want) {
  for (unsigned sp = 0; sp < (1u << 20); ++sp) {
    if (__builtin_amdgcn_readfirstlane(__hip_atomic_load((lu32*)(L + L_SYNC) + word, __ATOMIC_RELAXED, __HIP_MEMORY_SCOPE_WORKGROUP)) >= want) break;
  }
  asm volatile("" ::: "memory");
}
__device__ __forceinline__ void touch(unsigned voff, const void* sbase) {
  asm volatile("s_mov_b32 m0, %2\n\ts_nop 0\n\tglobal_load_lds_dword %0, %1" ::"v"(voff), "s"(sbase), "s"(L_SYNC + 64) : "memory");
}
__device__ __forceinline__ void touch64(const void* addr) {
  asm volatile("s_mov_b32 m0, %1\n\ts_nop 0\n\tglobal_load_lds_dword %0, off" ::"v"(addr), "s"(L_SYNC + 64) : "memory");
}
__device__ __forceinline__ void set_flag(char* ws, unsigned off, unsigned tag) { __hip_atomic_store((gu32*)(ws + off), tag, RLX_AGENT); }
#define STAMPW(i, wv) do {} while (0)
#define STAMP(i) STAMPW(i, 0)

__device__ __forceinline__ void splitk_exchange(LAS char* L, f32x4 (&acc)[5][2], f32x4 (&own)[3], int th, int kq, int l) {
  const int wb = L_RED + (th * 40 + kq) * 1024 + l * 16;
#pragma unroll
  for (int s = 0; s < 2; ++s)
#pragma unroll
    for (int T = 0; T < 5; ++T) *(LAS f32x4*)(L + wb + (s * 5 + T) * 4096) = acc[T][s];
  LBAR();
  const int rb = L_RED + (th * 2 + (kq & 1)) * 5 * 4096 + l * 16, t0 = rb + (kq >> 1) * 8192;
  f32x4 v[3][4];
#pragma unroll
  for (int k = 0; k < 4; ++k) {
    v[0][k] = *(const LAS f32x4*)(L + t0 + k * 1024);
    v[1][k] = *(const LAS f32x4*)(L + t0 + 4096 + k * 1024);
    v[2][k] = *(const LAS f32x4*)(L + rb + 4 * 4096 + k * 1024);
  }
#pragma unroll
  for (int i = 0; i < 3; ++i) own[i] = (v[i][0] + v[i][1]) + (v[i][2] + v[i][3]);
}

#define P4XW 4
constexpr int P4_XW = P4XW;
constexpr int P4_PRE = 4;
#define P4EARLY 10
constexpr int P4_EARLY = P4EARLY;
constexpr int p4_g(int it, int n) { return it == 4 ? n : it == 6 ? 2 * n : 0; }
constexpr int p4_x(int it) { return it + P4_XW < 14 ? 2 : 0; }
constexpr int p4_xwait(int st, int n) {
  int y = 0;
  if (st < P4_XW) { y = 2 * (P4_XW - 1 - st); for (int j = 0; j < st; ++j) y += p4_x(j) + p4_g(j, n); }
  else { y = p4_g(st - P4_XW, n); for (int j = st - P4_XW + 1; j < st; ++j) y += p4_x(j) + p4_g(j, n); }
  return y;
}
constexpr int p4_wwait(int st, int n) {
  int y = st == 10 ? n : 0;
  for (int j = 7; j < st; ++j) y += p4_x(j);
  return y;
}
template <int TH, int stg> __device__ __forceinline__ void p4_issue(char* ws, int cs, int kq, int l) {
  constexpr int N = TH == 0 ? 3 : 2;
  const int kb = kq < 2 ? 14 * kq : 28 + 13 * (kq - 2);
  gldsn_sc1<N, (stg % 7) * 20480>(l * 16, ws + WS_W4 + (size_t)cs * 270 * 1024 + TH * 3072 + (size_t)min(kb + stg, 53) * 5120, kq * 5120 + TH * 3072);
}
template <int J> __device__ __forceinline__ void p4_pre_piece(const char* src, unsigned l16, int th, unsigned lbase) {
  constexpr int q0 = 2 * J, q1 = (2 * J + 1 < 35) ? 2 * J + 1 : 34;
  constexpr int f0 = (q0 / 5) * 20480 + (q0 % 5) * 1024, f1 = (q1 / 5) * 20480 + (q1 % 5) * 1024;
  asm volatile("s_mov_b32 m0, %2\n\ts_nop 0\n\tglobal_load_lds_dwordx4 %0, %1 sc1" ::"v"(l16), "s"(src + (th ? q1 : q0) * 1024), "s"(lbase + (th ? f1 : f0)) : "memory");
}
template <int TH>
__device__ __forceinline__ void p4_main(LAS char* L, char* ws, int r, int r2, int cs, int kq, int w, int l, unsigned tag, f32x4 (&acc)[5][2]) {
  constexpr int N = TH == 0 ? 3 : 2;
  const int kb = kq < 2 ? 14 * kq : 28 + 13 * (kq - 2);
  const int nk = kq < 2 ? 14 : 13;
  const unsigned l16 = l * 16;
  auto issue = [&](auto STG) { p4_issue<TH, STG>(ws, cs, kq, l); };
  int wbs[3];
  wbs[0] = kq * 5120 + l16; wbs[1] = wbs[0] + 3 * 20480; wbs[2] = wbs[0] + 6 * 20480;
  asm volatile("" : "+v"(wbs[0]), "+v"(wbs[1]), "+v"(wbs[2]));
  {
    const char* w4src = ws + WS_W4 + (size_t)cs * 270 * 1024 + (size_t)kb * 5120;
    static_for<P4_EARLY, 18>([&](auto J) { p4_pre_piece<J>(w4src, l16, TH, kq * 5120); });
    static_assert(P4_EARLY >= 10, "");
  }
  v4u wp[P4_PRE][5];
  static_for<0, P4_PRE>([&](auto ST) { static_for<0, 5>([&](auto I) { wp[ST][I] = lds128(L, wbs[ST / 3] + (ST % 3) * 20480 + I * 1024); }); });
  LBAR();
  static_for<7, 7 + P4_PRE>([&](auto ST) { issue(ST); });
  STAMP(38);
  if (w == 0) wait16(ws, WS_FA + r * 256, tag, l);
  STAMP(39);
  waitbar<P4_PRE * N>();
  STAMP(13);
  v4u xf[14][2];
  const char* xa = ws + WS_ACT + (size_t)(4 * r2 + 2 * TH) * 54 * 1024;
  auto ldx = [&](auto K, auto S) {
    constexpr int k = K, s = S;
    ldg_asm_sc1(xf[k][s], l16, xa + (size_t)(s * 54 + min(kb + k, 53)) * 1024);
  };
  static_for<0, P4_XW>([&](auto K) { ldx(K, IC<0>{}); ldx(K, IC<1>{}); });
  v4u wf[2][5];
  auto rd1 = [&](auto STG, auto I) {
    constexpr int stg = STG, T = I, sl7 = stg % 7;
    wf[stg & 1][T] = lds128(L, wbs[sl7 / 3] + (sl7 % 3) * 20480 + T * 1024);
  };
  static_for<0, 14>([&](auto ST) {
    constexpr int st = ST;
    if constexpr (st == 4 || st == 6) LBAR();
    if constexpr (st == 10 || st == 12) waitbar<p4_wwait(st, N)>();
    if constexpr (st == 1) STAMP(40);
    if constexpr (st == 4) STAMP(41);
    if constexpr (st == 7) STAMP(42);
    if constexpr (st == 10) STAMP(43);
    if constexpr (st == 13) STAMP(44);
    wait_for<p4_xwait(st, N)>(xf[st][0], xf[st][1]);
    SB();
    if (st < 13 || st < nk) {
      static_for<0, 10>([&](auto I) {
        constexpr int i = I;
        if constexpr (st < P4_PRE) mfma_a<(st == 0)>(acc[i >> 1][i & 1], wp[st][i >> 1], xf[st][i & 1]);
        else mfma_a<false>(acc[i >> 1][i & 1], wf[st & 1][i >> 1], xf[st][i & 1]);
        if constexpr (st + P4_XW < 14 && (i == 0 || i == 2)) ldx(IC<st + P4_XW>{}, IC<(i >> 1)>{});
        if constexpr (st == 4 && i == 4) issue(IC<11>{});
        if constexpr (st == 6 && (i == 4 || i == 7)) issue(IC<12 + (i == 7)>{});
        if constexpr (st >= P4_PRE - 1 && st < 13 && i >= 2 && i < 7) rd1(IC<st + 1>{}, IC<i - 2>{});
        SB();
      });
    }
  });
  MFMA_FENCE();
}

template <int NT> struct Frag3 { v4u x[2], g[NT], u[NT]; };

template <int NT, int TILE0, bool LOADER>
__device__ __forceinline__ void p3_body(LAS char* L, char* ws, __amdgpu_buffer_rsrc_t rs, int r, int p, int cs, int w, int l, unsigned tag) {
  const int c = l & 15, g = l >> 4, tg = w & 3;
  const unsigned l16 = l * 16;
  constexpr int NWP = LOADER ? 3 : 4, NP = LOADER ? 7 : 4;
  const int wp0 = LOADER ? 16 + 3 * tg : 4 * tg;
  const char* wsrc = ws + WS_W3 + (size_t)p * 280 * 1024 + wp0 * 1024;
  const char* hx = ws + WS_H + (size_t)(8 * r + 2 * tg) * 20 * 1024;
  const unsigned lw = wp0 * 1024, lx = L_X3 + tg * 4096;
  auto piece = [&](auto STG, auto Q) {
    constexpr int stg = STG, q = Q, slot = stg % 3;
    if constexpr (q < NWP) gldso_sc1<slot * 28672, q * 1024>(l16, wsrc + stg * 28672, lw);
    else gldso_sc1<slot * 16384 + ((q - NWP) >> 1) * 2048, ((q - NWP) & 1) * 1024>(l16, hx + stg * 2048 + ((q - NWP) >> 1) * 20480, lx);
  };
  static_for<0, 3>([&](auto ST) { static_for<0, NWP>([&](auto Q) { piece(ST, Q); }); });
  if constexpr (!LOADER) {
    const int ct = min(w * 64 + l, 223), cm = ct >= 112;
    unsigned csv[20];
#pragma unroll
    for (int i = 0; i < 20; ++i) csv[i] = ldg1_sc1(rs, WS_CS + (p * 40 + 2 * i + cm) * 448 + (ct - 112 * cm) * 4);
    float s = 0.f;
#pragma unroll
    for (int i = 0; i < 20; ++i) s += __uint_as_float(csv[i]);
    *(LAS float*)(L + (w * 64 + l < 224 ? L_CS3 + ct * 4 : L_SYNC + 512)) = s;
  }
  STAMP(32);
  if (w == 7) { wait16(ws, WS_FH + r * 256, tag, l); STAMPW(33, 7); }
  DRAIN();
  LBAR();
  STAMP(10);
  if constexpr (LOADER) static_for<0, 3>([&](auto ST) { static_for<NWP, NP>([&](auto Q) { piece(ST, Q); }); });
  v2u stp[2][4];
#pragma unroll
  for (int s = 0; s < 2; ++s)
#pragma unroll
    for (int i = 0; i < 4; ++i) ldg2_asm_sc1(stp[s][i], (4 * g + i) * 128 + c * 8, ws + WS_HS + (8 * r + 2 * tg + s) * 16 * 128);

  f32x4 ag[NT][2], au[NT][2];
  float rstd[2], mr[2];
  const int xo = L_X3 + tg * 4096 + l16;
  int wb0 = TILE0 * 1024 + l16, wb2 = wb0 + 2 * 28672;
  asm volatile("" : "+v"(wb0), "+v"(wb2));
  auto rd1 = [&](Frag3<NT>& f, auto SLOT, auto KS, auto I) {
    constexpr int slot = SLOT, ks = KS, i = I;
    if constexpr (i < 2) f.x[i] = lds128(L, xo + slot * 16384 + ks * 1024 + i * 2048);
    else {
      constexpr int j = i - 2, t = j >> 1, u = j & 1, off = ks * 14336 + u * 7168 + t * 1024;
      const v4u v = slot < 2 ? lds128(L, wb0 + slot * 28672 + off) : lds128(L, wb2 + off);
      if constexpr (u == 0) f.g[t] = v; else f.u[t] = v;
    }
  };
  auto mma1 = [&](const Frag3<NT>& f, auto I, auto FIRST) {
    constexpr int i = I, t = i >> 2, u = (i >> 1) & 1, s = i & 1;
    if constexpr (u == 0) mfma_a<FIRST>(ag[t][s], f.g[t], f.x[s]); else mfma_a<FIRST>(au[t][s], f.u[t], f.x[s]);
  };
  constexpr int NM = 4 * NT, NR = 2 + 2 * NT;
  Frag3<NT> A, B;
  if constexpr (LOADER) waitbar<8 + 8>(); else LBAR();
  STAMP(35);
  static_for<0, NR>([&](auto I) { rd1(A, IC<0>{}, IC<0>{}, I); });
  static_for<0, 10>([&](auto ST) {
    constexpr int st = ST, slot = st % 3, nslot = (st + 1) % 3;
    static_for<0, NM>([&](auto I) {
      constexpr int i = I;
      mma1(A, I, IC<(st == 0)>{});
      if constexpr (i < NR) rd1(B, IC<slot>{}, IC<1>{}, I);
      SB();
    });
    if constexpr (st < 9) {
      if constexpr (st == 0) { if constexpr (LOADER) waitbar<4 + 8>(); else LBAR(); }
      else if constexpr (st == 1) { if constexpr (LOADER) waitbar<NP>(); else LBAR(); }
      else if constexpr (st < 8) waitbar<NP>(); else waitbar<0>();
    }
    if constexpr (st == 4) {
      asm volatile("" : "+v"(stp[0][0]), "+v"(stp[0][1]), "+v"(stp[0][2]), "+v"(stp[0][3]), "+v"(stp[1][0]), "+v"(stp[1][1]), "+v"(stp[1][2]), "+v"(stp[1][3]));
#pragma unroll
      for (int s = 0; s < 2; ++s) {
        float sm = 0.f, sq = 0.f;
#pragma unroll
        for (int i = 0; i < 4; ++i) { sm += __uint_as_float(stp[s][i].x); sq += __uint_as_float(stp[s][i].y); }
        sm = rowsum4(sm); sq = rowsum4(sq);
        const float mean = sm * (1.0f / 640.0f);
        rstd[s] = __builtin_amdgcn_rsqf(fmaxf(sq * (1.0f / 640.0f) - mean * mean, 0.f) + 1e-5f);
        mr[s] = -mean * rstd[s];
      }
      asm volatile("" : "+v"(rstd[0]), "+v"(rstd[1]), "+v"(mr[0]), "+v"(mr[1]));
    }
    if constexpr (st == 2) STAMP(45);
    if constexpr (st == 5) STAMP(46);
    if constexpr (st == 8) STAMP(47);
    static_for<0, NM>([&](auto I) {
      constexpr int i = I;
      mma1(B, I, IC<0>{});
      if constexpr (LOADER) { if constexpr (st <= 6 && i < NP) piece(IC<st + 3>{}, I); }
      else { if constexpr (st <= 6 && (i % 3) == 0 && i / 3 < NP) piece(IC<st + 3>{}, IC<i / 3>{}); }
      if constexpr (st < 9 && i < NR) rd1(A, IC<nslot>{}, IC<0>{}, I);
      SB();
    });
  });
  MFMA_FENCE();
  STAMP(11);
  LBAR();
  const int kq4 = w >> 1, th4 = w & 1;
  const char* w4src = ws + WS_W4 + (size_t)(cs & 7) * 270 * 1024 + (size_t)(kq4 < 2 ? 14 * kq4 : 28 + 13 * (kq4 - 2)) * 5120;
  const unsigned w4l = kq4 * 5120;
  f32x4 cgv[NT], cuv[NT];
#pragma unroll
  for (int t = 0; t < NT; ++t) {
    cgv[t] = *(const LAS f32x4*)(L + L_CS3 + ((TILE0 + t) * 16 + 4 * g) * 4);
    cuv[t] = *(const LAS f32x4*)(L + L_CS3 + (112 + (TILE0 + t) * 16 + 4 * g) * 4);
  }
  static_for<0, 2>([&](auto S) {
    constexpr int s = S;
    unsigned y[NT][2];
    const unsigned ab = WS_ACT + (8 * r + 2 * tg + s) * 54 * 1024 + l16;
    static_for<0, NT>([&](auto T) {
      constexpr int t = T, step = s * NT + t, j0 = step < NT ? step * P4_EARLY / NT : P4_EARLY, j1 = step < NT ? (step + 1) * P4_EARLY / NT : P4_EARLY;
      y[t][0] = geglu2(lo2(ag[t][s]), lo2(au[t][s]), lo2(cgv[t]), lo2(cuv[t]), rstd[s], mr[s]);
      static_for<j0, (j0 + j1 + 1) / 2>([&](auto J) { p4_pre_piece<J>(w4src, l16, th4, w4l); });
      y[t][1] = geglu2(hi2(ag[t][s]), hi2(au[t][s]), hi2(cgv[t]), hi2(cuv[t]), rstd[s], mr[s]);
      static_for<(j0 + j1 + 1) / 2, j1>([&](auto J) { p4_pre_piece<J>(w4src, l16, th4, w4l); });
      if constexpr (t & 1) {
        if constexpr (TILE0 == 0) stg_sc1(rs, p < 15 ? ab + (3 * p + (t >> 1)) * 1024 : 0x7FFFFFF0u, v4u{y[t - 1][0], y[t - 1][1], y[t][0], y[t][1]});
        else stg_sc1(rs, ab + (p < 15 ? 3 * p + 2 : 45) * 1024, v4u{y[t - 1][0], y[t - 1][1], y[t][0], y[t][1]});
      }
    });
    if constexpr (TILE0 != 0) stg2_sc1(rs, ab + (46 + (p >> 1)) * 1024 + (p & 1) * 8, v2u{y[NT - 1][0], y[NT - 1][1]});
  });
}

struct FragKV { v4u x[2], w[5]; };

__global__ __launch_bounds__(512, 2) void fused_block(const u16* __restrict__ x, const u16* __restrict__ wqkv, const u16* __restrict__ wo,
                                                      const u16* __restrict__ wg, const u16* __restrict__ wu, const u16* __restrict__ wd,
                                                      u16* __restrict__ out, char* __restrict__ ws) {
  extern __shared__ __attribute__((aligned(16))) char smem_[];
  LAS char* L = (LAS char*)smem_;
  const int tid = threadIdx.x, l = tid & 63, c = l & 15, g = l >> 4;
  const int w = __builtin_amdgcn_readfirstlane(tid >> 6);
  const int b = blockIdx.x, xcd = b & 7, jj = b >> 3, rg = xcd >> 1, cgp = xcd & 1;
  const int r = 4 * rg + (jj >> 3), p = 8 * cgp + (jj & 7);
  const int r2 = 4 * xcd + (jj >> 3), cs = jj & 7;
  const int r3 = 8 * (xcd >> 2) + (jj >> 2), c3 = 4 * (xcd & 3) + (jj & 3);
  const __amdgpu_buffer_rsrc_t rs = __builtin_amdgcn_make_buffer_rsrc(ws, 0, WS_END, 0x00020000);
  const v4u ONES = {0x3F803F80u, 0x3F803F80u, 0x3F803F80u, 0x3F803F80u};
  const int q4 = (l & 15) >> 2, pp = l & 3;
  STAMP(0);

  unsigned tag;
  constexpr int S1 = 26624;
  const int xo0 = c * 128 + ((g ^ (c & 7)) << 4);
  const int wrow = 16 * (g >> 1) + 4 * (g & 1) + q4;
  const int kvo = 16384 + wrow * 160 + 8 * pp;
  auto kv_product = [&]() {
    const int Ta = w < 2 ? 0 : w < 5 ? 1 : 2;
    const int Tb = (w == 0 || w == 2 || w == 7) ? 2 : (w == 4 || w == 6) ? 4 : 3;
    v4u A[4], B[4];
#pragma unroll
    for (int tg = 0; tg < 4; ++tg) {
      A[tg] = lds128(L, L_KVF + (tg * 5 + Ta) * 1024 + l * 16);
      B[tg] = lds128(L, L_KVF + (tg * 5 + Tb) * 1024 + l * 16);
    }
    f32x4 d = {0, 0, 0, 0};
    const bool one = Tb == 2 && c < 8;
#pragma unroll
    for (int tg = 0; tg < 4; ++tg) d = mfma(A[tg], one ? ONES : B[tg], d);
    const int kc = 16 * Ta + 4 * g;
    const int vc = 16 * Tb + c - 40;
    const int hk = kc >= 20;
    const bool iskv = kc < 40 && vc >= 0 && hk == (vc >= 20), isz = kc < 40 && vc < 0 && c == 0;
    *(LAS f32x4*)(L + (iskv ? L_KVC + (vc * 20 + kc - 20 * hk) * 4 : isz ? L_KVC + (800 + kc) * 4 : L_SYNC + 512 + (l & 31) * 16)) = d;
  };

  if (w < 4) {
    {
      const char* pc;
      asm volatile("s_getpc_b64 %0" : "=s"(pc));
      const int n = (jj * 4 + w) * 4 + (l & 3);
      if (l < 4) touch64(pc + min(n * 128, T0KB * 1024 - 128));
    }
#pragma unroll
    for (int i = 0; i < 2; ++i) {
      const int idx = ((jj * 4 + w) * 2 + i) * 64 + l;
      const int wr_ = idx / 15, pc = idx - wr_ * 15, sec = pc / 5;
      const int xi = min(idx - 9600, 5119);
      touch64(idx < 9600 ? (const char*)wqkv + wr_ * 3840 + sec * 1280 + cgp * 640 + (pc - sec * 5) * 128
                         : (const char*)x + (size_t)rg * 512 * 1280 + xi * 128);
    }
    if (l == 0) asm volatile("s_mov_b32 m0, %1\n\ts_nop 0\n\tglobal_load_lds_dword %0, off sc1" ::"v"(ws + WS_EPOCH), "s"(L_SYNC + 32) : "memory");
    *(LAS v4u*)(L + L_BD + tid * 16) = v4u{0, 0, 0, 0};
    *(LAS v4u*)(L + L_BD + 4096 + tid * 16) = v4u{0, 0, 0, 0};
    if (tid < 2) *(LAS v4u*)(L + L_SYNC + tid * 16) = v4u{0, 0, 0, 0};
    auto hint2 = [&](int i) {
      const int n = 96 * w + 16 * i + (l & 15);
      const int li = b * 228 + min(n, 227);
      const char* aw_ = li < 6400 ? (const char*)wo + li * 128 : li < 23680 ? (const char*)wg + (li - 6400) * 128
                      : li < 40960 ? (const char*)wu + (li - 23680) * 128 : (const char*)wd + min(li - 40960, 17279) * 128;
      const int m = n - 228;
      const unsigned o5 = m < 32 ? WS_O + ((8 * r + (m >> 2)) * 20 + 16 + (p >> 2)) * 1024 + ((p & 3) >> 1) * 512 + (m & 3) * 128
                        : m < 64 ? WS_H + ((4 * r2 + ((m - 32) >> 3)) * 20 + 16 + (cs >> 1)) * 1024 + (m & 7) * 128
                                 : WS_ACT + ((8 * r3 + (min(m - 64, 63) >> 3)) * 54 + 46 + (c3 >> 1)) * 1024 + (m & 7) * 128;
      const char* a = n < 228 ? aw_ : ws + o5;
      if (l < 16) touch64(a);
    };
    STAMP(1);
    f32x4 acc[2][5];
    {
      const int xw = w * 32 * 128;
      int xbs[3][2], kbs[3];
#pragma unroll
      for (int q = 0; q < 3; ++q) { xbs[q][0] = xw + xo0 + 2 * q * S1; xbs[q][1] = xw + (xo0 ^ 64) + 2 * q * S1; kbs[q] = kvo + 2 * q * S1; }
      asm volatile("" : "+v"(xbs[0][0]), "+v"(xbs[0][1]), "+v"(xbs[1][0]), "+v"(xbs[1][1]), "+v"(xbs[2][0]), "+v"(xbs[2][1]), "+v"(kbs[0]), "+v"(kbs[1]), "+v"(kbs[2]));
      auto rd1 = [&](FragKV& f, auto SLOT, auto KS, auto I) {
        constexpr int slot = SLOT, ks = KS, i = I;
        if constexpr (i < 2) f.x[i] = lds128(L, xbs[slot >> 1][ks] + (slot & 1) * S1 + i * 2048);
        else {
          constexpr int j = i - 2, t = j >> 1, h = j & 1;
          const v2u v = __builtin_bit_cast(v2u, __builtin_amdgcn_ds_read_tr16_b64_v4i16((LAS s16x4*)(L + kbs[slot >> 1] + (slot & 1) * S1 + ks * 32 * 160 + t * 32 + h * 8 * 160)));
          if constexpr (h == 0) { f.w[t].x = v.x; f.w[t].y = v.y; } else { f.w[t].z = v.x; f.w[t].w = v.y; }
        }
      };
      auto mma1 = [&](const FragKV& f, auto I, auto FIRST) { constexpr int i = I, t = i >> 1, s = i & 1; mfma_a<FIRST>(acc[s][t], f.x[s], f.w[t]); };
      FragKV A, B;
      LBAR();
      static_for<0, 12>([&](auto I) { rd1(A, IC<0>{}, IC<0>{}, I); });
      static_for<0, 10>([&](auto ST) {
        constexpr int st = ST;
        static_for<0, 10>([&](auto I) {
          constexpr int i = I;
          mma1(A, I, IC<(st == 0)>{});
          if constexpr (i < 6) { rd1(B, IC<(st % 5)>{}, IC<1>{}, IC<2 * i>{}); rd1(B, IC<(st % 5)>{}, IC<1>{}, IC<2 * i + 1>{}); }
          SB();
        });
        if constexpr (st < 9) LBAR();
        if constexpr (st == 0) STAMP(48);
        if constexpr (st == 3) STAMP(49);
        if constexpr (st == 6) STAMP(50);
        static_for<0, 10>([&](auto I) {
          constexpr int i = I;
          mma1(B, I, IC<0>{});
          if constexpr (st >= 2 && st < 8 && i == 8) hint2(st - 2);
          if constexpr (st < 9 && i < 6) { rd1(A, IC<((st + 1) % 5)>{}, IC<0>{}, IC<2 * i>{}); rd1(A, IC<((st + 1) % 5)>{}, IC<0>{}, IC<2 * i + 1>{}); }
          SB();
        });
      });
      MFMA_FENCE();
    }
    STAMP(2);
    __builtin_amdgcn_s_setprio(2);
    waitvm<(T2MODE == 1 ? 6 : 0)>();
    LBAR();
    STAMP(16);
    tag = __builtin_amdgcn_readfirstlane(*(const LAS unsigned*)(L + L_SYNC + 32)) + 1u;
    {
      f32x4 rstd[2], mr[2];
#pragma unroll
      for (int s = 0; s < 2; ++s) {
        const f32x4 sm = *(const LAS f32x4*)(L + L_STAT + (32 * w + 16 * s + 4 * g) * 4) * (1.0f / 640.0f);
        const f32x4 sq = *(const LAS f32x4*)(L + L_STAT + 512 + (32 * w + 16 * s + 4 * g) * 4);
        const f32x2 v0 = pfma(lo2(sq), sp2(1.0f / 640.0f), pfma(-lo2(sm), lo2(sm), sp2(1e-5f)));
        const f32x2 v1 = pfma(hi2(sq), sp2(1.0f / 640.0f), pfma(-hi2(sm), hi2(sm), sp2(1e-5f)));
        rstd[s] = f32x4{__builtin_amdgcn_rsqf(fmaxf(v0.x, 1e-5f)), __builtin_amdgcn_rsqf(fmaxf(v0.y, 1e-5f)), __builtin_amdgcn_rsqf(fmaxf(v1.x, 1e-5f)), __builtin_amdgcn_rsqf(fmaxf(v1.y, 1e-5f))};
        mr[s] = -sm * rstd[s];
      }
      float cw[5];
#pragma unroll
      for (int t = 0; t < 5; ++t) cw[t] = *(const LAS float*)(L + L_CSUM + (16 * t + c) * 4);
#pragma unroll
      for (int t = 0; t < 5; ++t) {
        unsigned o[4];
#pragma unroll
        for (int s = 0; s < 2; ++s)
#pragma unroll
          for (int h = 0; h < 2; ++h) {
            const f32x2 a2 = h ? hi2(acc[s][t]) : lo2(acc[s][t]), r2_ = h ? hi2(rstd[s]) : lo2(rstd[s]), m2 = h ? hi2(mr[s]) : lo2(mr[s]);
            f32x2 y;
            if (t < 2) y = phi2(pfma(a2, r2_, pfma(m2, sp2(cw[t]), sp2(1e-6f))));
            else if (t == 2) {
              const f32x2 te = pfma(a2, r2_, pfma(m2, sp2(cw[t]), sp2(1e-6f))), ph = phi2(te), v = te - sp2(1e-6f);
              y = c < 8 ? ph : v;
            } else y = pfma(a2, r2_, m2 * sp2(cw[t]));
            o[2 * s + h] = pk2v(y);
          }
        *(LAS v4u*)(L + L_KVF + (w * 5 + t) * 1024 + l * 16) = v4u{o[0], o[1], o[2], o[3]};
      }
    }
    STAMP(17);
    LBAR();
    kv_product();
    LBAR();
    STAMP(19);
    if (w == 0) {
      v4u v[4];
#pragma unroll
      for (int i = 0; i < 4; ++i) v[i] = lds128(L, L_KVC + min(i * 64 + l, 209) * 16);
#pragma unroll
      for (int i = 0; i < 4; ++i) stg_sc1(rs, i * 64 + l < 210 ? WS_KVP + (p * 16 + r) * KVP_STRIDE + (i * 64 + l) * 16 : 0x7FFFFFF0u, v[i]);
      STAMP(20);
      DRAIN();
      if (l == 0) set_flag(ws, WS_FKV + p * 256 + r * 4, tag);
      STAMP(3);
    }
    __builtin_amdgcn_s_setprio(0);
    const int f0 = min(7 * c3, 101);
    const int u3 = min(r3 + 16 * max(w - 1, 0), 39), ks3 = u3 >> 1, mat = u3 & 1;
    const int ks4 = min(r2 + 32 * max(w - 1, 0), 53);
    const unsigned area = L_KVF + max(w - 1, 0) * 12288;
    if (w > 0) {
      const char* w3 = (const char*)(mat ? wu : wg);
#pragma unroll
      for (int i = 0; i < 7; ++i) {
        const int n = 64 * i + l, pos = n / 14, ch = n - pos * 14;
        const int feat = ks3 < 16 ? 80 * (ks3 >> 1) + 32 * (ks3 & 1) + pos : 160 * (ks3 - 16) + 64 + (pos & 15) + 80 * (pos >> 4);
        glds(feat * 3456 + (f0 * 16 + 8 * ch) * 2, w3, area + i * 1024);
      }
      if (w < 3) {
#pragma unroll
        for (int i = 0; i < 5; ++i) {
          const int n = 64 * i + l, pos = n / 10, ch = n - pos * 10, t = pos >> 4;
          const int sp = ks4 < 45 ? ks4 / 3 : ks4 == 45 ? 15 : 2 * (ks4 - 46) + t;
          const int lt = ks4 < 45 ? 2 * (ks4 % 3) + t : ks4 == 45 ? 4 + t : 6;
          const int row = 16 * (min(7 * sp, 101) + lt) + (pos & 15);
          glds(row * 1280 + (80 * cs + 8 * ch) * 2, wd, area + 7168 + i * 1024);
        }
      }
    }
    auto wo_pieces = [&](int i0, int i1) {
#pragma unroll 1
      for (int i = i0 + w; i < i1; i += 4) {
        const int n = 64 * i + l, pos = n / 10, ch2 = n - pos * 10;
        const int ks = pos >> 5, p5 = pos & 31;
        const int t = p5 >> 4, gg = (p5 >> 2) & 3, qq = p5 & 3;
        const int feat = ks < 16 ? 40 * ks + p5 : 40 * (4 * (ks - 16) + 2 * (gg >> 1) + t) + 32 + 4 * (gg & 1) + qq;
        glds(feat * 1280 + (80 * cs + 8 * ch2) * 2, wo, i * 1024);
      }
    };
    if (w > 0) {
      if (w < 3) waitvm<5>(); else waitvm<0>();
      {
        const int tb = area + (4 * g + q4) * 224 + 8 * pp;
        v4u fr[7];
#pragma unroll
        for (int t = 0; t < 7; ++t) fr[t] = ldtr(L, tb + t * 32, 16 * 224);
        const bool ok = r3 + 16 * (w - 1) < 40;
        const unsigned ob = ok ? WS_W3 + ((c3 * 20 + ks3) * 14 + mat * 7) * 1024 + l * 16 : 0x7FFF0000u;
        float csel0 = 0.f, csel1 = 0.f;
#pragma unroll
        for (int t = 0; t < 7; ++t) {
          stg_sc1(rs, ob + t * 1024, fr[t]);
          const f32x4 cs_ = mfma(ONES, fr[t], f32x4{0, 0, 0, 0});
          if (t < 4) csel0 = (g == t) ? cs_[0] : csel0; else csel1 = (g == t - 4) ? cs_[0] : csel1;
        }
        const unsigned cb = ok ? WS_CS + (c3 * 40 + u3) * 448 + l * 4 : 0x7FFF0000u;
        stg1_sc1(rs, cb, __float_as_uint(csel0));
        stg1_sc1(rs, l < 48 ? cb + 256 : 0x7FFF0000u, __float_as_uint(csel1));
      }
      if (w < 3) {
        waitvm<9>();
        const int tb = area + 7168 + (4 * g + q4) * 160 + 8 * pp;
        v4u fr[5];
#pragma unroll
        for (int t = 0; t < 5; ++t) fr[t] = ldtr(L, tb + t * 32, 16 * 160);
        const unsigned ob = (r2 + 32 * (w - 1) < 54) ? WS_W4 + (cs * 54 + ks4) * 5 * 1024 + l * 16 : 0x7FFF0000u;
#pragma unroll
        for (int t = 0; t < 5; ++t) stg_sc1(rs, ob + t * 1024, fr[t]);
      }
    }
    lds_spin(L, 4, 4);
    wo_pieces(0, 100);
    DRAIN();
    if (l == 0 && lds_add(L, 3) == 3) { set_flag(ws, WS_FC + c3 * 256 + r3 * 4, tag); set_flag(ws, WS_FD + cs * 256 + r2 * 4, tag); }
    STAMPW(23, 1);
  } else {
    const int tw = w - 4;
    unsigned vx[4], vw[3];
#pragma unroll
    for (int i = 0; i < 4; ++i) { const int tok = 8 * (4 * tw + i) + (l >> 3); vx[i] = tok * 1280 + (((l & 7) ^ (tok & 7)) << 4); }
    const char* xb = (const char*)x + (size_t)r * 128 * 1280;
    const unsigned lxw = 4 * tw * 1024, lkw = 16384 + 160 * tw * 16;
    auto pieceX = [&](auto ST, auto I) { constexpr int st = ST, i = I; gldsi<(st % 5) * S1 + i * 1024>(vx[i], xb + st * 128, lxw); };
    static_for<0, 4>([&](auto I) { pieceX(IC<0>{}, I); });
#pragma unroll
    for (int j = 0; j < 3; ++j) {
      const int n = min(160 * tw + 64 * j + l, 639), pos = n / 10, ch = n - pos * 10;
      const int col = (ch < 5) ? 640 + 40 * p + 8 * ch : 1280 + 40 * p + 8 * (ch - 5);
      const int k = (pos & ~12) | ((pos & 4) << 1) | ((pos & 8) >> 1);
      vw[j] = k * 3840 + col * 2;
    }
    auto pieceW = [&](auto ST, auto J) {
      constexpr int st = ST, j = J;
      if constexpr (j < 2) gldsi<(st % 5) * S1 + j * 1024>(vw[j], (const char*)wqkv + (size_t)st * 64 * 3840, lkw);
      else { if (l < 32) gldsi<(st % 5) * S1 + j * 1024>(vw[j], (const char*)wqkv + (size_t)st * 64 * 3840, lkw); }
    };
    auto piece = [&](auto ST, auto Q) { constexpr int q = Q; if constexpr (q < 4) pieceX(ST, Q); else pieceW(ST, IC<q - 4>{}); };
    static_for<0, 3>([&](auto J) { pieceW(IC<0>{}, J); });
    static_for<0, 7>([&](auto Q) { piece(IC<1>{}, Q); });
    auto pieceQ = [&](auto QS, auto J) {
      constexpr int qs = QS, j = J, npc = qs < 2 ? 6 : 3;
      const int n = 64 * (tw * npc + j) + l, pos = n / 6, ch = n - pos * 6;
      const int k = 256 * qs + ((pos & ~12) | ((pos & 4) << 1) | ((pos & 8) >> 1));
      if (ch < 5) gldsi<qs * S1 + j * 1024>(k * 3840 + (40 * p + 8 * ch) * 2, wqkv, tw * npc * 1024);
    };
    STAMPW(51, 4);
    v4u xs[20][2];
    f32x4 sq[2], sm[2], cs0, cs1;
    v4u ones_ = ONES;
    asm volatile("" : "+v"(ones_));
    {
      const int xw = tw * 32 * 128;
      int xbs[3][2], ebs[3][2];
#pragma unroll
      for (int q = 0; q < 3; ++q) { xbs[q][0] = xw + xo0 + 2 * q * S1; xbs[q][1] = xw + (xo0 ^ 64) + 2 * q * S1; ebs[q][0] = kvo + tw * 32 + 2 * q * S1; ebs[q][1] = kvo + 4 * 32 + 2 * q * S1; }
      asm volatile("" : "+v"(xbs[0][0]), "+v"(xbs[0][1]), "+v"(xbs[1][0]), "+v"(xbs[1][1]), "+v"(xbs[2][0]), "+v"(xbs[2][1]),
                        "+v"(ebs[0][0]), "+v"(ebs[0][1]), "+v"(ebs[1][0]), "+v"(ebs[1][1]), "+v"(ebs[2][0]), "+v"(ebs[2][1]));
      v4u e[2][2];
      auto rd1 = [&](auto KK, auto I) {
        constexpr int kk = KK, i = I, slot = (kk >> 1) % 5, ks = kk & 1;
        if constexpr (i < 2) xs[kk][i] = lds128(L, xbs[slot >> 1][ks] + (slot & 1) * S1 + i * 2048);
        else {
          constexpr int t = (i - 2) >> 1, h = i & 1;
          const v2u v = __builtin_bit_cast(v2u, __builtin_amdgcn_ds_read_tr16_b64_v4i16((LAS s16x4*)(L + ebs[slot >> 1][t] + (slot & 1) * S1 + ks * 32 * 160 + h * 8 * 160)));
          if constexpr (h == 0) { e[kk & 1][t].x = v.x; e[kk & 1][t].y = v.y; } else { e[kk & 1][t].z = v.x; e[kk & 1][t].w = v.y; }
        }
      };
      auto mma1 = [&](auto KK, auto I) {
        constexpr int kk = KK, i = I;
        if constexpr (i < 2) mfma_a<(kk == 0)>(sq[i], xs[kk][i], xs[kk][i]);
        else if constexpr (i < 4) mfma_a<(kk == 0)>(sm[i - 2], ones_, xs[kk][i - 2]);
        else if constexpr (i == 4) mfma_a<(kk == 0)>(cs0, ones_, e[kk & 1][0]);
        else mfma_a<(kk == 0)>(cs1, ones_, e[kk & 1][1]);
      };
      waitbar<7>();
      static_for<0, 6>([&](auto I) { rd1(IC<0>{}, I); });
      static_for<0, 10>([&](auto ST) {
        constexpr int st = ST;
        static_for<0, 6>([&](auto I) {
          constexpr int i = I;
          mma1(IC<2 * st>{}, I);
          rd1(IC<2 * st + 1>{}, I);
          if constexpr (st == 0) { piece(IC<2>{}, I); if constexpr (i == 5) piece(IC<2>{}, IC<6>{}); }
          if constexpr (st == 1) { piece(IC<4>{}, I); if constexpr (i == 5) piece(IC<4>{}, IC<6>{}); }
          SB();
        });
        if constexpr (st < 9) { if constexpr (st == 0) waitbar<7>(); else if constexpr (st <= 6) waitbar<14>(); else if constexpr (st == 7) waitbar<13>(); else waitbar<12>(); }
        static_for<0, 6>([&](auto I) {
          constexpr int i = I;
          mma1(IC<2 * st + 1>{}, I);
          if constexpr (st == 0) { piece(IC<3>{}, I); if constexpr (i == 5) piece(IC<3>{}, IC<6>{}); }
          if constexpr (st >= 1 && st <= 5) { piece(IC<st + 4>{}, I); if constexpr (i == 5) piece(IC<st + 4>{}, IC<6>{}); }
          if constexpr (st == 6 || st == 7) pieceQ(IC<st - 6>{}, I);
          if constexpr (st == 8 && i < 3) pieceQ(IC<2>{}, I);
          if constexpr (st < 9) rd1(IC<2 * st + 2>{}, I);
          SB();
        });
      });
      MFMA_FENCE();
    }
#pragma unroll
    for (int s = 0; s < 2; ++s) {
      const int e_ = c & 3;
      const float v = e_ == 0 ? sq[s][0] : e_ == 1 ? sq[s][1] : e_ == 2 ? sq[s][2] : sq[s][3];
      *(LAS float*)(L + (g == 0 ? L_STAT + (32 * tw + 16 * s + c) * 4 : L_SYNC + 512 + l * 4)) = sm[s][0];
      *(LAS float*)(L + (g == (c >> 2) ? L_STAT + 512 + (32 * tw + 16 * s + c) * 4 : L_SYNC + 768 + l * 4)) = v;
    }
    *(LAS float*)(L + (g == 0 ? L_CSUM + (16 * tw + c) * 4 : L_SYNC + 512 + l * 4)) = cs0[0];
    *(LAS float*)(L + (g == 0 ? L_CSUM + (64 + c) * 4 : L_SYNC + 512 + l * 4)) = cs1[0];
    LBAR();
    tag = __builtin_amdgcn_readfirstlane(*(const LAS unsigned*)(L + L_SYNC + 32)) + 1u;
    LBAR();
    kv_product();
    DRAIN();
    LBAR();
    f32x4 qacc[3][2], csq;
    {
      int qbs[3];
#pragma unroll
      for (int q = 0; q < 3; ++q) qbs[q] = q * S1 + wrow * 96 + 8 * pp;
      asm volatile("" : "+v"(qbs[0]), "+v"(qbs[1]), "+v"(qbs[2]));
      int cbs[3];
#pragma unroll
      for (int q = 0; q < 3; ++q) cbs[q] = qbs[q] + (tw < 3 ? tw : 2) * 32;
      asm volatile("" : "+v"(cbs[0]), "+v"(cbs[1]), "+v"(cbs[2]));
      v4u f[2][4];
      auto rd1 = [&](auto KK, auto I) {
        constexpr int kk = KK, i = I, t = i >> 1, h = i & 1;
        const int base = t < 3 ? qbs[kk >> 3] + t * 32 : cbs[kk >> 3];
        const v2u v = __builtin_bit_cast(v2u, __builtin_amdgcn_ds_read_tr16_b64_v4i16((LAS s16x4*)(L + base + (kk & 7) * 32 * 96 + h * 8 * 96)));
        if constexpr (h == 0) { f[kk & 1][t].x = v.x; f[kk & 1][t].y = v.y; } else { f[kk & 1][t].z = v.x; f[kk & 1][t].w = v.y; }
      };
      static_for<0, 8>([&](auto I) { rd1(IC<0>{}, I); });
      static_for<0, 20>([&](auto KK) {
        constexpr int kk = KK;
        static_for<0, 7>([&](auto I) {
          constexpr int i = I;
          if constexpr (i < 6) mfma_a<(kk == 0)>(qacc[i >> 1][i & 1], f[kk & 1][i >> 1], xs[kk][i & 1]);
          else mfma_a<(kk == 0)>(csq, ones_, f[kk & 1][3]);
          if constexpr (kk < 19) { rd1(IC<kk + 1>{}, I); if constexpr (i == 6) rd1(IC<kk + 1>{}, IC<7>{}); }
          SB();
        });
      });
      MFMA_FENCE();
    }
    *(LAS float*)(L + (g == 0 && tw < 3 ? L_CSUM + (80 + 16 * tw + c) * 4 : L_SYNC + 512 + l * 4)) = csq[0];
    asm volatile("s_waitcnt lgkmcnt(0)" ::: "memory");
    if (l == 0) lds_add(L, 4);
    lds_spin(L, 4, 4);
    v4u pqA[2], pqB[2];
#pragma unroll
    for (int s = 0; s < 2; ++s) {
      const float sm = *(const LAS float*)(L + L_STAT + (32 * tw + 16 * s + c) * 4);
      const float sq2 = *(const LAS float*)(L + L_STAT + 512 + (32 * tw + 16 * s + c) * 4);
      const float m = sm * (1.0f / 640.0f);
      const float rs_ = __builtin_amdgcn_rsqf(fmaxf(sq2 * (1.0f / 640.0f) - m * m + 1e-5f, 1e-5f));
      const float mr = -m * rs_;
      unsigned pq[6];
#pragma unroll
      for (int t = 0; t < 3; ++t) {
        const f32x4 cw = *(const LAS f32x4*)(L + L_CSUM + (80 + 16 * t + 4 * g) * 4);
        pq[2 * t] = pk2v(phi2(pfma(lo2(qacc[t][s]), sp2(rs_), pfma(lo2(cw), sp2(mr), sp2(1e-6f)))));
        pq[2 * t + 1] = pk2v(phi2(pfma(hi2(qacc[t][s]), sp2(rs_), pfma(hi2(cw), sp2(mr), sp2(1e-6f)))));
      }
      pqA[s] = v4u{pq[0], pq[1], pq[2], pq[3]};
      pqB[s] = v4u{g < 2 ? pq[4] : 0u, g < 2 ? pq[5] : 0u, 0u, 0u};
    }
    asm volatile("" : "+v"(pqA[0]), "+v"(pqA[1]), "+v"(pqB[0]), "+v"(pqB[1]));
    STAMPW(22, 4);
    if (w == 4) { wait16(ws, WS_FKV + p * 256, tag, l); if (l == 0) lds_add(L, 0); } else lds_spin(L, 0, 1);
    STAMPW(21, 4);
    {
      const int t = min(tid - 256, 209);
      v4u v[16];
#pragma unroll
      for (int i = 0; i < 16; ++i) v[i] = ldg_sc1(rs, WS_KVP + (p * 16 + i) * KVP_STRIDE + t * 16);
      f32x4 s = {0, 0, 0, 0};
#pragma unroll
      for (int i = 0; i < 16; ++i) s += __builtin_bit_cast(f32x4, v[i]);
      const v2u pk = {pk2v(lo2(s)), pk2v(hi2(s))};
      const int fo = t / 5, f = t < 200 ? 4 * (t - fo * 5) + (fo >= 20 ? 20 : 0) : 4 * (t - 200);
      const int kst = f >> 5, tt = (f >> 4) & 1, gg = (f >> 2) & 3;
      if (t < 200) *(LAS v2u*)(L + L_BD + ((fo >> 4) * 2 + kst) * 1024 + (gg * 16 + (fo & 15)) * 16 + tt * 8) = pk;
      else {
#pragma unroll
        for (int i = 0; i < 8; ++i) *(LAS v2u*)(L + L_BD + (6 + kst) * 1024 + (gg * 16 + 2 * i + (f >= 20)) * 16 + tt * 8) = pk;
      }
    }
    asm volatile("s_waitcnt lgkmcnt(0)" ::: "memory");
    if (l == 0) lds_add(L, 1);
    lds_spin(L, 1, 4);
    STAMPW(24, 4);
    v4u bd[8];
#pragma unroll
    for (int i = 0; i < 8; ++i) bd[i] = lds128(L, L_BD + i * 1024 + l * 16);
#pragma unroll
    for (int s = 0; s < 2; ++s) {
      f32x4 den = mfma(bd[6], pqA[s], f32x4{0, 0, 0, 0}); den = mfma(bd[7], pqB[s], den);
      f32x4 num[3];
#pragma unroll
      for (int ft = 0; ft < 3; ++ft) { num[ft] = mfma(bd[2 * ft], pqA[s], f32x4{0, 0, 0, 0}); num[ft] = mfma(bd[2 * ft + 1], pqB[s], num[ft]); }
      const float r0 = __builtin_amdgcn_rcpf(den[0] + 1e-6f), r1 = __builtin_amdgcn_rcpf(den[1] + 1e-6f);
      const float rm = g == 0 ? r0 : r1;
      const unsigned base = WS_O + (8 * r + 2 * tw + s) * 20 * 1024;
      stg_sc1(rs, base + p * 1024 + l * 16, v4u{pk2v(lo2(num[0]) * r0), pk2v(hi2(num[0]) * r0), pk2v(lo2(num[1]) * rm), pk2v(hi2(num[1]) * rm)});
      stg2_sc1(rs, g < 2 ? base + (16 + (p >> 2)) * 1024 + ((2 * ((p & 3) >> 1) + g) * 16 + c) * 16 + (p & 1) * 8 : 0x7FFFFFF0u,
               v2u{pk2v(lo2(num[2]) * r1), pk2v(hi2(num[2]) * r1)});
    }
    STAMPW(26, 4);
    DRAIN();
    STAMPW(27, 4);
    if (l == 0 && lds_add(L, 2) == 3) set_flag(ws, WS_FO + r * 256 + p * 4, tag);
  }
  LBAR();
  STAMP(5);

  const int th = w & 1, kq = w >> 1;
  const int sl = 2 * th + (kq & 1);
  const int tok = 16 * (4 * r2 + sl) + c;
  {
    v2u xr[3];
#pragma unroll
    for (int i = 0; i < 3; ++i) {
      const int T = kq < 2 ? (i == 2 ? 4 : i) : (i == 2 ? 2 : 2 + i);
      xr[i] = *(const v2u*)(x + (size_t)tok * 640 + 80 * cs + 16 * T + 4 * g);
    }
    v4u wf[5][5];
    const int wb = (5 * kq * 32 + 4 * g + q4) * 160 + 8 * pp;
#pragma unroll
    for (int k = 0; k < 5; ++k)
#pragma unroll
      for (int T = 0; T < 5; ++T) wf[k][T] = ldtr(L, wb + k * 32 * 160 + T * 32, 16 * 160);
#pragma unroll
    for (int k = 0; k < 5; ++k) asm volatile("" : "+v"(wf[k][0]), "+v"(wf[k][1]), "+v"(wf[k][2]), "+v"(wf[k][3]), "+v"(wf[k][4]));
    asm volatile("" : "+v"(xr[0]), "+v"(xr[1]), "+v"(xr[2]));
    STAMP(28);
    if (w == 0) wait16(ws, WS_FO + (r2 >> 1) * 256, tag, l);
    if (w == 1) wait16(ws, WS_FC + c3 * 256, tag, l);
    if (w == 2) wait32(ws, WS_FD + cs * 256, tag, l);
    LBAR();
    STAMP(6);
    if (b == 0 && tid == 0) set_flag(ws, WS_EPOCH, tag);
    v4u xf[5][2];
#pragma unroll
    for (int k = 0; k < 5; ++k)
#pragma unroll
      for (int s = 0; s < 2; ++s) xf[k][s] = ldg_sc1(rs, WS_O + ((4 * r2 + 2 * th + s) * 20 + 5 * kq + k) * 1024 + l * 16);
    f32x4 acc[5][2];
#pragma unroll
    for (int T = 0; T < 5; ++T) { acc[T][0] = f32x4{0, 0, 0, 0}; acc[T][1] = f32x4{0, 0, 0, 0}; }
    SB();
#pragma unroll
    for (int k = 0; k < 5; ++k)
#pragma unroll
      for (int T = 0; T < 5; ++T)
#pragma unroll
        for (int s = 0; s < 2; ++s) acc[T][s] = mfma(wf[k][T], xf[k][s], acc[T][s]);
    STAMP(7);
    f32x4 own[3];
    splitk_exchange(L, acc, own, th, kq, l);
    STAMP(29);
    const bool hi = kq >= 2;
    unsigned hp[3][2];
    f32x2 sm2 = {0.f, 0.f}, sq2 = {0.f, 0.f};
#pragma unroll
    for (int i = 0; i < 3; ++i) {
      hp[i][0] = pk2v(f32x2{bflo(xr[i].x), bfhi(xr[i].x)} + lo2(own[i]));
      hp[i][1] = pk2v(f32x2{bflo(xr[i].y), bfhi(xr[i].y)} + hi2(own[i]));
      if (i == 2 && hi) { hp[2][0] = 0u; hp[2][1] = 0u; }
      const f32x2 a0 = {bflo(hp[i][0]), bfhi(hp[i][0])}, a1 = {bflo(hp[i][1]), bfhi(hp[i][1])};
      sm2 += a0 + a1;
      sq2 = pfma(a0, a0, pfma(a1, a1, sq2));
    }
    float sm = sm2.x + sm2.y, sq = sq2.x + sq2.y;
    const unsigned hb = WS_H + (4 * r2 + sl) * 20 * 1024;
    stg_sc1(rs, hb + (2 * cs + (kq >> 1)) * 1024 + l * 16, v4u{hp[0][0], hp[0][1], hp[1][0], hp[1][1]});
    stg2_sc1(rs, hi ? 0x7FFFFFF0u : hb + (16 + (cs >> 1)) * 1024 + l * 16 + (cs & 1) * 8, v2u{hp[2][0], hp[2][1]});
    sm = rowsum4(sm); sq = rowsum4(sq);
    stg2_sc1(rs, g == 0 ? WS_HS + ((4 * r2 + sl) * 16 + cs * 2 + (kq >> 1)) * 128 + c * 8 : 0x7FFFFFF0u, v2u{__float_as_uint(sm), __float_as_uint(sq)});
    *(LAS v2u*)(L + L_HT + (sl * 5 + (hi ? 2 : 0)) * 512 + l * 8) = v2u{hp[0][0], hp[0][1]};
    *(LAS v2u*)(L + L_HT + (sl * 5 + (hi ? 3 : 1)) * 512 + l * 8) = v2u{hp[1][0], hp[1][1]};
    *(LAS v2u*)(L + (hi ? L_SYNC + 512 : L_HT + (sl * 5 + 4) * 512) + l * 8) = v2u{hp[2][0], hp[2][1]};
    STAMP(30);
    DRAIN();
    STAMP(31);
    LBAR();
    if (tid == 0) set_flag(ws, WS_FH + (r2 >> 1) * 256 + ((r2 & 1) * 8 + cs) * 4, tag);
    STAMP(8);
  }
  {
    if (w < 4) p3_body<4, 0, false>(L, ws, rs, r3, c3, cs | ((r2 & 3) << 8), w, l, tag);
    else       p3_body<3, 4, true>(L, ws, rs, r3, c3, cs | ((r2 & 3) << 8), w, l, tag);
    STAMP(36);
    DRAIN();
    STAMP(37);
    LBAR();
    if (tid == 0) set_flag(ws, WS_FA + r3 * 256 + c3 * 4, tag);
    STAMP(12);
  }
  {
    f32x4 acc[5][2];
    if (th == 0) p4_main<0>(L, ws, r2 >> 1, r2, cs, kq, w, l, tag, acc); else p4_main<1>(L, ws, r2 >> 1, r2, cs, kq, w, l, tag, acc);
    LBAR();
    STAMP(14);
    f32x4 own[3];
    splitk_exchange(L, acc, own, th, kq, l);
    const bool hi = kq >= 2;
    u16* ob = out + (size_t)tok * 640 + 80 * cs + 4 * g;
#pragma unroll
    for (int i = 0; i < 3; ++i) {
      const int T = i == 2 ? 4 : (hi ? 2 : 0) + i;
      const v2u hh = *(const LAS v2u*)(L + L_HT + (sl * 5 + T) * 512 + l * 8);
      const v2u o = {pk2v(f32x2{bflo(hh.x), bfhi(hh.x)} + lo2(own[i])), pk2v(f32x2{bflo(hh.y), bfhi(hh.y)} + hi2(own[i]))};
      if (i < 2 || !hi) *(v2u*)(ob + 16 * T) = o;
    }
    STAMP(15);
  }
}

extern "C" void kernel_launch(void* const* d_in, const int* in_sizes, int n_in, void* d_out, int out_size, void* d_ws, size_t ws_size,
                              hipStream_t stream) {
  (void)in_sizes; (void)n_in; (void)out_size; (void)ws_size;
  (void)hipFuncSetAttribute((const void*)fused_block, hipFuncAttributeMaxDynamicSharedMemorySize, LDS_BYTES);
  fused_block<<<256, 512, LDS_BYTES, stream>>>((const u16*)d_in[0], (const u16*)d_in[1], (const u16*)d_in[2], (const u16*)d_in[4],
                                                (const u16*)d_in[5], (const u16*)d_in[6], (u16*)d_out, (char*)d_ws);
}
```

```cpp
#include <hip/hip_runtime.h>
#include <hip/hip_bf16.h>
#include <stdint.h>
#include <type_traits>

#define LAS __attribute__((address_space(3)))
#define GAS __attribute__((address_space(1)))
typedef __bf16 bf16x8 __attribute__((ext_vector_type(8)));
typedef __bf16 bf16x2 __attribute__((ext_vector_type(2)));
typedef short s16x4 __attribute__((ext_vector_type(4)));
typedef float f32x4 __attribute__((ext_vector_type(4)));
typedef float f32x2 __attribute__((ext_vector_type(2)));
typedef unsigned v4u __attribute__((ext_vector_type(4)));
typedef unsigned v2u __attribute__((ext_vector_type(2)));
typedef GAS unsigned gu32;
typedef unsigned short u16;
#define RLX_AGENT __ATOMIC_RELAXED, __HIP_MEMORY_SCOPE_AGENT

constexpr unsigned WS_EPOCH = 0, WS_TMO = 256;
constexpr unsigned WS_FKV = 4096, WS_FO = 8192, WS_FH = 12288, WS_FA = 16384, WS_FC = 20480, WS_FD = 24576, WS_FW = 28672;
constexpr unsigned WS_KVP = 32768, KVP_STRIDE = 3456;
constexpr unsigned WS_O = WS_KVP + 256 * KVP_STRIDE;
constexpr unsigned WS_H = WS_O + 2621440;
constexpr unsigned WS_HS = WS_H + 2621440;
constexpr unsigned WS_ACT = WS_HS + 262144;
constexpr unsigned WS_CS = WS_ACT + 7077888;
constexpr unsigned WS_W3 = WS_CS + 16 * 40 * 448;
constexpr unsigned WS_W4 = WS_W3 + 16 * 280 * 1024;
constexpr unsigned WS_W2 = WS_W4 + 8 * 270 * 1024;
constexpr unsigned WS_STAMP = WS_W2 + 8 * 100 * 1024;
constexpr unsigned WS_END = WS_STAMP + 256 * 64 * 8;

constexpr int LDS_BYTES = 163840;
constexpr int L_KVF = 102400;
constexpr int L_KVC = 135168;
constexpr int L_BD = 138752;
constexpr int L_STAT = 146944;
constexpr int L_CSUM = 147968;
constexpr int L_SIDE = 148480;
constexpr int L_CS3 = 151168;
constexpr int L_SYNC = 152064;
constexpr int L_HT = 153600;
constexpr int L_RED = 0;
constexpr int L_X3 = 86016;

#define STAMPS 0
#define T2MODE 1
#define WOMODE 1
#define T1MODE 4
#define T2POS 0
#define T1SLEEP 16
#define T0KB 52

__device__ __forceinline__ void glds(unsigned voff, const void* sbase, unsigned ldsaddr) {
  asm volatile("s_mov_b32 m0, %2\n\ts_nop 0\n\tglobal_load_lds_dwordx4 %0, %1" ::"v"(voff), "s"(sbase), "s"(ldsaddr) : "memory");
}
__device__ __forceinline__ void glds2_sc1(unsigned voff, const void* sbase, unsigned ldsaddr) {
  asm volatile("s_mov_b32 m0, %2\n\ts_nop 0\n\tglobal_load_lds_dwordx4 %0, %1 sc1\n\tglobal_load_lds_dwordx4 %0, %1 offset:1024 sc1" ::"v"(voff), "s"(sbase), "s"(ldsaddr) : "memory");
}
template <int IMM> __device__ __forceinline__ void gldsi(unsigned voff, const void* sbase, unsigned lbase) {
  asm volatile("s_add_u32 m0, %2, %3\n\ts_nop 0\n\tglobal_load_lds_dwordx4 %0, %1" ::"v"(voff), "s"(sbase), "s"(lbase), "i"(IMM) : "memory", "scc");
}
template <int IMM> __device__ __forceinline__ void glds2i_sc1(unsigned voff, const void* sbase, unsigned lbase) {
  asm volatile("s_add_u32 m0, %2, %3\n\ts_nop 0\n\tglobal_load_lds_dwordx4 %0, %1 sc1\n\tglobal_load_lds_dwordx4 %0, %1 offset:1024 sc1" ::"v"(voff), "s"(sbase), "s"(lbase), "i"(IMM) : "memory", "scc");
}
template <int N, int IMM> __device__ __forceinline__ void gldsn_sc1(unsigned voff, const void* sbase, unsigned lbase) {
  static_assert(N >= 1 && N <= 4, "");
  if constexpr (N == 1) asm volatile("s_add_u32 m0, %2, %3\n\ts_nop 0\n\tglobal_load_lds_dwordx4 %0, %1 sc1" ::"v"(voff), "s"(sbase), "s"(lbase), "i"(IMM) : "memory", "scc");
  if constexpr (N == 2) asm volatile("s_add_u32 m0, %2, %3\n\ts_nop 0\n\tglobal_load_lds_dwordx4 %0, %1 sc1\n\tglobal_load_lds_dwordx4 %0, %1 offset:1024 sc1" ::"v"(voff), "s"(sbase), "s"(lbase), "i"(IMM) : "memory", "scc");
  if constexpr (N == 3) asm volatile("s_add_u32 m0, %2, %3\n\ts_nop 0\n\tglobal_load_lds_dwordx4 %0, %1 sc1\n\tglobal_load_lds_dwordx4 %0, %1 offset:1024 sc1\n\tglobal_load_lds_dwordx4 %0, %1 offset:2048 sc1" ::"v"(voff), "s"(sbase), "s"(lbase), "i"(IMM) : "memory", "scc");
  if constexpr (N == 4) asm volatile("s_add_u32 m0, %2, %3\n\ts_nop 0\n\tglobal_load_lds_dwordx4 %0, %1 sc1\n\tglobal_load_lds_dwordx4 %0, %1 offset:1024 sc1\n\tglobal_load_lds_dwordx4 %0, %1 offset:2048 sc1\n\tglobal_load_lds_dwordx4 %0, %1 offset:3072 sc1" ::"v"(voff), "s"(sbase), "s"(lbase), "i"(IMM) : "memory", "scc");
}
template <int IMM, int OFF> __device__ __forceinline__ void gldso_sc1(unsigned voff, const void* sbase, unsigned lbase) {
  asm volatile("s_add_u32 m0, %2, %3\n\ts_nop 0\n\tglobal_load_lds_dwordx4 %0, %1 offset:%4 sc1" ::"v"(voff), "s"(sbase), "s"(lbase), "i"(IMM), "i"(OFF) : "memory", "scc");
}
__device__ __forceinline__ void ldg_asm_sc1(v4u& d, unsigned voff, const void* sbase) {
  asm volatile("global_load_dwordx4 %0, %1, %2 sc1" : "=v"(d) : "v"(voff), "s"(sbase) : "memory");
}
__device__ __forceinline__ void ldg2_asm_sc1(v2u& d, unsigned voff, const void* sbase) {
  asm volatile("global_load_dwordx2 %0, %1, %2 sc1" : "=v"(d) : "v"(voff), "s"(sbase) : "memory");
}
template <int N> __device__ __forceinline__ void wait_for(v4u& a, v4u& b) { asm volatile("s_waitcnt vmcnt(%2)" : "+v"(a), "+v"(b) : "i"(N) : "memory"); }
template <int N> __device__ __forceinline__ void waitvm() { asm volatile("s_waitcnt vmcnt(%0)" ::"i"(N) : "memory"); }
__device__ __forceinline__ f32x2 pfma(f32x2 a, f32x2 b, f32x2 c) { return __builtin_elementwise_fma(a, b, c); }
__device__ __forceinline__ f32x2 sp2(float a) { return f32x2{a, a}; }
__device__ __forceinline__ f32x2 lo2(f32x4 v) { return __builtin_shufflevector(v, v, 0, 1); }
__device__ __forceinline__ f32x2 hi2(f32x4 v) { return __builtin_shufflevector(v, v, 2, 3); }
__device__ __forceinline__ unsigned pk2v(f32x2 v) { return __builtin_bit_cast(unsigned, __builtin_convertvector(v, bf16x2)); }
__device__ __forceinline__ f32x2 phi2(f32x2 te) {
  const f32x2 y = pfma(te, sp2(1.44269504088896f), sp2(-1.44269504088896e-6f));
  const f32x2 e = {__builtin_amdgcn_fmed3f(__builtin_amdgcn_exp2f(y.x), 0.f, 1.f), __builtin_amdgcn_fmed3f(__builtin_amdgcn_exp2f(y.y), 0.f, 1.f)};
  const f32x2 m = {fmaxf(te.x, 1e-6f), fmaxf(te.y, 1e-6f)};
  return m + e;
}
__device__ __forceinline__ unsigned geglu2(f32x2 ag, f32x2 au, f32x2 cg, f32x2 cu, float rstd, float mr) {
  const f32x2 gv = pfma(ag, sp2(rstd), cg * sp2(mr)), uv = pfma(au, sp2(rstd), cu * sp2(mr));
  const f32x2 z = gv * pfma(gv * gv, sp2(-0.1029432397f), sp2(-2.302208198f));
  const f32x2 d = {1.0f + __builtin_amdgcn_exp2f(z.x), 1.0f + __builtin_amdgcn_exp2f(z.y)};
  const f32x2 rc = {__builtin_amdgcn_rcpf(d.x), __builtin_amdgcn_rcpf(d.y)};
  return pk2v(gv * uv * rc);
}
template <int V> using IC = std::integral_constant<int, V>;
template <int I, int N, class F> __device__ __forceinline__ void static_for(F&& f) {
  if constexpr (I < N) { f(std::integral_constant<int, I>{}); static_for<I + 1, N>(f); }
}
#define LBAR() asm volatile("s_waitcnt lgkmcnt(0)\n\ts_barrier" ::: "memory")
#define DRAIN() asm volatile("s_waitcnt vmcnt(0)" ::: "memory")
#define SB() __builtin_amdgcn_sched_barrier(0)
template <int N> __device__ __forceinline__ void waitbar() { asm volatile("s_waitcnt vmcnt(%0) lgkmcnt(0)\n\ts_barrier" ::"i"(N) : "memory"); }

__device__ __forceinline__ unsigned pk2(float a, float b) {
  bf16x2 v = {(__bf16)a, (__bf16)b};
  return __builtin_bit_cast(unsigned, v);
}
__device__ __forceinline__ float bflo(unsigned u) { return __uint_as_float(u << 16); }
__device__ __forceinline__ float bfhi(unsigned u) { return __uint_as_float(u & 0xffff0000u); }
__device__ __forceinline__ f32x4 mfma(v4u a, v4u b, f32x4 c) {
  return __builtin_amdgcn_mfma_f32_16x16x32_bf16(__builtin_bit_cast(bf16x8, a), __builtin_bit_cast(bf16x8, b), c, 0, 0, 0);
}
__device__ __forceinline__ void mfma_acc(f32x4& acc, v4u a, v4u b) {
  asm volatile("v_mfma_f32_16x16x32_bf16 %0, %1, %2, %0" : "+v"(acc) : "v"(a), "v"(b));
}
__device__ __forceinline__ void mfma_first(f32x4& acc, v4u a, v4u b) {
  asm volatile("v_mfma_f32_16x16x32_bf16 %0, %1, %2, 0" : "=&v"(acc) : "v"(a), "v"(b));
}
template <bool FIRST> __device__ __forceinline__ void mfma_a(f32x4& acc, v4u a, v4u b) { if constexpr (FIRST) mfma_first(acc, a, b); else mfma_acc(acc, a, b); }
#define MFMA_FENCE() asm volatile("s_nop 7\n\ts_nop 1" ::: "memory")
__device__ __forceinline__ v4u lds128(LAS char* L, int off) { return *(const LAS v4u*)(L + off); }
__device__ __forceinline__ v4u ldtr(LAS char* L, int off, int d) {
  s16x4 a = __builtin_amdgcn_ds_read_tr16_b64_v4i16((LAS s16x4*)(L + off));
  s16x4 b = __builtin_amdgcn_ds_read_tr16_b64_v4i16((LAS s16x4*)(L + off + d));
  v2u x = __builtin_bit_cast(v2u, a), y = __builtin_bit_cast(v2u, b);
  return v4u{x.x, x.y, y.x, y.y};
}
__device__ __forceinline__ float phi_eps(float te) {
  const float e = __builtin_amdgcn_fmed3f(__builtin_amdgcn_exp2f((te - 1e-6f) * 1.44269504088896f), 0.f, 1.f);
  return fmaxf(te, 1e-6f) + e;
}
__device__ __forceinline__ float rowsum4(float v) {
  auto a = __builtin_amdgcn_permlane16_swap(__float_as_uint(v), __float_as_uint(v), false, false);
  const float u = __uint_as_float(a[0]) + __uint_as_float(a[1]);
  auto b = __builtin_amdgcn_permlane32_swap(__float_as_uint(u), __float_as_uint(u), false, false);
  return __uint_as_float(b[0]) + __uint_as_float(b[1]);
}
__device__ __forceinline__ v4u ldg_sc1(__amdgpu_buffer_rsrc_t rs, unsigned off) { return __builtin_amdgcn_raw_buffer_load_b128(rs, off, 0, 16); }
__device__ __forceinline__ v2u ldg2_sc1(__amdgpu_buffer_rsrc_t rs, unsigned off) { return __builtin_amdgcn_raw_buffer_load_b64(rs, off, 0, 16); }
__device__ __forceinline__ unsigned ldg1_sc1(__amdgpu_buffer_rsrc_t rs, unsigned off) { return __builtin_amdgcn_raw_buffer_load_b32(rs, off, 0, 16); }
__device__ __forceinline__ void stg_sc1(__amdgpu_buffer_rsrc_t rs, unsigned off, v4u v) { __builtin_amdgcn_raw_buffer_store_b128(v, rs, off, 0, 16); }
__device__ __forceinline__ void stg2_sc1(__amdgpu_buffer_rsrc_t rs, unsigned off, v2u v) { __builtin_amdgcn_raw_buffer_store_b64(v, rs, off, 0, 16); }
__device__ __forceinline__ void stg1_sc1(__amdgpu_buffer_rsrc_t rs, unsigned off, unsigned v) { __builtin_amdgcn_raw_buffer_store_b32(v, rs, off, 0, 16); }

__device__ __forceinline__ void wait16(char* ws, unsigned off, unsigned tag, int lane) {
  gu32* f = (gu32*)(ws + off) + (lane & 15);
  for (unsigned sp = 0; sp < (1u << 15); ++sp) {
    unsigned v = __hip_atomic_load(f, RLX_AGENT);
    if (__all(v == tag)) return;
    __builtin_amdgcn_s_sleep(1);
  }
  if (lane == 0) __hip_atomic_store((gu32*)(ws + WS_TMO), 1u, RLX_AGENT);
}
__device__ __forceinline__ void wait32(char* ws, unsigned off, unsigned tag, int lane) {
  gu32* f = (gu32*)(ws + off) + (lane & 31);
  for (unsigned sp = 0; sp < (1u << 15); ++sp) {
    unsigned v = __hip_atomic_load(f, RLX_AGENT);
    if (__all(v == tag)) return;
    __builtin_amdgcn_s_sleep(1);
  }
  if (lane == 0) __hip_atomic_store((gu32*)(ws + WS_TMO), 1u, RLX_AGENT);
}
typedef LAS unsigned lu32;
__device__ __forceinline__ unsigned lds_add(LAS char* L, int word) { return __hip_atomic_fetch_add((lu32*)(L + L_SYNC) + word, 1u, __ATOMIC_RELAXED, __HIP_MEMORY_SCOPE_WORKGROUP); }
__device__ __forceinline__ void lds_spin(LAS char* L, int word, unsigned want) {
  for (unsigned sp = 0; sp < (1u << 20); ++sp) {
    if (__builtin_amdgcn_readfirstlane(__hip_atomic_load((lu32*)(L + L_SYNC) + word, __ATOMIC_RELAXED, __HIP_MEMORY_SCOPE_WORKGROUP)) >= want) break;
  }
  asm volatile("" ::: "memory");
}
__device__ __forceinline__ void group_sync(LAS char* L, int word, int l) {
  asm volatile("s_waitcnt lgkmcnt(0)" ::: "memory");
  if (l == 0) lds_add(L, word);
  lds_spin(L, word, 4);
}
__device__ __forceinline__ void touch(unsigned voff, const void* sbase) {
  asm volatile("s_mov_b32 m0, %2\n\ts_nop 0\n\tglobal_load_lds_dword %0, %1" ::"v"(voff), "s"(sbase), "s"(L_SYNC + 64) : "memory");
}
__device__ __forceinline__ void touch64(const void* addr) {
  asm volatile("s_mov_b32 m0, %1\n\ts_nop 0\n\tglobal_load_lds_dword %0, off" ::"v"(addr), "s"(L_SYNC + 64) : "memory");
}
__device__ __forceinline__ void set_flag(char* ws, unsigned off, unsigned tag) { __hip_atomic_store((gu32*)(ws + off), tag, RLX_AGENT); }
#define STAMPW(i, wv) do {} while (0)
#define STAMP(i) STAMPW(i, 0)

__device__ __forceinline__ void splitk_exchange(LAS char* L, f32x4 (&acc)[5][2], f32x4 (&own)[3], int th, int kq, int l) {
  const int wb = L_RED + (th * 40 + kq) * 1024 + l * 16;
#pragma unroll
  for (int s = 0; s < 2; ++s)
#pragma unroll
    for (int T = 0; T < 5; ++T) *(LAS f32x4*)(L + wb + (s * 5 + T) * 4096) = acc[T][s];
  LBAR();
  const int rb = L_RED + (th * 2 + (kq & 1)) * 5 * 4096 + l * 16, t0 = rb + (kq >> 1) * 8192;
  f32x4 v[3][4];
#pragma unroll
  for (int k = 0; k < 4; ++k) {
    v[0][k] = *(const LAS f32x4*)(L + t0 + k * 1024);
    v[1][k] = *(const LAS f32x4*)(L + t0 + 4096 + k * 1024);
    v[2][k] = *(const LAS f32x4*)(L + rb + 4 * 4096 + k * 1024);
  }
#pragma unroll
  for (int i = 0; i < 3; ++i) own[i] = (v[i][0] + v[i][1]) + (v[i][2] + v[i][3]);
}

#define P4XW 4
constexpr int P4_XW = P4XW;
constexpr int P4_PRE = 4;
#define P4EARLY 10
constexpr int P4_EARLY = P4EARLY;
constexpr int p4_g(int it, int n) { return it == 4 ? n : it == 6 ? 2 * n : 0; }
constexpr int p4_x(int it) { return it + P4_XW < 14 ? 2 : 0; }
constexpr int p4_xwait(int st, int n) {
  int y = 0;
  if (st < P4_XW) { y = 2 * (P4_XW - 1 - st); for (int j = 0; j < st; ++j) y += p4_x(j) + p4_g(j, n); }
  else { y = p4_g(st - P4_XW, n); for (int j = st - P4_XW + 1; j < st; ++j) y += p4_x(j) + p4_g(j, n); }
  return y;
}
constexpr int p4_wwait(int st, int n) {
  int y = st == 10 ? n : 0;
  for (int j = 7; j < st; ++j) y += p4_x(j);
  return y;
}
template <int TH, int stg> __device__ __forceinline__ void p4_issue(char* ws, int cs, int kq, int l) {
  constexpr int N = TH == 0 ? 3 : 2;
  const int kb = kq < 2 ? 14 * kq : 28 + 13 * (kq - 2);
  gldsn_sc1<N, (stg % 7) * 20480>(l * 16, ws + WS_W4 + (size_t)cs * 270 * 1024 + TH * 3072 + (size_t)min(kb + stg, 53) * 5120, kq * 5120 + TH * 3072);
}
template <int J> __device__ __forceinline__ void p4_pre_piece(const char* src, unsigned l16, int th, unsigned lbase) {
  constexpr int q0 = 2 * J, q1 = (2 * J + 1 < 35) ? 2 * J + 1 : 34;
  constexpr int f0 = (q0 / 5) * 20480 + (q0 % 5) * 1024, f1 = (q1 / 5) * 20480 + (q1 % 5) * 1024;
  asm volatile("s_mov_b32 m0, %2\n\ts_nop 0\n\tglobal_load_lds_dwordx4 %0, %1 sc1" ::"v"(l16), "s"(src + (th ? q1 : q0) * 1024), "s"(lbase + (th ? f1 : f0)) : "memory");
}
template <int TH>
__device__ __forceinline__ void p4_main(LAS char* L, char* ws, int r, int r2, int cs, int kq, int w, int l, unsigned tag, f32x4 (&acc)[5][2]) {
  constexpr int N = TH == 0 ? 3 : 2;
  const int kb = kq < 2 ? 14 * kq : 28 + 13 * (kq - 2);
  const int nk = kq < 2 ? 14 : 13;
  const unsigned l16 = l * 16;
  auto issue = [&](auto STG) { p4_issue<TH, STG>(ws, cs, kq, l); };
  int wbs[3];
  wbs[0] = kq * 5120 + l16; wbs[1] = wbs[0] + 3 * 20480; wbs[2] = wbs[0] + 6 * 20480;
  asm volatile("" : "+v"(wbs[0]), "+v"(wbs[1]), "+v"(wbs[2]));
  {
    const char* w4src = ws + WS_W4 + (size_t)cs * 270 * 1024 + (size_t)kb * 5120;
    static_for<P4_EARLY, 18>([&](auto J) { p4_pre_piece<J>(w4src, l16, TH, kq * 5120); });
    static_assert(P4_EARLY >= 10, "");
  }
  v4u wp[P4_PRE][5];
  static_for<0, P4_PRE>([&](auto ST) { static_for<0, 5>([&](auto I) { wp[ST][I] = lds128(L, wbs[ST / 3] + (ST % 3) * 20480 + I * 1024); }); });
  LBAR();
  static_for<7, 7 + P4_PRE>([&](auto ST) { issue(ST); });
  STAMP(38);
  if (w == 0) wait16(ws, WS_FA + r * 256, tag, l);
  STAMP(39);
  waitbar<P4_PRE * N>();
  STAMP(13);
  v4u xf[14][2];
  const char* xa = ws + WS_ACT + (size_t)(4 * r2 + 2 * TH) * 54 * 1024;
  auto ldx = [&](auto K, auto S) {
    constexpr int k = K, s = S;
    ldg_asm_sc1(xf[k][s], l16, xa + (size_t)(s * 54 + min(kb + k, 53)) * 1024);
  };
  static_for<0, P4_XW>([&](auto K) { ldx(K, IC<0>{}); ldx(K, IC<1>{}); });
  v4u wf[2][5];
  auto rd1 = [&](auto STG, auto I) {
    constexpr int stg = STG, T = I, sl7 = stg % 7;
    wf[stg & 1][T] = lds128(L, wbs[sl7 / 3] + (sl7 % 3) * 20480 + T * 1024);
  };
  static_for<0, 14>([&](auto ST) {
    constexpr int st = ST;
    if constexpr (st == 4 || st == 6) LBAR();
    if constexpr (st == 10 || st == 12) waitbar<p4_wwait(st, N)>();
    if constexpr (st == 1) STAMP(40);
    if constexpr (st == 4) STAMP(41);
    if constexpr (st == 7) STAMP(42);
    if constexpr (st == 10) STAMP(43);
    if constexpr (st == 13) STAMP(44);
    wait_for<p4_xwait(st, N)>(xf[st][0], xf[st][1]);
    SB();
    if (st < 13 || st < nk) {
      static_for<0, 10>([&](auto I) {
        constexpr int i = I;
        if constexpr (st < P4_PRE) mfma_a<(st == 0)>(acc[i >> 1][i & 1], wp[st][i >> 1], xf[st][i & 1]);
        else mfma_a<false>(acc[i >> 1][i & 1], wf[st & 1][i >> 1], xf[st][i & 1]);
        if constexpr (st + P4_XW < 14 && (i == 0 || i == 2)) ldx(IC<st + P4_XW>{}, IC<(i >> 1)>{});
        if constexpr (st == 4 && i == 4) issue(IC<11>{});
        if constexpr (st == 6 && (i == 4 || i == 7)) issue(IC<12 + (i == 7)>{});
        if constexpr (st >= P4_PRE - 1 && st < 13 && i >= 2 && i < 7) rd1(IC<st + 1>{}, IC<i - 2>{});
        SB();
      });
    }
  });
  MFMA_FENCE();
}

template <int NT> struct Frag3 { v4u x[2], g[NT], u[NT]; };

template <int NT, int TILE0, bool LOADER>
__device__ __forceinline__ void p3_body(LAS char* L, char* ws, __amdgpu_buffer_rsrc_t rs, int r, int p, int cs, int w, int l, unsigned tag) {
  const int c = l & 15, g = l >> 4, tg = w & 3;
  const unsigned l16 = l * 16;
  constexpr int NWP = LOADER ? 3 : 4, NP = LOADER ? 7 : 4;
  const int wp0 = LOADER ? 16 + 3 * tg : 4 * tg;
  const char* wsrc = ws + WS_W3 + (size_t)p * 280 * 1024 + wp0 * 1024;
  const char* hx = ws + WS_H + (size_t)(8 * r + 2 * tg) * 20 * 1024;
  const unsigned lw = wp0 * 1024, lx = L_X3 + tg * 4096;
  auto piece = [&](auto STG, auto Q) {
    constexpr int stg = STG, q = Q, slot = stg % 3;
    if constexpr (q < NWP) gldso_sc1<slot * 28672, q * 1024>(l16, wsrc + stg * 28672, lw);
    else gldso_sc1<slot * 16384 + ((q - NWP) >> 1) * 2048, ((q - NWP) & 1) * 1024>(l16, hx + stg * 2048 + ((q - NWP) >> 1) * 20480, lx);
  };
  static_for<0, 3>([&](auto ST) { static_for<0, NWP>([&](auto Q) { piece(ST, Q); }); });
  if constexpr (!LOADER) {
    const int ct = min(w * 64 + l, 223), cm = ct >= 112;
    unsigned csv[20];
#pragma unroll
    for (int i = 0; i < 20; ++i) csv[i] = ldg1_sc1(rs, WS_CS + (p * 40 + 2 * i + cm) * 448 + (ct - 112 * cm) * 4);
    float s = 0.f;
#pragma unroll
    for (int i = 0; i < 20; ++i) s += __uint_as_float(csv[i]);
    *(LAS float*)(L + (w * 64 + l < 224 ? L_CS3 + ct * 4 : L_SYNC + 512)) = s;
  }
  STAMP(32);
  if (w == 7) { wait16(ws, WS_FH + r * 256, tag, l); STAMPW(33, 7); }
  DRAIN();
  LBAR();
  STAMP(10);
  if constexpr (LOADER) static_for<0, 3>([&](auto ST) { static_for<NWP, NP>([&](auto Q) { piece(ST, Q); }); });
  v2u stp[2][4];
#pragma unroll
  for (int s = 0; s < 2; ++s)
#pragma unroll
    for (int i = 0; i < 4; ++i) ldg2_asm_sc1(stp[s][i], (4 * g + i) * 128 + c * 8, ws + WS_HS + (8 * r + 2 * tg + s) * 16 * 128);

  f32x4 ag[NT][2], au[NT][2];
  float rstd[2], mr[2];
  const int xo = L_X3 + tg * 4096 + l16;
  int wb0 = TILE0 * 1024 + l16, wb2 = wb0 + 2 * 28672;
  asm volatile("" : "+v"(wb0), "+v"(wb2));
  auto rd1 = [&](Frag3<NT>& f, auto SLOT, auto KS, auto I) {
    constexpr int slot = SLOT, ks = KS, i = I;
    if constexpr (i < 2) f.x[i] = lds128(L, xo + slot * 16384 + ks * 1024 + i * 2048);
    else {
      constexpr int j = i - 2, t = j >> 1, u = j & 1, off = ks * 14336 + u * 7168 + t * 1024;
      const v4u v = slot < 2 ? lds128(L, wb0 + slot * 28672 + off) : lds128(L, wb2 + off);
      if constexpr (u == 0) f.g[t] = v; else f.u[t] = v;
    }
  };
  auto mma1 = [&](const Frag3<NT>& f, auto I, auto FIRST) {
    constexpr int i = I, t = i >> 2, u = (i >> 1) & 1, s = i & 1;
    if constexpr (u == 0) mfma_a<FIRST>(ag[t][s], f.g[t], f.x[s]); else mfma_a<FIRST>(au[t][s], f.u[t], f.x[s]);
  };
  constexpr int NM = 4 * NT, NR = 2 + 2 * NT;
  Frag3<NT> A, B;
  if constexpr (LOADER) waitbar<8 + 8>(); else LBAR();
  STAMP(35);
  static_for<0, NR>([&](auto I) { rd1(A, IC<0>{}, IC<0>{}, I); });
  static_for<0, 10>([&](auto ST) {
    constexpr int st = ST, slot = st % 3, nslot = (st + 1) % 3;
    static_for<0, NM>([&](auto I) {
      constexpr int i = I;
      mma1(A, I, IC<(st == 0)>{});
      if constexpr (i < NR) rd1(B, IC<slot>{}, IC<1>{}, I);
      SB();
    });
    if constexpr (st < 9) {
      if constexpr (st == 0) { if constexpr (LOADER) waitbar<4 + 8>(); else LBAR(); }
      else if constexpr (st == 1) { if constexpr (LOADER) waitbar<NP>(); else LBAR(); }
      else if constexpr (st < 8) waitbar<NP>(); else waitbar<0>();
    }
    if constexpr (st == 4) {
      asm volatile("" : "+v"(stp[0][0]), "+v"(stp[0][1]), "+v"(stp[0][2]), "+v"(stp[0][3]), "+v"(stp[1][0]), "+v"(stp[1][1]), "+v"(stp[1][2]), "+v"(stp[1][3]));
#pragma unroll
      for (int s = 0; s < 2; ++s) {
        float sm = 0.f, sq = 0.f;
#pragma unroll
        for (int i = 0; i < 4; ++i) { sm += __uint_as_float(stp[s][i].x); sq += __uint_as_float(stp[s][i].y); }
        sm = rowsum4(sm); sq = rowsum4(sq);
        const float mean = sm * (1.0f / 640.0f);
        rstd[s] = __builtin_amdgcn_rsqf(fmaxf(sq * (1.0f / 640.0f) - mean * mean, 0.f) + 1e-5f);
        mr[s] = -mean * rstd[s];
      }
      asm volatile("" : "+v"(rstd[0]), "+v"(rstd[1]), "+v"(mr[0]), "+v"(mr[1]));
    }
    if constexpr (st == 2) STAMP(45);
    if constexpr (st == 5) STAMP(46);
    if constexpr (st == 8) STAMP(47);
    static_for<0, NM>([&](auto I) {
      constexpr int i = I;
      mma1(B, I, IC<0>{});
      if constexpr (LOADER) { if constexpr (st <= 6 && i < NP) piece(IC<st + 3>{}, I); }
      else { if constexpr (st <= 6 && (i % 3) == 0 && i / 3 < NP) piece(IC<st + 3>{}, IC<i / 3>{}); }
      if constexpr (st < 9 && i < NR) rd1(A, IC<nslot>{}, IC<0>{}, I);
      SB();
    });
  });
  MFMA_FENCE();
  STAMP(11);
  LBAR();
  const int kq4 = w >> 1, th4 = w & 1;
  const char* w4src = ws + WS_W4 + (size_t)(cs & 7) * 270 * 1024 + (size_t)(kq4 < 2 ? 14 * kq4 : 28 + 13 * (kq4 - 2)) * 5120;
  const unsigned w4l = kq4 * 5120;
  f32x4 cgv[NT], cuv[NT];
#pragma unroll
  for (int t = 0; t < NT; ++t) {
    cgv[t] = *(const LAS f32x4*)(L + L_CS3 + ((TILE0 + t) * 16 + 4 * g) * 4);
    cuv[t] = *(const LAS f32x4*)(L + L_CS3 + (112 + (TILE0 + t) * 16 + 4 * g) * 4);
  }
  static_for<0, 2>([&](auto S) {
    constexpr int s = S;
    unsigned y[NT][2];
    const unsigned ab = WS_ACT + (8 * r + 2 * tg + s) * 54 * 1024 + l16;
    static_for<0, NT>([&](auto T) {
      constexpr int t = T, step = s * NT + t, j0 = step < NT ? step * P4_EARLY / NT : P4_EARLY, j1 = step < NT ? (step + 1) * P4_EARLY / NT : P4_EARLY;
      y[t][0] = geglu2(lo2(ag[t][s]), lo2(au[t][s]), lo2(cgv[t]), lo2(cuv[t]), rstd[s], mr[s]);
      static_for<j0, (j0 + j1 + 1) / 2>([&](auto J) { p4_pre_piece<J>(w4src, l16, th4, w4l); });
      y[t][1] = geglu2(hi2(ag[t][s]), hi2(au[t][s]), hi2(cgv[t]), hi2(cuv[t]), rstd[s], mr[s]);
      static_for<(j0 + j1 + 1) / 2, j1>([&](auto J) { p4_pre_piece<J>(w4src, l16, th4, w4l); });
      if constexpr (t & 1) {
        if constexpr (TILE0 == 0) stg_sc1(rs, p < 15 ? ab + (3 * p + (t >> 1)) * 1024 : 0x7FFFFFF0u, v4u{y[t - 1][0], y[t - 1][1], y[t][0], y[t][1]});
        else stg_sc1(rs, ab + (p < 15 ? 3 * p + 2 : 45) * 1024, v4u{y[t - 1][0], y[t - 1][1], y[t][0], y[t][1]});
      }
    });
    if constexpr (TILE0 != 0) stg2_sc1(rs, ab + (46 + (p >> 1)) * 1024 + (p & 1) * 8, v2u{y[NT - 1][0], y[NT - 1][1]});
  });
}

struct FragKV { v4u x[2], w[5]; };

__global__ __launch_bounds__(512, 2) void fused_block(const u16* __restrict__ x, const u16* __restrict__ wqkv, const u16* __restrict__ wo,
                                                      const u16* __restrict__ wg, const u16* __restrict__ wu, const u16* __restrict__ wd,
                                                      u16* __restrict__ out, char* __restrict__ ws) {
  extern __shared__ __attribute__((aligned(16))) char smem_[];
  LAS char* L = (LAS char*)smem_;
  const int tid = threadIdx.x, l = tid & 63, c = l & 15, g = l >> 4;
  const int w = __builtin_amdgcn_readfirstlane(tid >> 6);
  const int b = blockIdx.x, xcd = b & 7, jj = b >> 3, rg = xcd >> 1, cgp = xcd & 1;
  const int r = 4 * rg + (jj >> 3), p = 8 * cgp + (jj & 7);
  const int r2 = 4 * xcd + (jj >> 3), cs = jj & 7;
  const int r3 = 8 * (xcd >> 2) + (jj >> 2), c3 = 4 * (xcd & 3) + (jj & 3);
  const __amdgpu_buffer_rsrc_t rs = __builtin_amdgcn_make_buffer_rsrc(ws, 0, WS_END, 0x00020000);
  const v4u ONES = {0x3F803F80u, 0x3F803F80u, 0x3F803F80u, 0x3F803F80u};
  const int q4 = (l & 15) >> 2, pp = l & 3;
  STAMP(0);

  unsigned tag;
  constexpr int S1 = 26624;
  const int xo0 = c * 128 + ((g ^ (c & 7)) << 4);
  const int wrow = 16 * (g >> 1) + 4 * (g & 1) + q4;
  const int kvo = 16384 + wrow * 160 + 8 * pp;
  auto kv_product = [&](int w) {
    const int Ta = w < 2 ? 0 : w < 5 ? 1 : 2;
    const int Tb = (w == 0 || w == 2 || w == 7) ? 2 : (w == 4 || w == 6) ? 4 : 3;
    v4u A[4], B[4];
#pragma unroll
    for (int tg = 0; tg < 4; ++tg) {
      A[tg] = lds128(L, L_KVF + (tg * 5 + Ta) * 1024 + l * 16);
      B[tg] = lds128(L, L_KVF + (tg * 5 + Tb) * 1024 + l * 16);
    }
    f32x4 d = {0, 0, 0, 0};
    const bool one = Tb == 2 && c < 8;
#pragma unroll
    for (int tg = 0; tg < 4; ++tg) d = mfma(A[tg], one ? ONES : B[tg], d);
    const int kc = 16 * Ta + 4 * g;
    const int vc = 16 * Tb + c - 40;
    const int hk = kc >= 20;
    const bool iskv = kc < 40 && vc >= 0 && hk == (vc >= 20), isz = kc < 40 && vc < 0 && c == 0;
    *(LAS f32x4*)(L + (iskv ? L_KVC + (vc * 20 + kc - 20 * hk) * 4 : isz ? L_KVC + (800 + kc) * 4 : L_SYNC + 512 + (l & 31) * 16)) = d;
  };

  if (w < 4) {
    {
      const char* pc;
      asm volatile("s_getpc_b64 %0" : "=s"(pc));
      const int n = (jj * 4 + w) * 4 + (l & 3);
      if (l < 4) touch64(pc + min(n * 128, T0KB * 1024 - 128));
    }
    auto hint1 = [&](int stg, int n) {
      const int idx = jj * 36 + min(n, 35);
      const int wr_ = idx / 10, pc = idx - wr_ * 10, sec = pc / 5;
      touch64(idx < 640 ? (const char*)wqkv + (64 * stg + wr_) * 3840 + (1 + sec) * 1280 + cgp * 640 + (pc - sec * 5) * 128
                        : (const char*)x + ((size_t)rg * 512 + (idx - 640)) * 1280 + stg * 128);
    };
    {
      __builtin_amdgcn_s_sleep(T1SLEEP);
      const int sl_ = l / 9;
      if (l < 63) hint1(2 + sl_, 9 * w + (l - 9 * sl_));
    }
    if (l == 0) asm volatile("s_mov_b32 m0, %1\n\ts_nop 0\n\tglobal_load_lds_dword %0, off sc1" ::"v"(ws + WS_EPOCH), "s"(L_SYNC + 32) : "memory");
    *(LAS v4u*)(L + L_BD + tid * 16) = v4u{0, 0, 0, 0};
    *(LAS v4u*)(L + L_BD + 4096 + tid * 16) = v4u{0, 0, 0, 0};
    if (tid < 2) *(LAS v4u*)(L + L_SYNC + tid * 16) = v4u{0, 0, 0, 0};
    if (tid == 2) *(LAS v4u*)(L + L_SYNC + 48) = v4u{0, 0, 0, 0};
    auto hint2 = [&](int i) {
      const int n = 96 * w + 16 * i + (l & 15);
      const int li = b * 228 + min(n, 227);
      const char* aw_ = li < 6400 ? (const char*)wo + li * 128 : li < 23680 ? (const char*)wg + (li - 6400) * 128
                      : li < 40960 ? (const char*)wu + (li - 23680) * 128 : (const char*)wd + min(li - 40960, 17279) * 128;
      const int m = n - 228;
      const unsigned o5 = m < 32 ? WS_O + ((8 * r + (m >> 2)) * 20 + 16 + (p >> 2)) * 1024 + ((p & 3) >> 1) * 512 + (m & 3) * 128
                        : m < 64 ? WS_H + ((4 * r2 + ((m - 32) >> 3)) * 20 + 16 + (cs >> 1)) * 1024 + (m & 7) * 128
                                 : WS_ACT + ((8 * r3 + (min(m - 64, 63) >> 3)) * 54 + 46 + (c3 >> 1)) * 1024 + (m & 7) * 128;
      const char* a = n < 228 ? aw_ : ws + o5;
      if (l < 16) touch64(a);
    };
    STAMP(1);
    f32x4 acc[2][5];
    {
      const int xw = w * 32 * 128;
      int xbs[3][2], kbs[3];
#pragma unroll
      for (int q = 0; q < 3; ++q) { xbs[q][0] = xw + xo0 + 2 * q * S1; xbs[q][1] = xw + (xo0 ^ 64) + 2 * q * S1; kbs[q] = kvo + 2 * q * S1; }
      asm volatile("" : "+v"(xbs[0][0]), "+v"(xbs[0][1]), "+v"(xbs[1][0]), "+v"(xbs[1][1]), "+v"(xbs[2][0]), "+v"(xbs[2][1]), "+v"(kbs[0]), "+v"(kbs[1]), "+v"(kbs[2]));
      auto rd1 = [&](FragKV& f, auto SLOT, auto KS, auto I) {
        constexpr int slot = SLOT, ks = KS, i = I;
        if constexpr (i < 2) f.x[i] = lds128(L, xbs[slot >> 1][ks] + (slot & 1) * S1 + i * 2048);
        else {
          constexpr int j = i - 2, t = j >> 1, h = j & 1;
          const v2u v = __builtin_bit_cast(v2u, __builtin_amdgcn_ds_read_tr16_b64_v4i16((LAS s16x4*)(L + kbs[slot >> 1] + (slot & 1) * S1 + ks * 32 * 160 + t * 32 + h * 8 * 160)));
          if constexpr (h == 0) { f.w[t].x = v.x; f.w[t].y = v.y; } else { f.w[t].z = v.x; f.w[t].w = v.y; }
        }
      };
      auto mma1 = [&](const FragKV& f, auto I, auto FIRST) { constexpr int i = I, t = i >> 1, s = i & 1; mfma_a<FIRST>(acc[s][t], f.x[s], f.w[t]); };
      FragKV A, B;
      LBAR();
      static_for<0, 12>([&](auto I) { rd1(A, IC<0>{}, IC<0>{}, I); });
      static_for<0, 10>([&](auto ST) {
        constexpr int st = ST;
        static_for<0, 10>([&](auto I) {
          constexpr int i = I;
          mma1(A, I, IC<(st == 0)>{});
          if constexpr (i < 6) { rd1(B, IC<(st % 5)>{}, IC<1>{}, IC<2 * i>{}); rd1(B, IC<(st % 5)>{}, IC<1>{}, IC<2 * i + 1>{}); }
          SB();
        });
        if constexpr (st < 9) LBAR();
        if constexpr (st == 0) STAMP(48);
        if constexpr (st == 3) STAMP(49);
        if constexpr (st == 6) STAMP(50);
        static_for<0, 10>([&](auto I) {
          constexpr int i = I;
          mma1(B, I, IC<0>{});
          if constexpr (st >= 2 && st < 8 && i == 8) hint2(st - 2);
          if constexpr (st < 9 && i < 6) { rd1(A, IC<((st + 1) % 5)>{}, IC<0>{}, IC<2 * i>{}); rd1(A, IC<((st + 1) % 5)>{}, IC<0>{}, IC<2 * i + 1>{}); }
          SB();
        });
      });
      MFMA_FENCE();
    }
    STAMP(2);
    __builtin_amdgcn_s_setprio(2);
    waitvm<(T2MODE == 1 ? 6 : 0)>();
    LBAR();
    STAMP(16);
    tag = __builtin_amdgcn_readfirstlane(*(const LAS unsigned*)(L + L_SYNC + 32)) + 1u;
    {
      f32x4 rstd[2], mr[2];
#pragma unroll
      for (int s = 0; s < 2; ++s) {
        rstd[s] = *(const LAS f32x4*)(L + L_STAT + (32 * w + 16 * s + 4 * g) * 4);
        mr[s] = *(const LAS f32x4*)(L + L_STAT + 512 + (32 * w + 16 * s + 4 * g) * 4);
      }
      float cw[5];
#pragma unroll
      for (int t = 0; t < 5; ++t) cw[t] = *(const LAS float*)(L + L_CSUM + (16 * t + c) * 4);
#pragma unroll
      for (int t = 0; t < 5; ++t) {
        unsigned o[4];
#pragma unroll
        for (int s = 0; s < 2; ++s)
#pragma unroll
          for (int h = 0; h < 2; ++h) {
            const f32x2 a2 = h ? hi2(acc[s][t]) : lo2(acc[s][t]), r2_ = h ? hi2(rstd[s]) : lo2(rstd[s]), m2 = h ? hi2(mr[s]) : lo2(mr[s]);
            f32x2 y;
            if (t < 2) y = phi2(pfma(a2, r2_, pfma(m2, sp2(cw[t]), sp2(1e-6f))));
            else if (t == 2) {
              const f32x2 te = pfma(a2, r2_, pfma(m2, sp2(cw[t]), sp2(1e-6f))), ph = phi2(te), v = te - sp2(1e-6f);
              y = c < 8 ? ph : v;
            } else y = pfma(a2, r2_, m2 * sp2(cw[t]));
            o[2 * s + h] = pk2v(y);
          }
        *(LAS v4u*)(L + L_KVF + (w * 5 + t) * 1024 + l * 16) = v4u{o[0], o[1], o[2], o[3]};
      }
    }
    STAMP(17);
    group_sync(L, 5, l);
    kv_product(w);
    kv_product(w + 4);
    group_sync(L, 6, l);
    STAMP(19);
    if (w == 0) {
      v4u v[4];
#pragma unroll
      for (int i = 0; i < 4; ++i) v[i] = lds128(L, L_KVC + min(i * 64 + l, 209) * 16);
#pragma unroll
      for (int i = 0; i < 4; ++i) stg_sc1(rs, i * 64 + l < 210 ? WS_KVP + (p * 16 + r) * KVP_STRIDE + (i * 64 + l) * 16 : 0x7FFFFFF0u, v[i]);
      STAMP(20);
      DRAIN();
      if (l == 0) set_flag(ws, WS_FKV + p * 256 + r * 4, tag);
      STAMP(3);
    }
    __builtin_amdgcn_s_setprio(0);
    const int f0 = min(7 * c3, 101);
    const int u3 = min(r3 + 16 * max(w - 1, 0), 39), ks3 = u3 >> 1, mat = u3 & 1;
    const int ks4 = min(r2 + 32 * max(w - 1, 0), 53);
    const unsigned area = L_KVF + max(w - 1, 0) * 12288;
    if (w > 0) {
      const char* w3 = (const char*)(mat ? wu : wg);
#pragma unroll
      for (int i = 0; i < 7; ++i) {
        const int n = 64 * i + l, pos = n / 14, ch = n - pos * 14;
        const int feat = ks3 < 16 ? 80 * (ks3 >> 1) + 32 * (ks3 & 1) + pos : 160 * (ks3 - 16) + 64 + (pos & 15) + 80 * (pos >> 4);
        glds(feat * 3456 + (f0 * 16 + 8 * ch) * 2, w3, area + i * 1024);
      }
      if (w < 3) {
#pragma unroll
        for (int i = 0; i < 5; ++i) {
          const int n = 64 * i + l, pos = n / 10, ch = n - pos * 10, t = pos >> 4;
          const int sp = ks4 < 45 ? ks4 / 3 : ks4 == 45 ? 15 : 2 * (ks4 - 46) + t;
          const int lt = ks4 < 45 ? 2 * (ks4 % 3) + t : ks4 == 45 ? 4 + t : 6;
          const int row = 16 * (min(7 * sp, 101) + lt) + (pos & 15);
          glds(row * 1280 + (80 * cs + 8 * ch) * 2, wd, area + 7168 + i * 1024);
        }
      }
    }
    if (w > 0) {
      if (w < 3) waitvm<5>(); else waitvm<0>();
      {
        const int tb = area + (4 * g + q4) * 224 + 8 * pp;
        v4u fr[7];
#pragma unroll
        for (int t = 0; t < 7; ++t) fr[t] = ldtr(L, tb + t * 32, 16 * 224);
        const bool ok = r3 + 16 * (w - 1) < 40;
        const unsigned ob = ok ? WS_W3 + ((c3 * 20 + ks3) * 14 + mat * 7) * 1024 + l * 16 : 0x7FFF0000u;
        float csel0 = 0.f, csel1 = 0.f;
#pragma unroll
        for (int t = 0; t < 7; ++t) {
          stg_sc1(rs, ob + t * 1024, fr[t]);
          const f32x4 cs_ = mfma(ONES, fr[t], f32x4{0, 0, 0, 0});
          if (t < 4) csel0 = (g == t) ? cs_[0] : csel0; else csel1 = (g == t - 4) ? cs_[0] : csel1;
        }
        const unsigned cb = ok ? WS_CS + (c3 * 40 + u3) * 448 + l * 4 : 0x7FFF0000u;
        stg1_sc1(rs, cb, __float_as_uint(csel0));
        stg1_sc1(rs, l < 48 ? cb + 256 : 0x7FFF0000u, __float_as_uint(csel1));
      }
      if (w < 3) {
        waitvm<9>();
        const int tb = area + 7168 + (4 * g + q4) * 160 + 8 * pp;
        v4u fr[5];
#pragma unroll
        for (int t = 0; t < 5; ++t) fr[t] = ldtr(L, tb + t * 32, 16 * 160);
        const unsigned ob = (r2 + 32 * (w - 1) < 54) ? WS_W4 + (cs * 54 + ks4) * 5 * 1024 + l * 16 : 0x7FFF0000u;
#pragma unroll
        for (int t = 0; t < 5; ++t) stg_sc1(rs, ob + t * 1024, fr[t]);
      }
    }
    if (w == 0) wait32(ws, WS_FW + cs * 256, tag, l);
    group_sync(L, 12, l);
    lds_spin(L, 4, 4);
    {
      const char* w2 = ws + WS_W2 + (size_t)cs * 100 * 1024 + w * 25 * 1024;
      static_for<0, 6>([&](auto I) { constexpr int i = I; gldsn_sc1<4, i * 4096>(l * 16, w2 + i * 4096, w * 25 * 1024); });
      gldsn_sc1<1, 24 * 1024>(l * 16, w2 + 24 * 1024, w * 25 * 1024);
    }
    DRAIN();
    if (l == 0 && lds_add(L, 3) == 3) { set_flag(ws, WS_FC + c3 * 256 + r3 * 4, tag); set_flag(ws, WS_FD + cs * 256 + r2 * 4, tag); }
    STAMPW(23, 1);
  } else {
    const int tw = w - 4;
    unsigned vx[4], vw[3];
#pragma unroll
    for (int i = 0; i < 4; ++i) { const int tok = 8 * (4 * tw + i) + (l >> 3); vx[i] = tok * 1280 + (((l & 7) ^ (tok & 7)) << 4); }
    const char* xb = (const char*)x + (size_t)r * 128 * 1280;
    const unsigned lxw = 4 * tw * 1024, lkw = 16384 + 160 * tw * 16;
    auto pieceX = [&](auto ST, auto I) { constexpr int st = ST, i = I; gldsi<(st % 5) * S1 + i * 1024>(vx[i], xb + st * 128, lxw); };
    static_for<0, 4>([&](auto I) { pieceX(IC<0>{}, I); });
    constexpr unsigned AREA_O = 133120;
    if (w == 7) {
      if (l == 0) asm volatile("s_mov_b32 m0, %1\n\ts_nop 0\n\tglobal_load_lds_dword %0, off sc1" ::"v"(ws + WS_EPOCH), "s"(L_SYNC + 36) : "memory");
      const int ks2 = min(r2, 19);
#pragma unroll
      for (int i = 0; i < 5; ++i) {
        const int n = 64 * i + l, pos = n / 10, ch = n - pos * 10, t = pos >> 4, gg = (pos >> 2) & 3;
        const int row = ks2 < 16 ? 40 * ks2 + pos : 40 * (4 * (ks2 - 16) + 2 * (gg >> 1) + t) + 32 + 4 * (gg & 1) + (pos & 3);
        glds(row * 1280 + (80 * cs + 8 * ch) * 2, wo, AREA_O + i * 1024);
      }
    }
#pragma unroll
    for (int j = 0; j < 3; ++j) {
      const int n = min(160 * tw + 64 * j + l, 639), pos = n / 10, ch = n - pos * 10;
      const int col = (ch < 5) ? 640 + 40 * p + 8 * ch : 1280 + 40 * p + 8 * (ch - 5);
      const int k = (pos & ~12) | ((pos & 4) << 1) | ((pos & 8) >> 1);
      vw[j] = k * 3840 + col * 2;
    }
    auto pieceW = [&](auto ST, auto J) {
      constexpr int st = ST, j = J;
      if constexpr (j < 2) gldsi<(st % 5) * S1 + j * 1024>(vw[j], (const char*)wqkv + (size_t)st * 64 * 3840, lkw);
      else { if (l < 32) gldsi<(st % 5) * S1 + j * 1024>(vw[j], (const char*)wqkv + (size_t)st * 64 * 3840, lkw); }
    };
    auto piece = [&](auto ST, auto Q) { constexpr int q = Q; if constexpr (q < 4) pieceX(ST, Q); else pieceW(ST, IC<q - 4>{}); };
    static_for<0, 3>([&](auto J) { pieceW(IC<0>{}, J); });
    static_for<0, 7>([&](auto Q) { piece(IC<1>{}, Q); });
    auto pieceQ = [&](auto QS, auto J) {
      constexpr int qs = QS, j = J, npc = qs < 2 ? 6 : 3;
      const int n = 64 * (tw * npc + j) + l, pos = n / 6, ch = n - pos * 6;
      const int k = 256 * qs + ((pos & ~12) | ((pos & 4) << 1) | ((pos & 8) >> 1));
      if (ch < 5) gldsi<qs * S1 + j * 1024>(k * 3840 + (40 * p + 8 * ch) * 2, wqkv, tw * npc * 1024);
    };
    STAMPW(51, 4);
    v4u xs[20][2];
    f32x4 sq[2], sm[2], cs0, cs1;
    v4u ones_ = ONES;
    asm volatile("" : "+v"(ones_));
    {
      const int xw = tw * 32 * 128;
      int xbs[3][2], ebs[3][2];
#pragma unroll
      for (int q = 0; q < 3; ++q) { xbs[q][0] = xw + xo0 + 2 * q * S1; xbs[q][1] = xw + (xo0 ^ 64) + 2 * q * S1; ebs[q][0] = kvo + tw * 32 + 2 * q * S1; ebs[q][1] = kvo + 4 * 32 + 2 * q * S1; }
      asm volatile("" : "+v"(xbs[0][0]), "+v"(xbs[0][1]), "+v"(xbs[1][0]), "+v"(xbs[1][1]), "+v"(xbs[2][0]), "+v"(xbs[2][1]),
                        "+v"(ebs[0][0]), "+v"(ebs[0][1]), "+v"(ebs[1][0]), "+v"(ebs[1][1]), "+v"(ebs[2][0]), "+v"(ebs[2][1]));
      v4u e[2][2];
      auto rd1 = [&](auto KK, auto I) {
        constexpr int kk = KK, i = I, slot = (kk >> 1) % 5, ks = kk & 1;
        if constexpr (i < 2) xs[kk][i] = lds128(L, xbs[slot >> 1][ks] + (slot & 1) * S1 + i * 2048);
        else {
          constexpr int t = (i - 2) >> 1, h = i & 1;
          const v2u v = __builtin_bit_cast(v2u, __builtin_amdgcn_ds_read_tr16_b64_v4i16((LAS s16x4*)(L + ebs[slot >> 1][t] + (slot & 1) * S1 + ks * 32 * 160 + h * 8 * 160)));
          if constexpr (h == 0) { e[kk & 1][t].x = v.x; e[kk & 1][t].y = v.y; } else { e[kk & 1][t].z = v.x; e[kk & 1][t].w = v.y; }
        }
      };
      auto mma1 = [&](auto KK, auto I) {
        constexpr int kk = KK, i = I;
        if constexpr (i < 2) mfma_a<(kk == 0)>(sq[i], xs[kk][i], xs[kk][i]);
        else if constexpr (i < 4) mfma_a<(kk == 0)>(sm[i - 2], ones_, xs[kk][i - 2]);
        else if constexpr (i == 4) mfma_a<(kk == 0)>(cs0, ones_, e[kk & 1][0]);
        else mfma_a<(kk == 0)>(cs1, ones_, e[kk & 1][1]);
      };
      waitbar<7>();
      if (w == 7) {
        v4u fr[5];
#pragma unroll
        for (int t = 0; t < 5; ++t) fr[t] = ldtr(L, AREA_O + (4 * g + q4) * 160 + 8 * pp + t * 32, 16 * 160);
        const unsigned ob = r2 < 20 ? WS_W2 + (cs * 20 + min(r2, 19)) * 5 * 1024 + l * 16 : 0x7FFF0000u;
#pragma unroll
        for (int t = 0; t < 5; ++t) stg_sc1(rs, ob + t * 1024, fr[t]);
      }
      static_for<0, 6>([&](auto I) { rd1(IC<0>{}, I); });
      static_for<0, 10>([&](auto ST) {
        constexpr int st = ST;
        static_for<0, 6>([&](auto I) {
          constexpr int i = I;
          mma1(IC<2 * st>{}, I);
          rd1(IC<2 * st + 1>{}, I);
          if constexpr (st == 0) { piece(IC<2>{}, I); if constexpr (i == 5) piece(IC<2>{}, IC<6>{}); }
          if constexpr (st == 1) { piece(IC<4>{}, I); if constexpr (i == 5) piece(IC<4>{}, IC<6>{}); }
          SB();
        });
        if constexpr (st < 9) { if constexpr (st == 0) waitbar<7>(); else if constexpr (st <= 6) waitbar<14>(); else if constexpr (st == 7) waitbar<13>(); else waitbar<12>(); }
        if constexpr (st == 6) {
          if (w == 7 && l == 0) set_flag(ws, WS_FW + cs * 256 + r2 * 4, *(const LAS unsigned*)(L + L_SYNC + 36) + 1u);
        }
        static_for<0, 6>([&](auto I) {
          constexpr int i = I;
          mma1(IC<2 * st + 1>{}, I);
          if constexpr (st == 0) { piece(IC<3>{}, I); if constexpr (i == 5) piece(IC<3>{}, IC<6>{}); }
          if constexpr (st >= 1 && st <= 5) { piece(IC<st + 4>{}, I); if constexpr (i == 5) piece(IC<st + 4>{}, IC<6>{}); }
          if constexpr (st == 6 || st == 7) pieceQ(IC<st - 6>{}, I);
          if constexpr (st == 8 && i < 3) pieceQ(IC<2>{}, I);
          if constexpr (st < 9) rd1(IC<2 * st + 2>{}, I);
          SB();
        });
      });
      MFMA_FENCE();
    }
#pragma unroll
    for (int s = 0; s < 2; ++s) {
      const int e_ = c & 3;
      const float v = e_ == 0 ? sq[s][0] : e_ == 1 ? sq[s][1] : e_ == 2 ? sq[s][2] : sq[s][3];
      const float m = sm[s][0] * (1.0f / 640.0f);
      const float rs_ = __builtin_amdgcn_rsqf(fmaxf(v * (1.0f / 640.0f) - m * m + 1e-5f, 1e-5f));
      const bool own = g == (c >> 2);
      *(LAS float*)(L + (own ? L_STAT + (32 * tw + 16 * s + c) * 4 : L_SYNC + 512 + l * 4)) = rs_;
      *(LAS float*)(L + (own ? L_STAT + 512 + (32 * tw + 16 * s + c) * 4 : L_SYNC + 768 + l * 4)) = -m * rs_;
    }
    *(LAS float*)(L + (g == 0 ? L_CSUM + (16 * tw + c) * 4 : L_SYNC + 512 + l * 4)) = cs0[0];
    *(LAS float*)(L + (g == 0 ? L_CSUM + (64 + c) * 4 : L_SYNC + 512 + l * 4)) = cs1[0];
    LBAR();
    tag = __builtin_amdgcn_readfirstlane(*(const LAS unsigned*)(L + L_SYNC + 32)) + 1u;
    DRAIN();
    group_sync(L, 7, l);
    lds_spin(L, 5, 4);
    f32x4 qacc[3][2], csq;
    {
      int qbs[3];
#pragma unroll
      for (int q = 0; q < 3; ++q) qbs[q] = q * S1 + wrow * 96 + 8 * pp;
      asm volatile("" : "+v"(qbs[0]), "+v"(qbs[1]), "+v"(qbs[2]));
      int cbs[3];
#pragma unroll
      for (int q = 0; q < 3; ++q) cbs[q] = qbs[q] + (tw < 3 ? tw : 2) * 32;
      asm volatile("" : "+v"(cbs[0]), "+v"(cbs[1]), "+v"(cbs[2]));
      v4u f[2][4];
      auto rd1 = [&](auto KK, auto I) {
        constexpr int kk = KK, i = I, t = i >> 1, h = i & 1;
        const int base = t < 3 ? qbs[kk >> 3] + t * 32 : cbs[kk >> 3];
        const v2u v = __builtin_bit_cast(v2u, __builtin_amdgcn_ds_read_tr16_b64_v4i16((LAS s16x4*)(L + base + (kk & 7) * 32 * 96 + h * 8 * 96)));
        if constexpr (h == 0) { f[kk & 1][t].x = v.x; f[kk & 1][t].y = v.y; } else { f[kk & 1][t].z = v.x; f[kk & 1][t].w = v.y; }
      };
      static_for<0, 8>([&](auto I) { rd1(IC<0>{}, I); });
      static_for<0, 20>([&](auto KK) {
        constexpr int kk = KK;
        static_for<0, 7>([&](auto I) {
          constexpr int i = I;
          if constexpr (i < 6) mfma_a<(kk == 0)>(qacc[i >> 1][i & 1], f[kk & 1][i >> 1], xs[kk][i & 1]);
          else mfma_a<(kk == 0)>(csq, ones_, f[kk & 1][3]);
          if constexpr (kk < 19) { rd1(IC<kk + 1>{}, I); if constexpr (i == 6) rd1(IC<kk + 1>{}, IC<7>{}); }
          SB();
        });
      });
      MFMA_FENCE();
    }
    *(LAS float*)(L + (g == 0 && tw < 3 ? L_CSUM + (80 + 16 * tw + c) * 4 : L_SYNC + 512 + l * 4)) = csq[0];
    asm volatile("s_waitcnt lgkmcnt(0)" ::: "memory");
    if (l == 0) lds_add(L, 4);
    lds_spin(L, 4, 4);
    v4u pqA[2], pqB[2];
#pragma unroll
    for (int s = 0; s < 2; ++s) {
      const float rs_ = *(const LAS float*)(L + L_STAT + (32 * tw + 16 * s + c) * 4);
      const float mr = *(const LAS float*)(L + L_STAT + 512 + (32 * tw + 16 * s + c) * 4);
      unsigned pq[6];
#pragma unroll
      for (int t = 0; t < 3; ++t) {
        const f32x4 cw = *(const LAS f32x4*)(L + L_CSUM + (80 + 16 * t + 4 * g) * 4);
        pq[2 * t] = pk2v(phi2(pfma(lo2(qacc[t][s]), sp2(rs_), pfma(lo2(cw), sp2(mr), sp2(1e-6f)))));
        pq[2 * t + 1] = pk2v(phi2(pfma(hi2(qacc[t][s]), sp2(rs_), pfma(hi2(cw), sp2(mr), sp2(1e-6f)))));
      }
      pqA[s] = v4u{pq[0], pq[1], pq[2], pq[3]};
      pqB[s] = v4u{g < 2 ? pq[4] : 0u, g < 2 ? pq[5] : 0u, 0u, 0u};
    }
    asm volatile("" : "+v"(pqA[0]), "+v"(pqA[1]), "+v"(pqB[0]), "+v"(pqB[1]));
    STAMPW(22, 4);
    if (w == 4) { wait16(ws, WS_FKV + p * 256, tag, l); if (l == 0) lds_add(L, 0); } else lds_spin(L, 0, 1);
    STAMPW(21, 4);
    {
      const int t = min(tid - 256, 209);
      v4u v[16];
#pragma unroll
      for (int i = 0; i < 16; ++i) v[i] = ldg_sc1(rs, WS_KVP + (p * 16 + i) * KVP_STRIDE + t * 16);
      f32x4 s = {0, 0, 0, 0};
#pragma unroll
      for (int i = 0; i < 16; ++i) s += __builtin_bit_cast(f32x4, v[i]);
      const v2u pk = {pk2v(lo2(s)), pk2v(hi2(s))};
      const int fo = t / 5, f = t < 200 ? 4 * (t - fo * 5) + (fo >= 20 ? 20 : 0) : 4 * (t - 200);
      const int kst = f >> 5, tt = (f >> 4) & 1, gg = (f >> 2) & 3;
      if (t < 200) *(LAS v2u*)(L + L_BD + ((fo >> 4) * 2 + kst) * 1024 + (gg * 16 + (fo & 15)) * 16 + tt * 8) = pk;
      else {
#pragma unroll
        for (int i = 0; i < 8; ++i) *(LAS v2u*)(L + L_BD + (6 + kst) * 1024 + (gg * 16 + 2 * i + (f >= 20)) * 16 + tt * 8) = pk;
      }
    }
    asm volatile("s_waitcnt lgkmcnt(0)" ::: "memory");
    if (l == 0) lds_add(L, 1);
    lds_spin(L, 1, 4);
    STAMPW(24, 4);
    v4u bd[8];
#pragma unroll
    for (int i = 0; i < 8; ++i) bd[i] = lds128(L, L_BD + i * 1024 + l * 16);
#pragma unroll
    for (int s = 0; s < 2; ++s) {
      f32x4 den = mfma(bd[6], pqA[s], f32x4{0, 0, 0, 0}); den = mfma(bd[7], pqB[s], den);
      f32x4 num[3];
#pragma unroll
      for (int ft = 0; ft < 3; ++ft) { num[ft] = mfma(bd[2 * ft], pqA[s], f32x4{0, 0, 0, 0}); num[ft] = mfma(bd[2 * ft + 1], pqB[s], num[ft]); }
      const float r0 = __builtin_amdgcn_rcpf(den[0] + 1e-6f), r1 = __builtin_amdgcn_rcpf(den[1] + 1e-6f);
      const float rm = g == 0 ? r0 : r1;
      const unsigned base = WS_O + (8 * r + 2 * tw + s) * 20 * 1024;
      stg_sc1(rs, base + p * 1024 + l * 16, v4u{pk2v(lo2(num[0]) * r0), pk2v(hi2(num[0]) * r0), pk2v(lo2(num[1]) * rm), pk2v(hi2(num[1]) * rm)});
      stg2_sc1(rs, g < 2 ? base + (16 + (p >> 2)) * 1024 + ((2 * ((p & 3) >> 1) + g) * 16 + c) * 16 + (p & 1) * 8 : 0x7FFFFFF0u,
               v2u{pk2v(lo2(num[2]) * r1), pk2v(hi2(num[2]) * r1)});
    }
    STAMPW(26, 4);
    DRAIN();
    STAMPW(27, 4);
    if (l == 0 && lds_add(L, 2) == 3) set_flag(ws, WS_FO + r * 256 + p * 4, tag);
  }
  LBAR();
  STAMP(5);

  const int th = w & 1, kq = w >> 1;
  const int sl = 2 * th + (kq & 1);
  const int tok = 16 * (4 * r2 + sl) + c;
  {
    v2u xr[3];
    const __amdgpu_buffer_rsrc_t rsx = __builtin_amdgcn_make_buffer_rsrc((void*)x, 0, 2048 * 640 * 2, 0x00020000);
#pragma unroll
    for (int i = 0; i < 3; ++i) {
      const int T = kq < 2 ? (i == 2 ? 4 : i) : (i == 2 ? 2 : 2 + i);
      xr[i] = __builtin_amdgcn_raw_buffer_load_b64(rsx, (tok * 640 + 80 * cs + 16 * T + 4 * g) * 2, 0, 0);
    }
    v4u wf[5][5];
#pragma unroll
    for (int k = 0; k < 5; ++k)
#pragma unroll
      for (int T = 0; T < 5; ++T) wf[k][T] = lds128(L, ((5 * kq + k) * 5 + T) * 1024 + l * 16);
#pragma unroll
    for (int k = 0; k < 5; ++k) asm volatile("" : "+v"(wf[k][0]), "+v"(wf[k][1]), "+v"(wf[k][2]), "+v"(wf[k][3]), "+v"(wf[k][4]));
    STAMP(28);
    if (w == 0) wait16(ws, WS_FO + (r2 >> 1) * 256, tag, l);
    if (w == 1) wait16(ws, WS_FC + c3 * 256, tag, l);
    if (w == 2) wait32(ws, WS_FD + cs * 256, tag, l);
    LBAR();
    STAMP(6);
    if (b == 0 && tid == 0) set_flag(ws, WS_EPOCH, tag);
    v4u xf[5][2];
#pragma unroll
    for (int k = 0; k < 5; ++k)
#pragma unroll
      for (int s = 0; s < 2; ++s) xf[k][s] = ldg_sc1(rs, WS_O + ((4 * r2 + 2 * th + s) * 20 + 5 * kq + k) * 1024 + l * 16);
    f32x4 acc[5][2];
#pragma unroll
    for (int T = 0; T < 5; ++T) { acc[T][0] = f32x4{0, 0, 0, 0}; acc[T][1] = f32x4{0, 0, 0, 0}; }
    SB();
#pragma unroll
    for (int k = 0; k < 5; ++k)
#pragma unroll
      for (int T = 0; T < 5; ++T)
#pragma unroll
        for (int s = 0; s < 2; ++s) acc[T][s] = mfma(wf[k][T], xf[k][s], acc[T][s]);
    STAMP(7);
    f32x4 own[3];
    splitk_exchange(L, acc, own, th, kq, l);
    STAMP(29);
    const bool hi = kq >= 2;
    unsigned hp[3][2];
    f32x2 sm2 = {0.f, 0.f}, sq2 = {0.f, 0.f};
#pragma unroll
    for (int i = 0; i < 3; ++i) {
      hp[i][0] = pk2v(f32x2{bflo(xr[i].x), bfhi(xr[i].x)} + lo2(own[i]));
      hp[i][1] = pk2v(f32x2{bflo(xr[i].y), bfhi(xr[i].y)} + hi2(own[i]));
      if (i == 2 && hi) { hp[2][0] = 0u; hp[2][1] = 0u; }
      const f32x2 a0 = {bflo(hp[i][0]), bfhi(hp[i][0])}, a1 = {bflo(hp[i][1]), bfhi(hp[i][1])};
      sm2 += a0 + a1;
      sq2 = pfma(a0, a0, pfma(a1, a1, sq2));
    }
    float sm = sm2.x + sm2.y, sq = sq2.x + sq2.y;
    const unsigned hb = WS_H + (4 * r2 + sl) * 20 * 1024;
    stg_sc1(rs, hb + (2 * cs + (kq >> 1)) * 1024 + l * 16, v4u{hp[0][0], hp[0][1], hp[1][0], hp[1][1]});
    stg2_sc1(rs, hi ? 0x7FFFFFF0u : hb + (16 + (cs >> 1)) * 1024 + l * 16 + (cs & 1) * 8, v2u{hp[2][0], hp[2][1]});
    sm = rowsum4(sm); sq = rowsum4(sq);
    stg2_sc1(rs, g == 0 ? WS_HS + ((4 * r2 + sl) * 16 + cs * 2 + (kq >> 1)) * 128 + c * 8 : 0x7FFFFFF0u, v2u{__float_as_uint(sm), __float_as_uint(sq)});
    *(LAS v2u*)(L + L_HT + (sl * 5 + (hi ? 2 : 0)) * 512 + l * 8) = v2u{hp[0][0], hp[0][1]};
    *(LAS v2u*)(L + L_HT + (sl * 5 + (hi ? 3 : 1)) * 512 + l * 8) = v2u{hp[1][0], hp[1][1]};
    *(LAS v2u*)(L + (hi ? L_SYNC + 512 : L_HT + (sl * 5 + 4) * 512) + l * 8) = v2u{hp[2][0], hp[2][1]};
    STAMP(30);
    DRAIN();
    STAMP(31);
    LBAR();
    if (tid == 0) set_flag(ws, WS_FH + (r2 >> 1) * 256 + ((r2 & 1) * 8 + cs) * 4, tag);
    STAMP(8);
  }
  {
    if (w < 4) p3_body<4, 0, false>(L, ws, rs, r3, c3, cs | ((r2 & 3) << 8), w, l, tag);
    else       p3_body<3, 4, true>(L, ws, rs, r3, c3, cs | ((r2 & 3) << 8), w, l, tag);
    STAMP(36);
    DRAIN();
    STAMP(37);
    LBAR();
    if (tid == 0) set_flag(ws, WS_FA + r3 * 256 + c3 * 4, tag);
    STAMP(12);
  }
  {
    f32x4 acc[5][2];
    if (th == 0) p4_main<0>(L, ws, r2 >> 1, r2, cs, kq, w, l, tag, acc); else p4_main<1>(L, ws, r2 >> 1, r2, cs, kq, w, l, tag, acc);
    LBAR();
    STAMP(14);
    f32x4 own[3];
    splitk_exchange(L, acc, own, th, kq, l);
    const bool hi = kq >= 2;
    u16* ob = out + (size_t)tok * 640 + 80 * cs + 4 * g;
#pragma unroll
    for (int i = 0; i < 3; ++i) {
      const int T = i == 2 ? 4 : (hi ? 2 : 0) + i;
      const v2u hh = *(const LAS v2u*)(L + L_HT + (sl * 5 + T) * 512 + l * 8);
      const v2u o = {pk2v(f32x2{bflo(hh.x), bfhi(hh.x)} + lo2(own[i])), pk2v(f32x2{bflo(hh.y), bfhi(hh.y)} + hi2(own[i]))};
      if (i < 2 || !hi) *(v2u*)(ob + 16 * T) = o;
    }
    STAMP(15);
  }
}

extern "C" void kernel_launch(void* const* d_in, const int* in_sizes, int n_in, void* d_out, int out_size, void* d_ws, size_t ws_size,
                              hipStream_t stream) {
  (void)in_sizes; (void)n_in; (void)out_size; (void)ws_size;
  (void)hipFuncSetAttribute((const void*)fused_block, hipFuncAttributeMaxDynamicSharedMemorySize, LDS_BYTES);
  fused_block<<<256, 512, LDS_BYTES, stream>>>((const u16*)d_in[0], (const u16*)d_in[1], (const u16*)d_in[2], (const u16*)d_in[4],
                                                (const u16*)d_in[5], (const u16*)d_in[6], (u16*)d_out, (char*)d_ws);
}
```

```cpp
#include <hip/hip_runtime.h>
#include <hip/hip_bf16.h>
#include <stdint.h>
#include <type_traits>

#define LAS __attribute__((address_space(3)))
#define GAS __attribute__((address_space(1)))
typedef __bf16 bf16x8 __attribute__((ext_vector_type(8)));
typedef __bf16 bf16x2 __attribute__((ext_vector_type(2)));
typedef short s16x4 __attribute__((ext_vector_type(4)));
typedef float f32x4 __attribute__((ext_vector_type(4)));
typedef float f32x2 __attribute__((ext_vector_type(2)));
typedef unsigned v4u __attribute__((ext_vector_type(4)));
typedef unsigned v2u __attribute__((ext_vector_type(2)));
typedef GAS unsigned gu32;
typedef unsigned short u16;
#define RLX_AGENT __ATOMIC_RELAXED, __HIP_MEMORY_SCOPE_AGENT

constexpr unsigned WS_EPOCH = 0, WS_TMO = 256;
constexpr unsigned WS_FKV = 4096, WS_FO = 8192, WS_FH = 12288, WS_FA = 16384, WS_FC = 20480, WS_FD = 24576;
constexpr unsigned WS_KVP = 32768, KVP_STRIDE = 3456;
constexpr unsigned WS_O = WS_KVP + 256 * KVP_STRIDE;
constexpr unsigned WS_H = WS_O + 2621440;
constexpr unsigned WS_HS = WS_H + 2621440;
constexpr unsigned WS_ACT = WS_HS + 262144;
constexpr unsigned WS_CS = WS_ACT + 7077888;
constexpr unsigned WS_W3 = WS_CS + 16 * 40 * 448;
constexpr unsigned WS_W4 = WS_W3 + 16 * 280 * 1024;
constexpr unsigned WS_STAMP = WS_W4 + 8 * 270 * 1024;
constexpr unsigned WS_END = WS_STAMP + 256 * 64 * 8;

constexpr int LDS_BYTES = 163840;
constexpr int L_KVF = 102400;
constexpr int L_KVC = 135168;
constexpr int L_BD = 138752;
constexpr int L_STAT = 146944;
constexpr int L_CSUM = 147968;
constexpr int L_SIDE = 148480;
constexpr int L_CS3 = 151168;
constexpr int L_SYNC = 152064;
constexpr int L_HT = 153600;
constexpr int L_RED = 0;
constexpr int L_X3 = 86016;

#define STAMPS 0
#define T2MODE 1
#define WOMODE 1
#define T1MODE 4
#define T2POS 0
#define T1SLEEP 16
#define T0KB 52

__device__ __forceinline__ void glds(unsigned voff, const void* sbase, unsigned ldsaddr) {
  asm volatile("s_mov_b32 m0, %2\n\ts_nop 0\n\tglobal_load_lds_dwordx4 %0, %1" ::"v"(voff), "s"(sbase), "s"(ldsaddr) : "memory");
}
__device__ __forceinline__ void glds2_sc1(unsigned voff, const void* sbase, unsigned ldsaddr) {
  asm volatile("s_mov_b32 m0, %2\n\ts_nop 0\n\tglobal_load_lds_dwordx4 %0, %1 sc1\n\tglobal_load_lds_dwordx4 %0, %1 offset:1024 sc1" ::"v"(voff), "s"(sbase), "s"(ldsaddr) : "memory");
}
template <int IMM> __device__ __forceinline__ void gldsi(unsigned voff, const void* sbase, unsigned lbase) {
  asm volatile("s_add_u32 m0, %2, %3\n\ts_nop 0\n\tglobal_load_lds_dwordx4 %0, %1" ::"v"(voff), "s"(sbase), "s"(lbase), "i"(IMM) : "memory", "scc");
}
template <int IMM> __device__ __forceinline__ void glds2i_sc1(unsigned voff, const void* sbase, unsigned lbase) {
  asm volatile("s_add_u32 m0, %2, %3\n\ts_nop 0\n\tglobal_load_lds_dwordx4 %0, %1 sc1\n\tglobal_load_lds_dwordx4 %0, %1 offset:1024 sc1" ::"v"(voff), "s"(sbase), "s"(lbase), "i"(IMM) : "memory", "scc");
}
template <int N, int IMM> __device__ __forceinline__ void gldsn_sc1(unsigned voff, const void* sbase, unsigned lbase) {
  static_assert(N >= 1 && N <= 4, "");
  if constexpr (N == 1) asm volatile("s_add_u32 m0, %2, %3\n\ts_nop 0\n\tglobal_load_lds_dwordx4 %0, %1 sc1" ::"v"(voff), "s"(sbase), "s"(lbase), "i"(IMM) : "memory", "scc");
  if constexpr (N == 2) asm volatile("s_add_u32 m0, %2, %3\n\ts_nop 0\n\tglobal_load_lds_dwordx4 %0, %1 sc1\n\tglobal_load_lds_dwordx4 %0, %1 offset:1024 sc1" ::"v"(voff), "s"(sbase), "s"(lbase), "i"(IMM) : "memory", "scc");
  if constexpr (N == 3) asm volatile("s_add_u32 m0, %2, %3\n\ts_nop 0\n\tglobal_load_lds_dwordx4 %0, %1 sc1\n\tglobal_load_lds_dwordx4 %0, %1 offset:1024 sc1\n\tglobal_load_lds_dwordx4 %0, %1 offset:2048 sc1" ::"v"(voff), "s"(sbase), "s"(lbase), "i"(IMM) : "memory", "scc");
  if constexpr (N == 4) asm volatile("s_add_u32 m0, %2, %3\n\ts_nop 0\n\tglobal_load_lds_dwordx4 %0, %1 sc1\n\tglobal_load_lds_dwordx4 %0, %1 offset:1024 sc1\n\tglobal_load_lds_dwordx4 %0, %1 offset:2048 sc1\n\tglobal_load_lds_dwordx4 %0, %1 offset:3072 sc1" ::"v"(voff), "s"(sbase), "s"(lbase), "i"(IMM) : "memory", "scc");
}
template <int IMM, int OFF> __device__ __forceinline__ void gldso_sc1(unsigned voff, const void* sbase, unsigned lbase) {
  asm volatile("s_add_u32 m0, %2, %3\n\ts_nop 0\n\tglobal_load_lds_dwordx4 %0, %1 offset:%4 sc1" ::"v"(voff), "s"(sbase), "s"(lbase), "i"(IMM), "i"(OFF) : "memory", "scc");
}
__device__ __forceinline__ void ldg_asm_sc1(v4u& d, unsigned voff, const void* sbase) {
  asm volatile("global_load_dwordx4 %0, %1, %2 sc1" : "=v"(d) : "v"(voff), "s"(sbase) : "memory");
}
__device__ __forceinline__ void ldg2_asm_sc1(v2u& d, unsigned voff, const void* sbase) {
  asm volatile("global_load_dwordx2 %0, %1, %2 sc1" : "=v"(d) : "v"(voff), "s"(sbase) : "memory");
}
template <int N> __device__ __forceinline__ void wait_for(v4u& a, v4u& b) { asm volatile("s_waitcnt vmcnt(%2)" : "+v"(a), "+v"(b) : "i"(N) : "memory"); }
template <int N> __device__ __forceinline__ void waitvm() { asm volatile("s_waitcnt vmcnt(%0)" ::"i"(N) : "memory"); }
__device__ __forceinline__ f32x2 pfma(f32x2 a, f32x2 b, f32x2 c) { return __builtin_elementwise_fma(a, b, c); }
__device__ __forceinline__ f32x2 sp2(float a) { return f32x2{a, a}; }
__device__ __forceinline__ f32x2 lo2(f32x4 v) { return __builtin_shufflevector(v, v, 0, 1); }
__device__ __forceinline__ f32x2 hi2(f32x4 v) { return __builtin_shufflevector(v, v, 2, 3); }
__device__ __forceinline__ unsigned pk2v(f32x2 v) { return __builtin_bit_cast(unsigned, __builtin_convertvector(v, bf16x2)); }
__device__ __forceinline__ f32x2 phi2(f32x2 te) {
  const f32x2 y = pfma(te, sp2(1.44269504088896f), sp2(-1.44269504088896e-6f));
  const f32x2 e = {__builtin_amdgcn_fmed3f(__builtin_amdgcn_exp2f(y.x), 0.f, 1.f), __builtin_amdgcn_fmed3f(__builtin_amdgcn_exp2f(y.y), 0.f, 1.f)};
  const f32x2 m = {fmaxf(te.x, 1e-6f), fmaxf(te.y, 1e-6f)};
  return m + e;
}
__device__ __forceinline__ unsigned geglu2(f32x2 ag, f32x2 au, f32x2 cg, f32x2 cu, float rstd, float mr) {
  const f32x2 gv = pfma(ag, sp2(rstd), cg * sp2(mr)), uv = pfma(au, sp2(rstd), cu * sp2(mr));
  const f32x2 z = gv * pfma(gv * gv, sp2(-0.1029432397f), sp2(-2.302208198f));
  const f32x2 d = {1.0f + __builtin_amdgcn_exp2f(z.x), 1.0f + __builtin_amdgcn_exp2f(z.y)};
  const f32x2 rc = {__builtin_amdgcn_rcpf(d.x), __builtin_amdgcn_rcpf(d.y)};
  return pk2v(gv * uv * rc);
}
template <int V> using IC = std::integral_constant<int, V>;
template <int I, int N, class F> __device__ __forceinline__ void static_for(F&& f) {
  if constexpr (I < N) { f(std::integral_constant<int, I>{}); static_for<I + 1, N>(f); }
}
#define LBAR() asm volatile("s_waitcnt lgkmcnt(0)\n\ts_barrier" ::: "memory")
#define DRAIN() asm volatile("s_waitcnt vmcnt(0)" ::: "memory")
#define SB() __builtin_amdgcn_sched_barrier(0)
template <int N> __device__ __forceinline__ void waitbar() { asm volatile("s_waitcnt vmcnt(%0) lgkmcnt(0)\n\ts_barrier" ::"i"(N) : "memory"); }

__device__ __forceinline__ unsigned pk2(float a, float b) {
  bf16x2 v = {(__bf16)a, (__bf16)b};
  return __builtin_bit_cast(unsigned, v);
}
__device__ __forceinline__ float bflo(unsigned u) { return __uint_as_float(u << 16); }
__device__ __forceinline__ float bfhi(unsigned u) { return __uint_as_float(u & 0xffff0000u); }
__device__ __forceinline__ f32x4 mfma(v4u a, v4u b, f32x4 c) {
  return __builtin_amdgcn_mfma_f32_16x16x32_bf16(__builtin_bit_cast(bf16x8, a), __builtin_bit_cast(bf16x8, b), c, 0, 0, 0);
}
__device__ __forceinline__ void mfma_acc(f32x4& acc, v4u a, v4u b) {
  asm volatile("v_mfma_f32_16x16x32_bf16 %0, %1, %2, %0" : "+v"(acc) : "v"(a), "v"(b));
}
__device__ __forceinline__ void mfma_first(f32x4& acc, v4u a, v4u b) {
  asm volatile("v_mfma_f32_16x16x32_bf16 %0, %1, %2, 0" : "=&v"(acc) : "v"(a), "v"(b));
}
template <bool FIRST> __device__ __forceinline__ void mfma_a(f32x4& acc, v4u a, v4u b) { if constexpr (FIRST) mfma_first(acc, a, b); else mfma_acc(acc, a, b); }
#define MFMA_FENCE() asm volatile("s_nop 7\n\ts_nop 1" ::: "memory")
__device__ __forceinline__ v4u lds128(LAS char* L, int off) { return *(const LAS v4u*)(L + off); }
__device__ __forceinline__ v4u ldtr(LAS char* L, int off, int d) {
  s16x4 a = __builtin_amdgcn_ds_read_tr16_b64_v4i16((LAS s16x4*)(L + off));
  s16x4 b = __builtin_amdgcn_ds_read_tr16_b64_v4i16((LAS s16x4*)(L + off + d));
  v2u x = __builtin_bit_cast(v2u, a), y = __builtin_bit_cast(v2u, b);
  return v4u{x.x, x.y, y.x, y.y};
}
__device__ __forceinline__ float phi_eps(float te) {
  const float e = __builtin_amdgcn_fmed3f(__builtin_amdgcn_exp2f((te - 1e-6f) * 1.44269504088896f), 0.f, 1.f);
  return fmaxf(te, 1e-6f) + e;
}
__device__ __forceinline__ float rowsum4(float v) {
  auto a = __builtin_amdgcn_permlane16_swap(__float_as_uint(v), __float_as_uint(v), false, false);
  const float u = __uint_as_float(a[0]) + __uint_as_float(a[1]);
  auto b = __builtin_amdgcn_permlane32_swap(__float_as_uint(u), __float_as_uint(u), false, false);
  return __uint_as_float(b[0]) + __uint_as_float(b[1]);
}
__device__ __forceinline__ v4u ldg_sc1(__amdgpu_buffer_rsrc_t rs, unsigned off) { return __builtin_amdgcn_raw_buffer_load_b128(rs, off, 0, 16); }
__device__ __forceinline__ v2u ldg2_sc1(__amdgpu_buffer_rsrc_t rs, unsigned off) { return __builtin_amdgcn_raw_buffer_load_b64(rs, off, 0, 16); }
__device__ __forceinline__ unsigned ldg1_sc1(__amdgpu_buffer_rsrc_t rs, unsigned off) { return __builtin_amdgcn_raw_buffer_load_b32(rs, off, 0, 16); }
__device__ __forceinline__ void stg_sc1(__amdgpu_buffer_rsrc_t rs, unsigned off, v4u v) { __builtin_amdgcn_raw_buffer_store_b128(v, rs, off, 0, 16); }
__device__ __forceinline__ void stg2_sc1(__amdgpu_buffer_rsrc_t rs, unsigned off, v2u v) { __builtin_amdgcn_raw_buffer_store_b64(v, rs, off, 0, 16); }
__device__ __forceinline__ void stg1_sc1(__amdgpu_buffer_rsrc_t rs, unsigned off, unsigned v) { __builtin_amdgcn_raw_buffer_store_b32(v, rs, off, 0, 16); }

__device__ __forceinline__ void wait16(char* ws, unsigned off, unsigned tag, int lane) {
  gu32* f = (gu32*)(ws + off) + (lane & 15);
  for (unsigned sp = 0; sp < (1u << 15); ++sp) {
    unsigned v = __hip_atomic_load(f, RLX_AGENT);
    if (__all(v == tag)) return;
    __builtin_amdgcn_s_sleep(1);
  }
  if (lane == 0) __hip_atomic_store((gu32*)(ws + WS_TMO), 1u, RLX_AGENT);
}
__device__ __forceinline__ void wait32(char* ws, unsigned off, unsigned tag, int lane) {
  gu32* f = (gu32*)(ws + off) + (lane & 31);
  for (unsigned sp = 0; sp < (1u << 15); ++sp) {
    unsigned v = __hip_atomic_load(f, RLX_AGENT);
    if (__all(v == tag)) return;
    __builtin_amdgcn_s_sleep(1);
  }
  if (lane == 0) __hip_atomic_store((gu32*)(ws + WS_TMO), 1u, RLX_AGENT);
}
typedef LAS unsigned lu32;
__device__ __forceinline__ unsigned lds_add(LAS char* L, int word) { return __hip_atomic_fetch_add((lu32*)(L + L_SYNC) + word, 1u, __ATOMIC_RELAXED, __HIP_MEMORY_SCOPE_WORKGROUP); }
__device__ __forceinline__ void lds_spin(LAS char* L, int word, unsigned want) {
  for (unsigned sp = 0; sp < (1u << 20); ++sp) {
    if (__builtin_amdgcn_readfirstlane(__hip_atomic_load((lu32*)(L + L_SYNC) + word, __ATOMIC_RELAXED, __HIP_MEMORY_SCOPE_WORKGROUP)) >= want) break;
  }
  asm volatile("" ::: "memory");
}
__device__ __forceinline__ void group_sync(LAS char* L, int word, int l) {
  asm volatile("s_waitcnt lgkmcnt(0)" ::: "memory");
  if (l == 0) lds_add(L, word);
  lds_spin(L, word, 4);
}
__device__ __forceinline__ void touch(unsigned voff, const void* sbase) {
  asm volatile("s_mov_b32 m0, %2\n\ts_nop 0\n\tglobal_load_lds_dword %0, %1" ::"v"(voff), "s"(sbase), "s"(L_SYNC + 64) : "memory");
}
__device__ __forceinline__ void touch64(const void* addr) {
  asm volatile("s_mov_b32 m0, %1\n\ts_nop 0\n\tglobal_load_lds_dword %0, off" ::"v"(addr), "s"(L_SYNC + 64) : "memory");
}
__device__ __forceinline__ void set_flag(char* ws, unsigned off, unsigned tag) { __hip_atomic_store((gu32*)(ws + off), tag, RLX_AGENT); }
#define STAMPW(i, wv) do {} while (0)
#define STAMP(i) STAMPW(i, 0)

__device__ __forceinline__ void splitk_exchange(LAS char* L, f32x4 (&acc)[5][2], f32x4 (&own)[3], int th, int kq, int l) {
  const int wb = L_RED + (th * 40 + kq) * 1024 + l * 16;
#pragma unroll
  for (int s = 0; s < 2; ++s)
#pragma unroll
    for (int T = 0; T < 5; ++T) *(LAS f32x4*)(L + wb + (s * 5 + T) * 4096) = acc[T][s];
  LBAR();
  const int rb = L_RED + (th * 2 + (kq & 1)) * 5 * 4096 + l * 16, t0 = rb + (kq >> 1) * 8192;
  f32x4 v[3][4];
#pragma unroll
  for (int k = 0; k < 4; ++k) {
    v[0][k] = *(const LAS f32x4*)(L + t0 + k * 1024);
    v[1][k] = *(const LAS f32x4*)(L + t0 + 4096 + k * 1024);
    v[2][k] = *(const LAS f32x4*)(L + rb + 4 * 4096 + k * 1024);
  }
#pragma unroll
  for (int i = 0; i < 3; ++i) own[i] = (v[i][0] + v[i][1]) + (v[i][2] + v[i][3]);
}

#define P4XW 4
constexpr int P4_XW = P4XW;
constexpr int P4_PRE = 4;
#define P4EARLY 10
constexpr int P4_EARLY = P4EARLY;
constexpr int p4_g(int it, int n) { return it == 4 ? n : it == 6 ? 2 * n : 0; }
constexpr int p4_x(int it) { return it + P4_XW < 14 ? 2 : 0; }
constexpr int p4_xwait(int st, int n) {
  int y = 0;
  if (st < P4_XW) { y = 2 * (P4_XW - 1 - st); for (int j = 0; j < st; ++j) y += p4_x(j) + p4_g(j, n); }
  else { y = p4_g(st - P4_XW, n); for (int j = st - P4_XW + 1; j < st; ++j) y += p4_x(j) + p4_g(j, n); }
  return y;
}
constexpr int p4_wwait(int st, int n) {
  int y = st == 10 ? n : 0;
  for (int j = 7; j < st; ++j) y += p4_x(j);
  return y;
}
template <int TH, int stg> __device__ __forceinline__ void p4_issue(char* ws, int cs, int kq, int l) {
  constexpr int N = TH == 0 ? 3 : 2;
  const int kb = kq < 2 ? 14 * kq : 28 + 13 * (kq - 2);
  gldsn_sc1<N, (stg % 7) * 20480>(l * 16, ws + WS_W4 + (size_t)cs * 270 * 1024 + TH * 3072 + (size_t)min(kb + stg, 53) * 5120, kq * 5120 + TH * 3072);
}
template <int J> __device__ __forceinline__ void p4_pre_piece(const char* src, unsigned l16, int th, unsigned lbase) {
  constexpr int q0 = 2 * J, q1 = (2 * J + 1 < 35) ? 2 * J + 1 : 34;
  constexpr int f0 = (q0 / 5) * 20480 + (q0 % 5) * 1024, f1 = (q1 / 5) * 20480 + (q1 % 5) * 1024;
  asm volatile("s_mov_b32 m0, %2\n\ts_nop 0\n\tglobal_load_lds_dwordx4 %0, %1 sc1" ::"v"(l16), "s"(src + (th ? q1 : q0) * 1024), "s"(lbase + (th ? f1 : f0)) : "memory");
}
template <int TH>
__device__ __forceinline__ void p4_main(LAS char* L, char* ws, int r, int r2, int cs, int kq, int w, int l, unsigned tag, f32x4 (&acc)[5][2]) {
  constexpr int N = TH == 0 ? 3 : 2;
  const int kb = kq < 2 ? 14 * kq : 28 + 13 * (kq - 2);
  const int nk = kq < 2 ? 14 : 13;
  const unsigned l16 = l * 16;
  auto issue = [&](auto STG) { p4_issue<TH, STG>(ws, cs, kq, l); };
  int wbs[3];
  wbs[0] = kq * 5120 + l16; wbs[1] = wbs[0] + 3 * 20480; wbs[2] = wbs[0] + 6 * 20480;
  asm volatile("" : "+v"(wbs[0]), "+v"(wbs[1]), "+v"(wbs[2]));
  {
    const char* w4src = ws + WS_W4 + (size_t)cs * 270 * 1024 + (size_t)kb * 5120;
    static_for<P4_EARLY, 18>([&](auto J) { p4_pre_piece<J>(w4src, l16, TH, kq * 5120); });
    static_assert(P4_EARLY >= 10, "");
  }
  v4u wp[P4_PRE][5];
  static_for<0, P4_PRE>([&](auto ST) { static_for<0, 5>([&](auto I) { wp[ST][I] = lds128(L, wbs[ST / 3] + (ST % 3) * 20480 + I * 1024); }); });
  LBAR();
  static_for<7, 7 + P4_PRE>([&](auto ST) { issue(ST); });
  STAMP(38);
  if (w == 0) wait16(ws, WS_FA + r * 256, tag, l);
  STAMP(39);
  waitbar<P4_PRE * N>();
  STAMP(13);
  v4u xf[14][2];
  const char* xa = ws + WS_ACT + (size_t)(4 * r2 + 2 * TH) * 54 * 1024;
  auto ldx = [&](auto K, auto S) {
    constexpr int k = K, s = S;
    ldg_asm_sc1(xf[k][s], l16, xa + (size_t)(s * 54 + min(kb + k, 53)) * 1024);
  };
  static_for<0, P4_XW>([&](auto K) { ldx(K, IC<0>{}); ldx(K, IC<1>{}); });
  v4u wf[2][5];
  auto rd1 = [&](auto STG, auto I) {
    constexpr int stg = STG, T = I, sl7 = stg % 7;
    wf[stg & 1][T] = lds128(L, wbs[sl7 / 3] + (sl7 % 3) * 20480 + T * 1024);
  };
  static_for<0, 14>([&](auto ST) {
    constexpr int st = ST;
    if constexpr (st == 4 || st == 6) LBAR();
    if constexpr (st == 10 || st == 12) waitbar<p4_wwait(st, N)>();
    if constexpr (st == 1) STAMP(40);
    if constexpr (st == 4) STAMP(41);
    if constexpr (st == 7) STAMP(42);
    if constexpr (st == 10) STAMP(43);
    if constexpr (st == 13) STAMP(44);
    wait_for<p4_xwait(st, N)>(xf[st][0], xf[st][1]);
    SB();
    if (st < 13 || st < nk) {
      static_for<0, 10>([&](auto I) {
        constexpr int i = I;
        if constexpr (st < P4_PRE) mfma_a<(st == 0)>(acc[i >> 1][i & 1], wp[st][i >> 1], xf[st][i & 1]);
        else mfma_a<false>(acc[i >> 1][i & 1], wf[st & 1][i >> 1], xf[st][i & 1]);
        if constexpr (st + P4_XW < 14 && (i == 0 || i == 2)) ldx(IC<st + P4_XW>{}, IC<(i >> 1)>{});
        if constexpr (st == 4 && i == 4) issue(IC<11>{});
        if constexpr (st == 6 && (i == 4 || i == 7)) issue(IC<12 + (i == 7)>{});
        if constexpr (st >= P4_PRE - 1 && st < 13 && i >= 2 && i < 7) rd1(IC<st + 1>{}, IC<i - 2>{});
        SB();
      });
    }
  });
  MFMA_FENCE();
}

template <int NT> struct Frag3 { v4u x[2], g[NT], u[NT]; };

template <int NT, int TILE0, bool LOADER>
__device__ __forceinline__ void p3_body(LAS char* L, char* ws, __amdgpu_buffer_rsrc_t rs, int r, int p, int cs, int w, int l, unsigned tag) {
  const int c = l & 15, g = l >> 4, tg = w & 3;
  const unsigned l16 = l * 16;
  constexpr int NWP = LOADER ? 3 : 4, NP = LOADER ? 7 : 4;
  const int wp0 = LOADER ? 16 + 3 * tg : 4 * tg;
  const char* wsrc = ws + WS_W3 + (size_t)p * 280 * 1024 + wp0 * 1024;
  const char* hx = ws + WS_H + (size_t)(8 * r + 2 * tg) * 20 * 1024;
  const unsigned lw = wp0 * 1024, lx = L_X3 + tg * 4096;
  auto piece = [&](auto STG, auto Q) {
    constexpr int stg = STG, q = Q, slot = stg % 3;
    if constexpr (q < NWP) gldso_sc1<slot * 28672, q * 1024>(l16, wsrc + stg * 28672, lw);
    else gldso_sc1<slot * 16384 + ((q - NWP) >> 1) * 2048, ((q - NWP) & 1) * 1024>(l16, hx + stg * 2048 + ((q - NWP) >> 1) * 20480, lx);
  };
  static_for<0, 3>([&](auto ST) { static_for<0, NWP>([&](auto Q) { piece(ST, Q); }); });
  if constexpr (!LOADER) {
    const int ct = min(w * 64 + l, 223), cm = ct >= 112;
    unsigned csv[20];
#pragma unroll
    for (int i = 0; i < 20; ++i) csv[i] = ldg1_sc1(rs, WS_CS + (p * 40 + 2 * i + cm) * 448 + (ct - 112 * cm) * 4);
    float s = 0.f;
#pragma unroll
    for (int i = 0; i < 20; ++i) s += __uint_as_float(csv[i]);
    *(LAS float*)(L + (w * 64 + l < 224 ? L_CS3 + ct * 4 : L_SYNC + 512)) = s;
  }
  STAMP(32);
  if (w == 7) { wait16(ws, WS_FH + r * 256, tag, l); STAMPW(33, 7); }
  DRAIN();
  LBAR();
  STAMP(10);
  if constexpr (LOADER) static_for<0, 3>([&](auto ST) { static_for<NWP, NP>([&](auto Q) { piece(ST, Q); }); });
  v2u stp[2][4];
#pragma unroll
  for (int s = 0; s < 2; ++s)
#pragma unroll
    for (int i = 0; i < 4; ++i) ldg2_asm_sc1(stp[s][i], (4 * g + i) * 128 + c * 8, ws + WS_HS + (8 * r + 2 * tg + s) * 16 * 128);

  f32x4 ag[NT][2], au[NT][2];
  float rstd[2], mr[2];
  const int xo = L_X3 + tg * 4096 + l16;
  int wb0 = TILE0 * 1024 + l16, wb2 = wb0 + 2 * 28672;
  asm volatile("" : "+v"(wb0), "+v"(wb2));
  auto rd1 = [&](Frag3<NT>& f, auto SLOT, auto KS, auto I) {
    constexpr int slot = SLOT, ks = KS, i = I;
    if constexpr (i < 2) f.x[i] = lds128(L, xo + slot * 16384 + ks * 1024 + i * 2048);
    else {
      constexpr int j = i - 2, t = j >> 1, u = j & 1, off = ks * 14336 + u * 7168 + t * 1024;
      const v4u v = slot < 2 ? lds128(L, wb0 + slot * 28672 + off) : lds128(L, wb2 + off);
      if constexpr (u == 0) f.g[t] = v; else f.u[t] = v;
    }
  };
  auto mma1 = [&](const Frag3<NT>& f, auto I, auto FIRST) {
    constexpr int i = I, t = i >> 2, u = (i >> 1) & 1, s = i & 1;
    if constexpr (u == 0) mfma_a<FIRST>(ag[t][s], f.g[t], f.x[s]); else mfma_a<FIRST>(au[t][s], f.u[t], f.x[s]);
  };
  constexpr int NM = 4 * NT, NR = 2 + 2 * NT;
  Frag3<NT> A, B;
  if constexpr (LOADER) waitbar<8 + 8>(); else LBAR();
  STAMP(35);
  static_for<0, NR>([&](auto I) { rd1(A, IC<0>{}, IC<0>{}, I); });
  static_for<0, 10>([&](auto ST) {
    constexpr int st = ST, slot = st % 3, nslot = (st + 1) % 3;
    static_for<0, NM>([&](auto I) {
      constexpr int i = I;
      mma1(A, I, IC<(st == 0)>{});
      if constexpr (i < NR) rd1(B, IC<slot>{}, IC<1>{}, I);
      SB();
    });
    if constexpr (st < 9) {
      if constexpr (st == 0) { if constexpr (LOADER) waitbar<4 + 8>(); else LBAR(); }
      else if constexpr (st == 1) { if constexpr (LOADER) waitbar<NP>(); else LBAR(); }
      else if constexpr (st < 8) waitbar<NP>(); else waitbar<0>();
    }
    if constexpr (st == 4) {
      asm volatile("" : "+v"(stp[0][0]), "+v"(stp[0][1]), "+v"(stp[0][2]), "+v"(stp[0][3]), "+v"(stp[1][0]), "+v"(stp[1][1]), "+v"(stp[1][2]), "+v"(stp[1][3]));
#pragma unroll
      for (int s = 0; s < 2; ++s) {
        float sm = 0.f, sq = 0.f;
#pragma unroll
        for (int i = 0; i < 4; ++i) { sm += __uint_as_float(stp[s][i].x); sq += __uint_as_float(stp[s][i].y); }
        sm = rowsum4(sm); sq = rowsum4(sq);
        const float mean = sm * (1.0f / 640.0f);
        rstd[s] = __builtin_amdgcn_rsqf(fmaxf(sq * (1.0f / 640.0f) - mean * mean, 0.f) + 1e-5f);
        mr[s] = -mean * rstd[s];
      }
      asm volatile("" : "+v"(rstd[0]), "+v"(rstd[1]), "+v"(mr[0]), "+v"(mr[1]));
    }
    if constexpr (st == 2) STAMP(45);
    if constexpr (st == 5) STAMP(46);
    if constexpr (st == 8) STAMP(47);
    static_for<0, NM>([&](auto I) {
      constexpr int i = I;
      mma1(B, I, IC<0>{});
      if constexpr (LOADER) { if constexpr (st <= 6 && i < NP) piece(IC<st + 3>{}, I); }
      else { if constexpr (st <= 6 && (i % 3) == 0 && i / 3 < NP) piece(IC<st + 3>{}, IC<i / 3>{}); }
      if constexpr (st < 9 && i < NR) rd1(A, IC<nslot>{}, IC<0>{}, I);
      SB();
    });
  });
  MFMA_FENCE();
  STAMP(11);
  LBAR();
  const int kq4 = w >> 1, th4 = w & 1;
  const char* w4src = ws + WS_W4 + (size_t)(cs & 7) * 270 * 1024 + (size_t)(kq4 < 2 ? 14 * kq4 : 28 + 13 * (kq4 - 2)) * 5120;
  const unsigned w4l = kq4 * 5120;
  f32x4 cgv[NT], cuv[NT];
#pragma unroll
  for (int t = 0; t < NT; ++t) {
    cgv[t] = *(const LAS f32x4*)(L + L_CS3 + ((TILE0 + t) * 16 + 4 * g) * 4);
    cuv[t] = *(const LAS f32x4*)(L + L_CS3 + (112 + (TILE0 + t) * 16 + 4 * g) * 4);
  }
  static_for<0, 2>([&](auto S) {
    constexpr int s = S;
    unsigned y[NT][2];
    const unsigned ab = WS_ACT + (8 * r + 2 * tg + s) * 54 * 1024 + l16;
    static_for<0, NT>([&](auto T) {
      constexpr int t = T, step = s * NT + t, j0 = step < NT ? step * P4_EARLY / NT : P4_EARLY, j1 = step < NT ? (step + 1) * P4_EARLY / NT : P4_EARLY;
      y[t][0] = geglu2(lo2(ag[t][s]), lo2(au[t][s]), lo2(cgv[t]), lo2(cuv[t]), rstd[s], mr[s]);
      static_for<j0, (j0 + j1 + 1) / 2>([&](auto J) { p4_pre_piece<J>(w4src, l16, th4, w4l); });
      y[t][1] = geglu2(hi2(ag[t][s]), hi2(au[t][s]), hi2(cgv[t]), hi2(cuv[t]), rstd[s], mr[s]);
      static_for<(j0 + j1 + 1) / 2, j1>([&](auto J) { p4_pre_piece<J>(w4src, l16, th4, w4l); });
      if constexpr (t & 1) {
        if constexpr (TILE0 == 0) stg_sc1(rs, p < 15 ? ab + (3 * p + (t >> 1)) * 1024 : 0x7FFFFFF0u, v4u{y[t - 1][0], y[t - 1][1], y[t][0], y[t][1]});
        else stg_sc1(rs, ab + (p < 15 ? 3 * p + 2 : 45) * 1024, v4u{y[t - 1][0], y[t - 1][1], y[t][0], y[t][1]});
      }
    });
    if constexpr (TILE0 != 0) stg2_sc1(rs, ab + (46 + (p >> 1)) * 1024 + (p & 1) * 8, v2u{y[NT - 1][0], y[NT - 1][1]});
  });
}

struct FragKV { v4u x[2], w[5]; };

__global__ __launch_bounds__(512, 2) void fused_block(const u16* __restrict__ x, const u16* __restrict__ wqkv, const u16* __restrict__ wo,
                                                      const u16* __restrict__ wg, const u16* __restrict__ wu, const u16* __restrict__ wd,
                                                      u16* __restrict__ out, char* __restrict__ ws) {
  extern __shared__ __attribute__((aligned(16))) char smem_[];
  LAS char* L = (LAS char*)smem_;
  const int tid = threadIdx.x, l = tid & 63, c = l & 15, g = l >> 4;
  const int w = __builtin_amdgcn_readfirstlane(tid >> 6);
  const int b = blockIdx.x, xcd = b & 7, jj = b >> 3, rg = xcd >> 1, cgp = xcd & 1;
  const int r = 4 * rg + (jj >> 3), p = 8 * cgp + (jj & 7);
  const int r2 = 4 * xcd + (jj >> 3), cs = jj & 7;
  const int r3 = 8 * (xcd >> 2) + (jj >> 2), c3 = 4 * (xcd & 3) + (jj & 3);
  const __amdgpu_buffer_rsrc_t rs = __builtin_amdgcn_make_buffer_rsrc(ws, 0, WS_END, 0x00020000);
  const v4u ONES = {0x3F803F80u, 0x3F803F80u, 0x3F803F80u, 0x3F803F80u};
  const int q4 = (l & 15) >> 2, pp = l & 3;
  STAMP(0);

  unsigned tag;
  constexpr int S1 = 26624;
  const int xo0 = c * 128 + ((g ^ (c & 7)) << 4);
  const int wrow = 16 * (g >> 1) + 4 * (g & 1) + q4;
  const int kvo = 16384 + wrow * 160 + 8 * pp;
  auto kv_product = [&](int w) {
    const int Ta = w < 2 ? 0 : w < 5 ? 1 : 2;
    const int Tb = (w == 0 || w == 2 || w == 7) ? 2 : (w == 4 || w == 6) ? 4 : 3;
    v4u A[4], B[4];
#pragma unroll
    for (int tg = 0; tg < 4; ++tg) {
      A[tg] = lds128(L, L_KVF + (tg * 5 + Ta) * 1024 + l * 16);
      B[tg] = lds128(L, L_KVF + (tg * 5 + Tb) * 1024 + l * 16);
    }
    f32x4 d = {0, 0, 0, 0};
    const bool one = Tb == 2 && c < 8;
#pragma unroll
    for (int tg = 0; tg < 4; ++tg) d = mfma(A[tg], one ? ONES : B[tg], d);
    const int kc = 16 * Ta + 4 * g;
    const int vc = 16 * Tb + c - 40;
    const int hk = kc >= 20;
    const bool iskv = kc < 40 && vc >= 0 && hk == (vc >= 20), isz = kc < 40 && vc < 0 && c == 0;
    *(LAS f32x4*)(L + (iskv ? L_KVC + (vc * 20 + kc - 20 * hk) * 4 : isz ? L_KVC + (800 + kc) * 4 : L_SYNC + 512 + (l & 31) * 16)) = d;
  };

  if (w < 4) {
    {
      const char* pc;
      asm volatile("s_getpc_b64 %0" : "=s"(pc));
      const int n = (jj * 4 + w) * 4 + (l & 3);
      if (l < 4) touch64(pc + min(n * 128, T0KB * 1024 - 128));
    }
    auto hint1 = [&](int stg, int n) {
      const int idx = jj * 36 + min(n, 35);
      const int wr_ = idx / 10, pc = idx - wr_ * 10, sec = pc / 5;
      touch64(idx < 640 ? (const char*)wqkv + (64 * stg + wr_) * 3840 + (1 + sec) * 1280 + cgp * 640 + (pc - sec * 5) * 128
                        : (const char*)x + ((size_t)rg * 512 + (idx - 640)) * 1280 + stg * 128);
    };
    {
      __builtin_amdgcn_s_sleep(T1SLEEP);
      const int sl_ = l / 9;
      if (l < 63) hint1(2 + sl_, 9 * w + (l - 9 * sl_));
    }
    if (l == 0) asm volatile("s_mov_b32 m0, %1\n\ts_nop 0\n\tglobal_load_lds_dword %0, off sc1" ::"v"(ws + WS_EPOCH), "s"(L_SYNC + 32) : "memory");
    *(LAS v4u*)(L + L_BD + tid * 16) = v4u{0, 0, 0, 0};
    *(LAS v4u*)(L + L_BD + 4096 + tid * 16) = v4u{0, 0, 0, 0};
    if (tid < 2) *(LAS v4u*)(L + L_SYNC + tid * 16) = v4u{0, 0, 0, 0};
    auto hint2 = [&](int i) {
      const int n = 96 * w + 16 * i + (l & 15);
      const int li = b * 228 + min(n, 227);
      const char* aw_ = li < 6400 ? (const char*)wo + li * 128 : li < 23680 ? (const char*)wg + (li - 6400) * 128
                      : li < 40960 ? (const char*)wu + (li - 23680) * 128 : (const char*)wd + min(li - 40960, 17279) * 128;
      const int m = n - 228;
      const unsigned o5 = m < 32 ? WS_O + ((8 * r + (m >> 2)) * 20 + 16 + (p >> 2)) * 1024 + ((p & 3) >> 1) * 512 + (m & 3) * 128
                        : m < 64 ? WS_H + ((4 * r2 + ((m - 32) >> 3)) * 20 + 16 + (cs >> 1)) * 1024 + (m & 7) * 128
                                 : WS_ACT + ((8 * r3 + (min(m - 64, 63) >> 3)) * 54 + 46 + (c3 >> 1)) * 1024 + (m & 7) * 128;
      const char* a = n < 228 ? aw_ : ws + o5;
      if (l < 16) touch64(a);
    };
    STAMP(1);
    f32x4 acc[2][5];
    {
      const int xw = w * 32 * 128;
      int xbs[3][2], kbs[3];
#pragma unroll
      for (int q = 0; q < 3; ++q) { xbs[q][0] = xw + xo0 + 2 * q * S1; xbs[q][1] = xw + (xo0 ^ 64) + 2 * q * S1; kbs[q] = kvo + 2 * q * S1; }
      asm volatile("" : "+v"(xbs[0][0]), "+v"(xbs[0][1]), "+v"(xbs[1][0]), "+v"(xbs[1][1]), "+v"(xbs[2][0]), "+v"(xbs[2][1]), "+v"(kbs[0]), "+v"(kbs[1]), "+v"(kbs[2]));
      auto rd1 = [&](FragKV& f, auto SLOT, auto KS, auto I) {
        constexpr int slot = SLOT, ks = KS, i = I;
        if constexpr (i < 2) f.x[i] = lds128(L, xbs[slot >> 1][ks] + (slot & 1) * S1 + i * 2048);
        else {
          constexpr int j = i - 2, t = j >> 1, h = j & 1;
          const v2u v = __builtin_bit_cast(v2u, __builtin_amdgcn_ds_read_tr16_b64_v4i16((LAS s16x4*)(L + kbs[slot >> 1] + (slot & 1) * S1 + ks * 32 * 160 + t * 32 + h * 8 * 160)));
          if constexpr (h == 0) { f.w[t].x = v.x; f.w[t].y = v.y; } else { f.w[t].z = v.x; f.w[t].w = v.y; }
        }
      };
      auto mma1 = [&](const FragKV& f, auto I, auto FIRST) { constexpr int i = I, t = i >> 1, s = i & 1; mfma_a<FIRST>(acc[s][t], f.x[s], f.w[t]); };
      FragKV A, B;
      LBAR();
      static_for<0, 12>([&](auto I) { rd1(A, IC<0>{}, IC<0>{}, I); });
      static_for<0, 10>([&](auto ST) {
        constexpr int st = ST;
        static_for<0, 10>([&](auto I) {
          constexpr int i = I;
          mma1(A, I, IC<(st == 0)>{});
          if constexpr (i < 6) { rd1(B, IC<(st % 5)>{}, IC<1>{}, IC<2 * i>{}); rd1(B, IC<(st % 5)>{}, IC<1>{}, IC<2 * i + 1>{}); }
          SB();
        });
        if constexpr (st < 9) LBAR();
        if constexpr (st == 0) STAMP(48);
        if constexpr (st == 3) STAMP(49);
        if constexpr (st == 6) STAMP(50);
        static_for<0, 10>([&](auto I) {
          constexpr int i = I;
          mma1(B, I, IC<0>{});
          if constexpr (st >= 2 && st < 8 && i == 8) hint2(st - 2);
          if constexpr (st < 9 && i < 6) { rd1(A, IC<((st + 1) % 5)>{}, IC<0>{}, IC<2 * i>{}); rd1(A, IC<((st + 1) % 5)>{}, IC<0>{}, IC<2 * i + 1>{}); }
          SB();
        });
      });
      MFMA_FENCE();
    }
    STAMP(2);
    __builtin_amdgcn_s_setprio(2);
    waitvm<(T2MODE == 1 ? 6 : 0)>();
    LBAR();
    STAMP(16);
    tag = __builtin_amdgcn_readfirstlane(*(const LAS unsigned*)(L + L_SYNC + 32)) + 1u;
    {
      f32x4 rstd[2], mr[2];
#pragma unroll
      for (int s = 0; s < 2; ++s) {
        rstd[s] = *(const LAS f32x4*)(L + L_STAT + (32 * w + 16 * s + 4 * g) * 4);
        mr[s] = *(const LAS f32x4*)(L + L_STAT + 512 + (32 * w + 16 * s + 4 * g) * 4);
      }
      float cw[5];
#pragma unroll
      for (int t = 0; t < 5; ++t) cw[t] = *(const LAS float*)(L + L_CSUM + (16 * t + c) * 4);
#pragma unroll
      for (int t = 0; t < 5; ++t) {
        unsigned o[4];
#pragma unroll
        for (int s = 0; s < 2; ++s)
#pragma unroll
          for (int h = 0; h < 2; ++h) {
            const f32x2 a2 = h ? hi2(acc[s][t]) : lo2(acc[s][t]), r2_ = h ? hi2(rstd[s]) : lo2(rstd[s]), m2 = h ? hi2(mr[s]) : lo2(mr[s]);
            f32x2 y;
            if (t < 2) y = phi2(pfma(a2, r2_, pfma(m2, sp2(cw[t]), sp2(1e-6f))));
            else if (t == 2) {
              const f32x2 te = pfma(a2, r2_, pfma(m2, sp2(cw[t]), sp2(1e-6f))), ph = phi2(te), v = te - sp2(1e-6f);
              y = c < 8 ? ph : v;
            } else y = pfma(a2, r2_, m2 * sp2(cw[t]));
            o[2 * s + h] = pk2v(y);
          }
        *(LAS v4u*)(L + L_KVF + (w * 5 + t) * 1024 + l * 16) = v4u{o[0], o[1], o[2], o[3]};
      }
    }
    STAMP(17);
    group_sync(L, 5, l);
    kv_product(w);
    kv_product(w + 4);
    group_sync(L, 6, l);
    STAMP(19);
    if (w == 0) {
      v4u v[4];
#pragma unroll
      for (int i = 0; i < 4; ++i) v[i] = lds128(L, L_KVC + min(i * 64 + l, 209) * 16);
#pragma unroll
      for (int i = 0; i < 4; ++i) stg_sc1(rs, i * 64 + l < 210 ? WS_KVP + (p * 16 + r) * KVP_STRIDE + (i * 64 + l) * 16 : 0x7FFFFFF0u, v[i]);
      STAMP(20);
      DRAIN();
      if (l == 0) set_flag(ws, WS_FKV + p * 256 + r * 4, tag);
      STAMP(3);
    }
    __builtin_amdgcn_s_setprio(0);
    const int f0 = min(7 * c3, 101);
    const int u3 = min(r3 + 16 * max(w - 1, 0), 39), ks3 = u3 >> 1, mat = u3 & 1;
    const int ks4 = min(r2 + 32 * max(w - 1, 0), 53);
    const unsigned area = L_KVF + max(w - 1, 0) * 12288;
    if (w > 0) {
      const char* w3 = (const char*)(mat ? wu : wg);
#pragma unroll
      for (int i = 0; i < 7; ++i) {
        const int n = 64 * i + l, pos = n / 14, ch = n - pos * 14;
        const int feat = ks3 < 16 ? 80 * (ks3 >> 1) + 32 * (ks3 & 1) + pos : 160 * (ks3 - 16) + 64 + (pos & 15) + 80 * (pos >> 4);
        glds(feat * 3456 + (f0 * 16 + 8 * ch) * 2, w3, area + i * 1024);
      }
      if (w < 3) {
#pragma unroll
        for (int i = 0; i < 5; ++i) {
          const int n = 64 * i + l, pos = n / 10, ch = n - pos * 10, t = pos >> 4;
          const int sp = ks4 < 45 ? ks4 / 3 : ks4 == 45 ? 15 : 2 * (ks4 - 46) + t;
          const int lt = ks4 < 45 ? 2 * (ks4 % 3) + t : ks4 == 45 ? 4 + t : 6;
          const int row = 16 * (min(7 * sp, 101) + lt) + (pos & 15);
          glds(row * 1280 + (80 * cs + 8 * ch) * 2, wd, area + 7168 + i * 1024);
        }
      }
    }
    auto wo_pieces = [&](int i0, int i1) {
#pragma unroll 1
      for (int i = i0 + w; i < i1; i += 4) {
        const int n = 64 * i + l, pos = n / 10, ch2 = n - pos * 10;
        const int ks = pos >> 5, p5 = pos & 31;
        const int t = p5 >> 4, gg = (p5 >> 2) & 3, qq = p5 & 3;
        const int feat = ks < 16 ? 40 * ks + p5 : 40 * (4 * (ks - 16) + 2 * (gg >> 1) + t) + 32 + 4 * (gg & 1) + qq;
        glds(feat * 1280 + (80 * cs + 8 * ch2) * 2, wo, i * 1024);
      }
    };
    if (w > 0) {
      if (w < 3) waitvm<5>(); else waitvm<0>();
      {
        const int tb = area + (4 * g + q4) * 224 + 8 * pp;
        v4u fr[7];
#pragma unroll
        for (int t = 0; t < 7; ++t) fr[t] = ldtr(L, tb + t * 32, 16 * 224);
        const bool ok = r3 + 16 * (w - 1) < 40;
        const unsigned ob = ok ? WS_W3 + ((c3 * 20 + ks3) * 14 + mat * 7) * 1024 + l * 16 : 0x7FFF0000u;
        float csel0 = 0.f, csel1 = 0.f;
#pragma unroll
        for (int t = 0; t < 7; ++t) {
          stg_sc1(rs, ob + t * 1024, fr[t]);
          const f32x4 cs_ = mfma(ONES, fr[t], f32x4{0, 0, 0, 0});
          if (t < 4) csel0 = (g == t) ? cs_[0] : csel0; else csel1 = (g == t - 4) ? cs_[0] : csel1;
        }
        const unsigned cb = ok ? WS_CS + (c3 * 40 + u3) * 448 + l * 4 : 0x7FFF0000u;
        stg1_sc1(rs, cb, __float_as_uint(csel0));
        stg1_sc1(rs, l < 48 ? cb + 256 : 0x7FFF0000u, __float_as_uint(csel1));
      }
      if (w < 3) {
        waitvm<9>();
        const int tb = area + 7168 + (4 * g + q4) * 160 + 8 * pp;
        v4u fr[5];
#pragma unroll
        for (int t = 0; t < 5; ++t) fr[t] = ldtr(L, tb + t * 32, 16 * 160);
        const unsigned ob = (r2 + 32 * (w - 1) < 54) ? WS_W4 + (cs * 54 + ks4) * 5 * 1024 + l * 16 : 0x7FFF0000u;
#pragma unroll
        for (int t = 0; t < 5; ++t) stg_sc1(rs, ob + t * 1024, fr[t]);
      }
    }
    lds_spin(L, 4, 4);
    wo_pieces(0, 100);
    DRAIN();
    if (l == 0 && lds_add(L, 3) == 3) { set_flag(ws, WS_FC + c3 * 256 + r3 * 4, tag); set_flag(ws, WS_FD + cs * 256 + r2 * 4, tag); }
    STAMPW(23, 1);
  } else {
    const int tw = w - 4;
    unsigned vx[4], vw[3];
#pragma unroll
    for (int i = 0; i < 4; ++i) { const int tok = 8 * (4 * tw + i) + (l >> 3); vx[i] = tok * 1280 + (((l & 7) ^ (tok & 7)) << 4); }
    const char* xb = (const char*)x + (size_t)r * 128 * 1280;
    const unsigned lxw = 4 * tw * 1024, lkw = 16384 + 160 * tw * 16;
    auto pieceX = [&](auto ST, auto I) { constexpr int st = ST, i = I; gldsi<(st % 5) * S1 + i * 1024>(vx[i], xb + st * 128, lxw); };
    static_for<0, 4>([&](auto I) { pieceX(IC<0>{}, I); });
#pragma unroll
    for (int j = 0; j < 3; ++j) {
      const int n = min(160 * tw + 64 * j + l, 639), pos = n / 10, ch = n - pos * 10;
      const int col = (ch < 5) ? 640 + 40 * p + 8 * ch : 1280 + 40 * p + 8 * (ch - 5);
      const int k = (pos & ~12) | ((pos & 4) << 1) | ((pos & 8) >> 1);
      vw[j] = k * 3840 + col * 2;
    }
    auto pieceW = [&](auto ST, auto J) {
      constexpr int st = ST, j = J;
      if constexpr (j < 2) gldsi<(st % 5) * S1 + j * 1024>(vw[j], (const char*)wqkv + (size_t)st * 64 * 3840, lkw);
      else { if (l < 32) gldsi<(st % 5) * S1 + j * 1024>(vw[j], (const char*)wqkv + (size_t)st * 64 * 3840, lkw); }
    };
    auto piece = [&](auto ST, auto Q) { constexpr int q = Q; if constexpr (q < 4) pieceX(ST, Q); else pieceW(ST, IC<q - 4>{}); };
    static_for<0, 3>([&](auto J) { pieceW(IC<0>{}, J); });
    static_for<0, 7>([&](auto Q) { piece(IC<1>{}, Q); });
    auto pieceQ = [&](auto QS, auto J) {
      constexpr int qs = QS, j = J, npc = qs < 2 ? 6 : 3;
      const int n = 64 * (tw * npc + j) + l, pos = n / 6, ch = n - pos * 6;
      const int k = 256 * qs + ((pos & ~12) | ((pos & 4) << 1) | ((pos & 8) >> 1));
      if (ch < 5) gldsi<qs * S1 + j * 1024>(k * 3840 + (40 * p + 8 * ch) * 2, wqkv, tw * npc * 1024);
    };
    STAMPW(51, 4);
    v4u xs[20][2];
    f32x4 sq[2], sm[2], cs0, cs1;
    v4u ones_ = ONES;
    asm volatile("" : "+v"(ones_));
    {
      const int xw = tw * 32 * 128;
      int xbs[3][2], ebs[3][2];
#pragma unroll
      for (int q = 0; q < 3; ++q) { xbs[q][0] = xw + xo0 + 2 * q * S1; xbs[q][1] = xw + (xo0 ^ 64) + 2 * q * S1; ebs[q][0] = kvo + tw * 32 + 2 * q * S1; ebs[q][1] = kvo + 4 * 32 + 2 * q * S1; }
      asm volatile("" : "+v"(xbs[0][0]), "+v"(xbs[0][1]), "+v"(xbs[1][0]), "+v"(xbs[1][1]), "+v"(xbs[2][0]), "+v"(xbs[2][1]),
                        "+v"(ebs[0][0]), "+v"(ebs[0][1]), "+v"(ebs[1][0]), "+v"(ebs[1][1]), "+v"(ebs[2][0]), "+v"(ebs[2][1]));
      v4u e[2][2];
      auto rd1 = [&](auto KK, auto I) {
        constexpr int kk = KK, i = I, slot = (kk >> 1) % 5, ks = kk & 1;
        if constexpr (i < 2) xs[kk][i] = lds128(L, xbs[slot >> 1][ks] + (slot & 1) * S1 + i * 2048);
        else {
          constexpr int t = (i - 2) >> 1, h = i & 1;
          const v2u v = __builtin_bit_cast(v2u, __builtin_amdgcn_ds_read_tr16_b64_v4i16((LAS s16x4*)(L + ebs[slot >> 1][t] + (slot & 1) * S1 + ks * 32 * 160 + h * 8 * 160)));
          if constexpr (h == 0) { e[kk & 1][t].x = v.x; e[kk & 1][t].y = v.y; } else { e[kk & 1][t].z = v.x; e[kk & 1][t].w = v.y; }
        }
      };
      auto mma1 = [&](auto KK, auto I) {
        constexpr int kk = KK, i = I;
        if constexpr (i < 2) mfma_a<(kk == 0)>(sq[i], xs[kk][i], xs[kk][i]);
        else if constexpr (i < 4) mfma_a<(kk == 0)>(sm[i - 2], ones_, xs[kk][i - 2]);
        else if constexpr (i == 4) mfma_a<(kk == 0)>(cs0, ones_, e[kk & 1][0]);
        else mfma_a<(kk == 0)>(cs1, ones_, e[kk & 1][1]);
      };
      waitbar<7>();
      static_for<0, 6>([&](auto I) { rd1(IC<0>{}, I); });
      static_for<0, 10>([&](auto ST) {
        constexpr int st = ST;
        static_for<0, 6>([&](auto I) {
          constexpr int i = I;
          mma1(IC<2 * st>{}, I);
          rd1(IC<2 * st + 1>{}, I);
          if constexpr (st == 0) { piece(IC<2>{}, I); if constexpr (i == 5) piece(IC<2>{}, IC<6>{}); }
          if constexpr (st == 1) { piece(IC<4>{}, I); if constexpr (i == 5) piece(IC<4>{}, IC<6>{}); }
          SB();
        });
        if constexpr (st < 9) { if constexpr (st == 0) waitbar<7>(); else if constexpr (st <= 6) waitbar<14>(); else if constexpr (st == 7) waitbar<13>(); else waitbar<12>(); }
        static_for<0, 6>([&](auto I) {
          constexpr int i = I;
          mma1(IC<2 * st + 1>{}, I);
          if constexpr (st == 0) { piece(IC<3>{}, I); if constexpr (i == 5) piece(IC<3>{}, IC<6>{}); }
          if constexpr (st >= 1 && st <= 5) { piece(IC<st + 4>{}, I); if constexpr (i == 5) piece(IC<st + 4>{}, IC<6>{}); }
          if constexpr (st == 6 || st == 7) pieceQ(IC<st - 6>{}, I);
          if constexpr (st == 8 && i < 3) pieceQ(IC<2>{}, I);
          if constexpr (st < 9) rd1(IC<2 * st + 2>{}, I);
          SB();
        });
      });
      MFMA_FENCE();
    }
#pragma unroll
    for (int s = 0; s < 2; ++s) {
      const int e_ = c & 3;
      const float v = e_ == 0 ? sq[s][0] : e_ == 1 ? sq[s][1] : e_ == 2 ? sq[s][2] : sq[s][3];
      const float m = sm[s][0] * (1.0f / 640.0f);
      const float rs_ = __builtin_amdgcn_rsqf(fmaxf(v * (1.0f / 640.0f) - m * m + 1e-5f, 1e-5f));
      const bool own = g == (c >> 2);
      *(LAS float*)(L + (own ? L_STAT + (32 * tw + 16 * s + c) * 4 : L_SYNC + 512 + l * 4)) = rs_;
      *(LAS float*)(L + (own ? L_STAT + 512 + (32 * tw + 16 * s + c) * 4 : L_SYNC + 768 + l * 4)) = -m * rs_;
    }
    *(LAS float*)(L + (g == 0 ? L_CSUM + (16 * tw + c) * 4 : L_SYNC + 512 + l * 4)) = cs0[0];
    *(LAS float*)(L + (g == 0 ? L_CSUM + (64 + c) * 4 : L_SYNC + 512 + l * 4)) = cs1[0];
    LBAR();
    tag = __builtin_amdgcn_readfirstlane(*(const LAS unsigned*)(L + L_SYNC + 32)) + 1u;
    DRAIN();
    group_sync(L, 7, l);
    lds_spin(L, 5, 4);
    f32x4 qacc[3][2], csq;
    {
      int qbs[3];
#pragma unroll
      for (int q = 0; q < 3; ++q) qbs[q] = q * S1 + wrow * 96 + 8 * pp;
      asm volatile("" : "+v"(qbs[0]), "+v"(qbs[1]), "+v"(qbs[2]));
      int cbs[3];
#pragma unroll
      for (int q = 0; q < 3; ++q) cbs[q] = qbs[q] + (tw < 3 ? tw : 2) * 32;
      asm volatile("" : "+v"(cbs[0]), "+v"(cbs[1]), "+v"(cbs[2]));
      v4u f[2][4];
      auto rd1 = [&](auto KK, auto I) {
        constexpr int kk = KK, i = I, t = i >> 1, h = i & 1;
        const int base = t < 3 ? qbs[kk >> 3] + t * 32 : cbs[kk >> 3];
        const v2u v = __builtin_bit_cast(v2u, __builtin_amdgcn_ds_read_tr16_b64_v4i16((LAS s16x4*)(L + base + (kk & 7) * 32 * 96 + h * 8 * 96)));
        if constexpr (h == 0) { f[kk & 1][t].x = v.x; f[kk & 1][t].y = v.y; } else { f[kk & 1][t].z = v.x; f[kk & 1][t].w = v.y; }
      };
      static_for<0, 8>([&](auto I) { rd1(IC<0>{}, I); });
      static_for<0, 20>([&](auto KK) {
        constexpr int kk = KK;
        static_for<0, 7>([&](auto I) {
          constexpr int i = I;
          if constexpr (i < 6) mfma_a<(kk == 0)>(qacc[i >> 1][i & 1], f[kk & 1][i >> 1], xs[kk][i & 1]);
          else mfma_a<(kk == 0)>(csq, ones_, f[kk & 1][3]);
          if constexpr (kk < 19) { rd1(IC<kk + 1>{}, I); if constexpr (i == 6) rd1(IC<kk + 1>{}, IC<7>{}); }
          SB();
        });
      });
      MFMA_FENCE();
    }
    *(LAS float*)(L + (g == 0 && tw < 3 ? L_CSUM + (80 + 16 * tw + c) * 4 : L_SYNC + 512 + l * 4)) = csq[0];
    asm volatile("s_waitcnt lgkmcnt(0)" ::: "memory");
    if (l == 0) lds_add(L, 4);
    lds_spin(L, 4, 4);
    v4u pqA[2], pqB[2];
#pragma unroll
    for (int s = 0; s < 2; ++s) {
      const float rs_ = *(const LAS float*)(L + L_STAT + (32 * tw + 16 * s + c) * 4);
      const float mr = *(const LAS float*)(L + L_STAT + 512 + (32 * tw + 16 * s + c) * 4);
      unsigned pq[6];
#pragma unroll
      for (int t = 0; t < 3; ++t) {
        const f32x4 cw = *(const LAS f32x4*)(L + L_CSUM + (80 + 16 * t + 4 * g) * 4);
        pq[2 * t] = pk2v(phi2(pfma(lo2(qacc[t][s]), sp2(rs_), pfma(lo2(cw), sp2(mr), sp2(1e-6f)))));
        pq[2 * t + 1] = pk2v(phi2(pfma(hi2(qacc[t][s]), sp2(rs_), pfma(hi2(cw), sp2(mr), sp2(1e-6f)))));
      }
      pqA[s] = v4u{pq[0], pq[1], pq[2], pq[3]};
      pqB[s] = v4u{g < 2 ? pq[4] : 0u, g < 2 ? pq[5] : 0u, 0u, 0u};
    }
    asm volatile("" : "+v"(pqA[0]), "+v"(pqA[1]), "+v"(pqB[0]), "+v"(pqB[1]));
    STAMPW(22, 4);
    if (w == 4) { wait16(ws, WS_FKV + p * 256, tag, l); if (l == 0) lds_add(L, 0); } else lds_spin(L, 0, 1);
    STAMPW(21, 4);
    {
      const int t = min(tid - 256, 209);
      v4u v[16];
#pragma unroll
      for (int i = 0; i < 16; ++i) v[i] = ldg_sc1(rs, WS_KVP + (p * 16 + i) * KVP_STRIDE + t * 16);
      f32x4 s = {0, 0, 0, 0};
#pragma unroll
      for (int i = 0; i < 16; ++i) s += __builtin_bit_cast(f32x4, v[i]);
      const v2u pk = {pk2v(lo2(s)), pk2v(hi2(s))};
      const int fo = t / 5, f = t < 200 ? 4 * (t - fo * 5) + (fo >= 20 ? 20 : 0) : 4 * (t - 200);
      const int kst = f >> 5, tt = (f >> 4) & 1, gg = (f >> 2) & 3;
      if (t < 200) *(LAS v2u*)(L + L_BD + ((fo >> 4) * 2 + kst) * 1024 + (gg * 16 + (fo & 15)) * 16 + tt * 8) = pk;
      else {
#pragma unroll
        for (int i = 0; i < 8; ++i) *(LAS v2u*)(L + L_BD + (6 + kst) * 1024 + (gg * 16 + 2 * i + (f >= 20)) * 16 + tt * 8) = pk;
      }
    }
    asm volatile("s_waitcnt lgkmcnt(0)" ::: "memory");
    if (l == 0) lds_add(L, 1);
    lds_spin(L, 1, 4);
    STAMPW(24, 4);
    v4u bd[8];
#pragma unroll
    for (int i = 0; i < 8; ++i) bd[i] = lds128(L, L_BD + i * 1024 + l * 16);
#pragma unroll
    for (int s = 0; s < 2; ++s) {
      f32x4 den = mfma(bd[6], pqA[s], f32x4{0, 0, 0, 0}); den = mfma(bd[7], pqB[s], den);
      f32x4 num[3];
#pragma unroll
      for (int ft = 0; ft < 3; ++ft) { num[ft] = mfma(bd[2 * ft], pqA[s], f32x4{0, 0, 0, 0}); num[ft] = mfma(bd[2 * ft + 1], pqB[s], num[ft]); }
      const float r0 = __builtin_amdgcn_rcpf(den[0] + 1e-6f), r1 = __builtin_amdgcn_rcpf(den[1] + 1e-6f);
      const float rm = g == 0 ? r0 : r1;
      const unsigned base = WS_O + (8 * r + 2 * tw + s) * 20 * 1024;
      stg_sc1(rs, base + p * 1024 + l * 16, v4u{pk2v(lo2(num[0]) * r0), pk2v(hi2(num[0]) * r0), pk2v(lo2(num[1]) * rm), pk2v(hi2(num[1]) * rm)});
      stg2_sc1(rs, g < 2 ? base + (16 + (p >> 2)) * 1024 + ((2 * ((p & 3) >> 1) + g) * 16 + c) * 16 + (p & 1) * 8 : 0x7FFFFFF0u,
               v2u{pk2v(lo2(num[2]) * r1), pk2v(hi2(num[2]) * r1)});
    }
    STAMPW(26, 4);
    DRAIN();
    STAMPW(27, 4);
    if (l == 0 && lds_add(L, 2) == 3) set_flag(ws, WS_FO + r * 256 + p * 4, tag);
  }
  LBAR();
  STAMP(5);

  const int th = w & 1, kq = w >> 1;
  const int sl = 2 * th + (kq & 1);
  const int tok = 16 * (4 * r2 + sl) + c;
  {
    v2u xr[3];
#pragma unroll
    for (int i = 0; i < 3; ++i) {
      const int T = kq < 2 ? (i == 2 ? 4 : i) : (i == 2 ? 2 : 2 + i);
      xr[i] = *(const v2u*)(x + (size_t)tok * 640 + 80 * cs + 16 * T + 4 * g);
    }
    v4u wf[5][5];
    const int wb = (5 * kq * 32 + 4 * g + q4) * 160 + 8 * pp;
#pragma unroll
    for (int k = 0; k < 5; ++k)
#pragma unroll
      for (int T = 0; T < 5; ++T) wf[k][T] = ldtr(L, wb + k * 32 * 160 + T * 32, 16 * 160);
#pragma unroll
    for (int k = 0; k < 5; ++k) asm volatile("" : "+v"(wf[k][0]), "+v"(wf[k][1]), "+v"(wf[k][2]), "+v"(wf[k][3]), "+v"(wf[k][4]));
    asm volatile("" : "+v"(xr[0]), "+v"(xr[1]), "+v"(xr[2]));
    STAMP(28);
    if (w == 0) wait16(ws, WS_FO + (r2 >> 1) * 256, tag, l);
    if (w == 1) wait16(ws, WS_FC + c3 * 256, tag, l);
    if (w == 2) wait32(ws, WS_FD + cs * 256, tag, l);
    LBAR();
    STAMP(6);
    if (b == 0 && tid == 0) set_flag(ws, WS_EPOCH, tag);
    v4u xf[5][2];
#pragma unroll
    for (int k = 0; k < 5; ++k)
#pragma unroll
      for (int s = 0; s < 2; ++s) xf[k][s] = ldg_sc1(rs, WS_O + ((4 * r2 + 2 * th + s) * 20 + 5 * kq + k) * 1024 + l * 16);
    f32x4 acc[5][2];
#pragma unroll
    for (int T = 0; T < 5; ++T) { acc[T][0] = f32x4{0, 0, 0, 0}; acc[T][1] = f32x4{0, 0, 0, 0}; }
    SB();
#pragma unroll
    for (int k = 0; k < 5; ++k)
#pragma unroll
      for (int T = 0; T < 5; ++T)
#pragma unroll
        for (int s = 0; s < 2; ++s) acc[T][s] = mfma(wf[k][T], xf[k][s], acc[T][s]);
    STAMP(7);
    f32x4 own[3];
    splitk_exchange(L, acc, own, th, kq, l);
    STAMP(29);
    const bool hi = kq >= 2;
    unsigned hp[3][2];
    f32x2 sm2 = {0.f, 0.f}, sq2 = {0.f, 0.f};
#pragma unroll
    for (int i = 0; i < 3; ++i) {
      hp[i][0] = pk2v(f32x2{bflo(xr[i].x), bfhi(xr[i].x)} + lo2(own[i]));
      hp[i][1] = pk2v(f32x2{bflo(xr[i].y), bfhi(xr[i].y)} + hi2(own[i]));
      if (i == 2 && hi) { hp[2][0] = 0u; hp[2][1] = 0u; }
      const f32x2 a0 = {bflo(hp[i][0]), bfhi(hp[i][0])}, a1 = {bflo(hp[i][1]), bfhi(hp[i][1])};
      sm2 += a0 + a1;
      sq2 = pfma(a0, a0, pfma(a1, a1, sq2));
    }
    float sm = sm2.x + sm2.y, sq = sq2.x + sq2.y;
    const unsigned hb = WS_H + (4 * r2 + sl) * 20 * 1024;
    stg_sc1(rs, hb + (2 * cs + (kq >> 1)) * 1024 + l * 16, v4u{hp[0][0], hp[0][1], hp[1][0], hp[1][1]});
    stg2_sc1(rs, hi ? 0x7FFFFFF0u : hb + (16 + (cs >> 1)) * 1024 + l * 16 + (cs & 1) * 8, v2u{hp[2][0], hp[2][1]});
    sm = rowsum4(sm); sq = rowsum4(sq);
    stg2_sc1(rs, g == 0 ? WS_HS + ((4 * r2 + sl) * 16 + cs * 2 + (kq >> 1)) * 128 + c * 8 : 0x7FFFFFF0u, v2u{__float_as_uint(sm), __float_as_uint(sq)});
    *(LAS v2u*)(L + L_HT + (sl * 5 + (hi ? 2 : 0)) * 512 + l * 8) = v2u{hp[0][0], hp[0][1]};
    *(LAS v2u*)(L + L_HT + (sl * 5 + (hi ? 3 : 1)) * 512 + l * 8) = v2u{hp[1][0], hp[1][1]};
    *(LAS v2u*)(L + (hi ? L_SYNC + 512 : L_HT + (sl * 5 + 4) * 512) + l * 8) = v2u{hp[2][0], hp[2][1]};
    STAMP(30);
    DRAIN();
    STAMP(31);
    LBAR();
    if (tid == 0) set_flag(ws, WS_FH + (r2 >> 1) * 256 + ((r2 & 1) * 8 + cs) * 4, tag);
    STAMP(8);
  }
  {
    if (w < 4) p3_body<4, 0, false>(L, ws, rs, r3, c3, cs | ((r2 & 3) << 8), w, l, tag);
    else       p3_body<3, 4, true>(L, ws, rs, r3, c3, cs | ((r2 & 3) << 8), w, l, tag);
    STAMP(36);
    DRAIN();
    STAMP(37);
    LBAR();
    if (tid == 0) set_flag(ws, WS_FA + r3 * 256 + c3 * 4, tag);
    STAMP(12);
  }
  {
    f32x4 acc[5][2];
    if (th == 0) p4_main<0>(L, ws, r2 >> 1, r2, cs, kq, w, l, tag, acc); else p4_main<1>(L, ws, r2 >> 1, r2, cs, kq, w, l, tag, acc);
    LBAR();
    STAMP(14);
    f32x4 own[3];
    splitk_exchange(L, acc, own, th, kq, l);
    const bool hi = kq >= 2;
    u16* ob = out + (size_t)tok * 640 + 80 * cs + 4 * g;
#pragma unroll
    for (int i = 0; i < 3; ++i) {
      const int T = i == 2 ? 4 : (hi ? 2 : 0) + i;
      const v2u hh = *(const LAS v2u*)(L + L_HT + (sl * 5 + T) * 512 + l * 8);
      const v2u o = {pk2v(f32x2{bflo(hh.x), bfhi(hh.x)} + lo2(own[i])), pk2v(f32x2{bflo(hh.y), bfhi(hh.y)} + hi2(own[i]))};
      if (i < 2 || !hi) *(v2u*)(ob + 16 * T) = o;
    }
    STAMP(15);
  }
}

extern "C" void kernel_launch(void* const* d_in, const int* in_sizes, int n_in, void* d_out, int out_size, void* d_ws, size_t ws_size,
                              hipStream_t stream) {
  (void)in_sizes; (void)n_in; (void)out_size; (void)ws_size;
  (void)hipFuncSetAttribute((const void*)fused_block, hipFuncAttributeMaxDynamicSharedMemorySize, LDS_BYTES);
  fused_block<<<256, 512, LDS_BYTES, stream>>>((const u16*)d_in[0], (const u16*)d_in[1], (const u16*)d_in[2], (const u16*)d_in[4],
                                                (const u16*)d_in[5], (const u16*)d_in[6], (u16*)d_out, (char*)d_ws);
}
```

```cpp
#include <hip/hip_runtime.h>
#include <hip/hip_bf16.h>
#include <stdint.h>
#include <type_traits>

#define LAS __attribute__((address_space(3)))
#define GAS __attribute__((address_space(1)))
typedef __bf16 bf16x8 __attribute__((ext_vector_type(8)));
typedef __bf16 bf16x2 __attribute__((ext_vector_type(2)));
typedef short s16x4 __attribute__((ext_vector_type(4)));
typedef float f32x4 __attribute__((ext_vector_type(4)));
typedef float f32x2 __attribute__((ext_vector_type(2)));
typedef unsigned v4u __attribute__((ext_vector_type(4)));
typedef unsigned v2u __attribute__((ext_vector_type(2)));
typedef GAS unsigned gu32;
typedef unsigned short u16;
#define RLX_AGENT __ATOMIC_RELAXED, __HIP_MEMORY_SCOPE_AGENT

constexpr unsigned WS_EPOCH = 0, WS_TMO = 256;
constexpr unsigned WS_FKV = 4096, WS_FO = 8192, WS_FH = 12288, WS_FA = 16384, WS_FC = 20480, WS_FD = 24576, WS_FW = 28672;
constexpr unsigned WS_KVP = 32768, KVP_STRIDE = 3456;
constexpr unsigned WS_O = WS_KVP + 256 * KVP_STRIDE;
constexpr unsigned WS_H = WS_O + 2621440;
constexpr unsigned WS_HS = WS_H + 2621440;
constexpr unsigned WS_ACT = WS_HS + 262144;
constexpr unsigned WS_CS = WS_ACT + 7077888;
constexpr unsigned WS_W3 = WS_CS + 16 * 40 * 448;
constexpr unsigned WS_W4 = WS_W3 + 16 * 280 * 1024;
constexpr unsigned WS_W2 = WS_W4 + 8 * 270 * 1024;
constexpr unsigned WS_STAMP = WS_W2 + 8 * 100 * 1024;
constexpr unsigned WS_END = WS_STAMP + 256 * 64 * 8;

constexpr int LDS_BYTES = 163840;
constexpr int L_KVF = 102400;
constexpr int L_KVC = 135168;
constexpr int L_BD = 138752;
constexpr int L_STAT = 146944;
constexpr int L_CSUM = 147968;
constexpr int L_SIDE = 148480;
constexpr int L_CS3 = 151168;
constexpr int L_SYNC = 152064;
constexpr int L_HT = 153600;
constexpr int L_RED = 0;
constexpr int L_X3 = 86016;

#define STAMPS 0
#define T2MODE 1
#define WOMODE 1
#define T1MODE 4
#define T2POS 0
#define T1SLEEP 16
#define OUTAUX 16
#define T0KB 52

__device__ __forceinline__ void glds(unsigned voff, const void* sbase, unsigned ldsaddr) {
  asm volatile("s_mov_b32 m0, %2\n\ts_nop 0\n\tglobal_load_lds_dwordx4 %0, %1" ::"v"(voff), "s"(sbase), "s"(ldsaddr) : "memory");
}
__device__ __forceinline__ void glds2_sc1(unsigned voff, const void* sbase, unsigned ldsaddr) {
  asm volatile("s_mov_b32 m0, %2\n\ts_nop 0\n\tglobal_load_lds_dwordx4 %0, %1 sc1\n\tglobal_load_lds_dwordx4 %0, %1 offset:1024 sc1" ::"v"(voff), "s"(sbase), "s"(ldsaddr) : "memory");
}
template <int IMM> __device__ __forceinline__ void gldsi(unsigned voff, const void* sbase, unsigned lbase) {
  asm volatile("s_add_u32 m0, %2, %3\n\ts_nop 0\n\tglobal_load_lds_dwordx4 %0, %1" ::"v"(voff), "s"(sbase), "s"(lbase), "i"(IMM) : "memory", "scc");
}
template <int IMM> __device__ __forceinline__ void glds2i_sc1(unsigned voff, const void* sbase, unsigned lbase) {
  asm volatile("s_add_u32 m0, %2, %3\n\ts_nop 0\n\tglobal_load_lds_dwordx4 %0, %1 sc1\n\tglobal_load_lds_dwordx4 %0, %1 offset:1024 sc1" ::"v"(voff), "s"(sbase), "s"(lbase), "i"(IMM) : "memory", "scc");
}
template <int N, int IMM> __device__ __forceinline__ void gldsn_sc1(unsigned voff, const void* sbase, unsigned lbase) {
  static_assert(N >= 1 && N <= 4, "");
  if constexpr (N == 1) asm volatile("s_add_u32 m0, %2, %3\n\ts_nop 0\n\tglobal_load_lds_dwordx4 %0, %1 sc1" ::"v"(voff), "s"(sbase), "s"(lbase), "i"(IMM) : "memory", "scc");
  if constexpr (N == 2) asm volatile("s_add_u32 m0, %2, %3\n\ts_nop 0\n\tglobal_load_lds_dwordx4 %0, %1 sc1\n\tglobal_load_lds_dwordx4 %0, %1 offset:1024 sc1" ::"v"(voff), "s"(sbase), "s"(lbase), "i"(IMM) : "memory", "scc");
  if constexpr (N == 3) asm volatile("s_add_u32 m0, %2, %3\n\ts_nop 0\n\tglobal_load_lds_dwordx4 %0, %1 sc1\n\tglobal_load_lds_dwordx4 %0, %1 offset:1024 sc1\n\tglobal_load_lds_dwordx4 %0, %1 offset:2048 sc1" ::"v"(voff), "s"(sbase), "s"(lbase), "i"(IMM) : "memory", "scc");
  if constexpr (N == 4) asm volatile("s_add_u32 m0, %2, %3\n\ts_nop 0\n\tglobal_load_lds_dwordx4 %0, %1 sc1\n\tglobal_load_lds_dwordx4 %0, %1 offset:1024 sc1\n\tglobal_load_lds_dwordx4 %0, %1 offset:2048 sc1\n\tglobal_load_lds_dwordx4 %0, %1 offset:3072 sc1" ::"v"(voff), "s"(sbase), "s"(lbase), "i"(IMM) : "memory", "scc");
}
template <int IMM, int OFF> __device__ __forceinline__ void gldso_sc1(unsigned voff, const void* sbase, unsigned lbase) {
  asm volatile("s_add_u32 m0, %2, %3\n\ts_nop 0\n\tglobal_load_lds_dwordx4 %0, %1 offset:%4 sc1" ::"v"(voff), "s"(sbase), "s"(lbase), "i"(IMM), "i"(OFF) : "memory", "scc");
}
__device__ __forceinline__ void ldg_asm_sc1(v4u& d, unsigned voff, const void* sbase) {
  asm volatile("global_load_dwordx4 %0, %1, %2 sc1" : "=v"(d) : "v"(voff), "s"(sbase) : "memory");
}
__device__ __forceinline__ void ldg2_asm_sc1(v2u& d, unsigned voff, const void* sbase) {
  asm volatile("global_load_dwordx2 %0, %1, %2 sc1" : "=v"(d) : "v"(voff), "s"(sbase) : "memory");
}
template <int N> __device__ __forceinline__ void wait_for(v4u& a, v4u& b) { asm volatile("s_waitcnt vmcnt(%2)" : "+v"(a), "+v"(b) : "i"(N) : "memory"); }
template <int N> __device__ __forceinline__ void waitvm() { asm volatile("s_waitcnt vmcnt(%0)" ::"i"(N) : "memory"); }
__device__ __forceinline__ f32x2 pfma(f32x2 a, f32x2 b, f32x2 c) { return __builtin_elementwise_fma(a, b, c); }
__device__ __forceinline__ f32x2 sp2(float a) { return f32x2{a, a}; }
__device__ __forceinline__ f32x2 lo2(f32x4 v) { return __builtin_shufflevector(v, v, 0, 1); }
__device__ __forceinline__ f32x2 hi2(f32x4 v) { return __builtin_shufflevector(v, v, 2, 3); }
__device__ __forceinline__ unsigned pk2v(f32x2 v) { return __builtin_bit_cast(unsigned, __builtin_convertvector(v, bf16x2)); }
__device__ __forceinline__ f32x2 phi2(f32x2 te) {
  const f32x2 y = pfma(te, sp2(1.44269504088896f), sp2(-1.44269504088896e-6f));
  const f32x2 e = {__builtin_amdgcn_fmed3f(__builtin_amdgcn_exp2f(y.x), 0.f, 1.f), __builtin_amdgcn_fmed3f(__builtin_amdgcn_exp2f(y.y), 0.f, 1.f)};
  const f32x2 m = {fmaxf(te.x, 1e-6f), fmaxf(te.y, 1e-6f)};
  return m + e;
}
__device__ __forceinline__ unsigned geglu2(f32x2 ag, f32x2 au, f32x2 cg, f32x2 cu, float rstd, float mr) {
  const f32x2 gv = pfma(ag, sp2(rstd), cg * sp2(mr)), uv = pfma(au, sp2(rstd), cu * sp2(mr));
  const f32x2 z = gv * pfma(gv * gv, sp2(-0.1029432397f), sp2(-2.302208198f));
  const f32x2 d = {1.0f + __builtin_amdgcn_exp2f(z.x), 1.0f + __builtin_amdgcn_exp2f(z.y)};
  const f32x2 rc = {__builtin_amdgcn_rcpf(d.x), __builtin_amdgcn_rcpf(d.y)};
  return pk2v(gv * uv * rc);
}
template <int V> using IC = std::integral_constant<int, V>;
template <int I, int N, class F> __device__ __forceinline__ void static_for(F&& f) {
  if constexpr (I < N) { f(std::integral_constant<int, I>{}); static_for<I + 1, N>(f); }
}
#define LBAR() asm volatile("s_waitcnt lgkmcnt(0)\n\ts_barrier" ::: "memory")
#define DRAIN() asm volatile("s_waitcnt vmcnt(0)" ::: "memory")
#define SB() __builtin_amdgcn_sched_barrier(0)
template <int N> __device__ __forceinline__ void waitbar() { asm volatile("s_waitcnt vmcnt(%0) lgkmcnt(0)\n\ts_barrier" ::"i"(N) : "memory"); }

__device__ __forceinline__ unsigned pk2(float a, float b) {
  bf16x2 v = {(__bf16)a, (__bf16)b};
  return __builtin_bit_cast(unsigned, v);
}
__device__ __forceinline__ float bflo(unsigned u) { return __uint_as_float(u << 16); }
__device__ __forceinline__ float bfhi(unsigned u) { return __uint_as_float(u & 0xffff0000u); }
__device__ __forceinline__ f32x4 mfma(v4u a, v4u b, f32x4 c) {
  return __builtin_amdgcn_mfma_f32_16x16x32_bf16(__builtin_bit_cast(bf16x8, a), __builtin_bit_cast(bf16x8, b), c, 0, 0, 0);
}
__device__ __forceinline__ void mfma_acc(f32x4& acc, v4u a, v4u b) {
  asm volatile("v_mfma_f32_16x16x32_bf16 %0, %1, %2, %0" : "+v"(acc) : "v"(a), "v"(b));
}
__device__ __forceinline__ void mfma_first(f32x4& acc, v4u a, v4u b) {
  asm volatile("v_mfma_f32_16x16x32_bf16 %0, %1, %2, 0" : "=&v"(acc) : "v"(a), "v"(b));
}
template <bool FIRST> __device__ __forceinline__ void mfma_a(f32x4& acc, v4u a, v4u b) { if constexpr (FIRST) mfma_first(acc, a, b); else mfma_acc(acc, a, b); }
#define MFMA_FENCE() asm volatile("s_nop 7\n\ts_nop 1" ::: "memory")
__device__ __forceinline__ v4u lds128(LAS char* L, int off) { return *(const LAS v4u*)(L + off); }
__device__ __forceinline__ v4u ldtr(LAS char* L, int off, int d) {
  s16x4 a = __builtin_amdgcn_ds_read_tr16_b64_v4i16((LAS s16x4*)(L + off));
  s16x4 b = __builtin_amdgcn_ds_read_tr16_b64_v4i16((LAS s16x4*)(L + off + d));
  v2u x = __builtin_bit_cast(v2u, a), y = __builtin_bit_cast(v2u, b);
  return v4u{x.x, x.y, y.x, y.y};
}
__device__ __forceinline__ float phi_eps(float te) {
  const float e = __builtin_amdgcn_fmed3f(__builtin_amdgcn_exp2f((te - 1e-6f) * 1.44269504088896f), 0.f, 1.f);
  return fmaxf(te, 1e-6f) + e;
}
__device__ __forceinline__ float rowsum4(float v) {
  auto a = __builtin_amdgcn_permlane16_swap(__float_as_uint(v), __float_as_uint(v), false, false);
  const float u = __uint_as_float(a[0]) + __uint_as_float(a[1]);
  auto b = __builtin_amdgcn_permlane32_swap(__float_as_uint(u), __float_as_uint(u), false, false);
  return __uint_as_float(b[0]) + __uint_as_float(b[1]);
}
__device__ __forceinline__ v4u ldg_sc1(__amdgpu_buffer_rsrc_t rs, unsigned off) { return __builtin_amdgcn_raw_buffer_load_b128(rs, off, 0, 16); }
__device__ __forceinline__ v2u ldg2_sc1(__amdgpu_buffer_rsrc_t rs, unsigned off) { return __builtin_amdgcn_raw_buffer_load_b64(rs, off, 0, 16); }
__device__ __forceinline__ unsigned ldg1_sc1(__amdgpu_buffer_rsrc_t rs, unsigned off) { return __builtin_amdgcn_raw_buffer_load_b32(rs, off, 0, 16); }
__device__ __forceinline__ void stg_sc1(__amdgpu_buffer_rsrc_t rs, unsigned off, v4u v) { __builtin_amdgcn_raw_buffer_store_b128(v, rs, off, 0, 16); }
__device__ __forceinline__ void stg2_sc1(__amdgpu_buffer_rsrc_t rs, unsigned off, v2u v) { __builtin_amdgcn_raw_buffer_store_b64(v, rs, off, 0, 16); }
__device__ __forceinline__ void stg1_sc1(__amdgpu_buffer_rsrc_t rs, unsigned off, unsigned v) { __builtin_amdgcn_raw_buffer_store_b32(v, rs, off, 0, 16); }

__device__ __forceinline__ void wait16(char* ws, unsigned off, unsigned tag, int lane) {
  gu32* f = (gu32*)(ws + off) + (lane & 15);
  for (unsigned sp = 0; sp < (1u << 15); ++sp) {
    unsigned v = __hip_atomic_load(f, RLX_AGENT);
    if (__all(v == tag)) return;
    __builtin_amdgcn_s_sleep(1);
  }
  if (lane == 0) __hip_atomic_store((gu32*)(ws + WS_TMO), 1u, RLX_AGENT);
}
__device__ __forceinline__ void wait32(char* ws, unsigned off, unsigned tag, int lane) {
  gu32* f = (gu32*)(ws + off) + (lane & 31);
  for (unsigned sp = 0; sp < (1u << 15); ++sp) {
    unsigned v = __hip_atomic_load(f, RLX_AGENT);
    if (__all(v == tag)) return;
    __builtin_amdgcn_s_sleep(1);
  }
  if (lane == 0) __hip_atomic_store((gu32*)(ws + WS_TMO), 1u, RLX_AGENT);
}
typedef LAS unsigned lu32;
__device__ __forceinline__ unsigned lds_add(LAS char* L, int word) { return __hip_atomic_fetch_add((lu32*)(L + L_SYNC) + word, 1u, __ATOMIC_RELAXED, __HIP_MEMORY_SCOPE_WORKGROUP); }
__device__ __forceinline__ void lds_spin(LAS char* L, int word, unsigned want) {
  for (unsigned sp = 0; sp < (1u << 20); ++sp) {
    if (__builtin_amdgcn_readfirstlane(__hip_atomic_load((lu32*)(L + L_SYNC) + word, __ATOMIC_RELAXED, __HIP_MEMORY_SCOPE_WORKGROUP)) >= want) break;
  }
  asm volatile("" ::: "memory");
}
__device__ __forceinline__ void group_sync(LAS char* L, int word, int l) {
  asm volatile("s_waitcnt lgkmcnt(0)" ::: "memory");
  if (l == 0) lds_add(L, word);
  lds_spin(L, word, 4);
}
__device__ __forceinline__ void touch(unsigned voff, const void* sbase) {
  asm volatile("s_mov_b32 m0, %2\n\ts_nop 0\n\tglobal_load_lds_dword %0, %1" ::"v"(voff), "s"(sbase), "s"(L_SYNC + 64) : "memory");
}
__device__ __forceinline__ void touch64(const void* addr) {
  asm volatile("s_mov_b32 m0, %1\n\ts_nop 0\n\tglobal_load_lds_dword %0, off" ::"v"(addr), "s"(L_SYNC + 64) : "memory");
}
__device__ __forceinline__ void set_flag(char* ws, unsigned off, unsigned tag) { __hip_atomic_store((gu32*)(ws + off), tag, RLX_AGENT); }
#define STAMPW(i, wv) do {} while (0)
#define STAMP(i) STAMPW(i, 0)

__device__ __forceinline__ void splitk_exchange(LAS char* L, f32x4 (&acc)[5][2], f32x4 (&own)[3], int th, int kq, int l) {
  const int wb = L_RED + (th * 40 + kq) * 1024 + l * 16;
#pragma unroll
  for (int s = 0; s < 2; ++s)
#pragma unroll
    for (int T = 0; T < 5; ++T) *(LAS f32x4*)(L + wb + (s * 5 + T) * 4096) = acc[T][s];
  LBAR();
  const int rb = L_RED + (th * 2 + (kq & 1)) * 5 * 4096 + l * 16, t0 = rb + (kq >> 1) * 8192;
  f32x4 v[3][4];
#pragma unroll
  for (int k = 0; k < 4; ++k) {
    v[0][k] = *(const LAS f32x4*)(L + t0 + k * 1024);
    v[1][k] = *(const LAS f32x4*)(L + t0 + 4096 + k * 1024);
    v[2][k] = *(const LAS f32x4*)(L + rb + 4 * 4096 + k * 1024);
  }
#pragma unroll
  for (int i = 0; i < 3; ++i) own[i] = (v[i][0] + v[i][1]) + (v[i][2] + v[i][3]);
}

#define P4XW 4
constexpr int P4_XW = P4XW;
constexpr int P4_PRE = 4;
#define P4EARLY 10
constexpr int P4_EARLY = P4EARLY;
constexpr int p4_g(int it, int n) { return it == 4 ? n : it == 6 ? 2 * n : 0; }
constexpr int p4_x(int it) { return it + P4_XW < 14 ? 2 : 0; }
constexpr int p4_xwait(int st, int n) {
  int y = 0;
  if (st < P4_XW) { y = 2 * (P4_XW - 1 - st); for (int j = 0; j < st; ++j) y += p4_x(j) + p4_g(j, n); }
  else { y = p4_g(st - P4_XW, n); for (int j = st - P4_XW + 1; j < st; ++j) y += p4_x(j) + p4_g(j, n); }
  return y;
}
constexpr int p4_wwait(int st, int n) {
  int y = st == 10 ? n : 0;
  for (int j = 7; j < st; ++j) y += p4_x(j);
  return y;
}
template <int TH, int stg> __device__ __forceinline__ void p4_issue(char* ws, int cs, int kq, int l) {
  constexpr int N = TH == 0 ? 3 : 2;
  const int kb = kq < 2 ? 14 * kq : 28 + 13 * (kq - 2);
  gldsn_sc1<N, (stg % 7) * 20480>(l * 16, ws + WS_W4 + (size_t)cs * 270 * 1024 + TH * 3072 + (size_t)min(kb + stg, 53) * 5120, kq * 5120 + TH * 3072);
}
template <int J> __device__ __forceinline__ void p4_pre_piece(const char* src, unsigned l16, int th, unsigned lbase) {
  constexpr int q0 = 2 * J, q1 = (2 * J + 1 < 35) ? 2 * J + 1 : 34;
  constexpr int f0 = (q0 / 5) * 20480 + (q0 % 5) * 1024, f1 = (q1 / 5) * 20480 + (q1 % 5) * 1024;
  asm volatile("s_mov_b32 m0, %2\n\ts_nop 0\n\tglobal_load_lds_dwordx4 %0, %1 sc1" ::"v"(l16), "s"(src + (th ? q1 : q0) * 1024), "s"(lbase + (th ? f1 : f0)) : "memory");
}
template <int TH>
__device__ __forceinline__ void p4_main(LAS char* L, char* ws, int r, int r2, int cs, int kq, int w, int l, unsigned tag, f32x4 (&acc)[5][2]) {
  constexpr int N = TH == 0 ? 3 : 2;
  const int kb = kq < 2 ? 14 * kq : 28 + 13 * (kq - 2);
  const int nk = kq < 2 ? 14 : 13;
  const unsigned l16 = l * 16;
  auto issue = [&](auto STG) { p4_issue<TH, STG>(ws, cs, kq, l); };
  int wbs[3];
  wbs[0] = kq * 5120 + l16; wbs[1] = wbs[0] + 3 * 20480; wbs[2] = wbs[0] + 6 * 20480;
  asm volatile("" : "+v"(wbs[0]), "+v"(wbs[1]), "+v"(wbs[2]));
  {
    const char* w4src = ws + WS_W4 + (size_t)cs * 270 * 1024 + (size_t)kb * 5120;
    static_for<P4_EARLY, 18>([&](auto J) { p4_pre_piece<J>(w4src, l16, TH, kq * 5120); });
    static_assert(P4_EARLY >= 10, "");
  }
  v4u wp[P4_PRE][5];
  static_for<0, P4_PRE>([&](auto ST) { static_for<0, 5>([&](auto I) { wp[ST][I] = lds128(L, wbs[ST / 3] + (ST % 3) * 20480 + I * 1024); }); });
  LBAR();
  static_for<7, 7 + P4_PRE>([&](auto ST) { issue(ST); });
  STAMP(38);
  if (w == 0) wait16(ws, WS_FA + r * 256, tag, l);
  STAMP(39);
  waitbar<P4_PRE * N>();
  STAMP(13);
  v4u xf[14][2];
  const char* xa = ws + WS_ACT + (size_t)(4 * r2 + 2 * TH) * 54 * 1024;
  auto ldx = [&](auto K, auto S) {
    constexpr int k = K, s = S;
    ldg_asm_sc1(xf[k][s], l16, xa + (size_t)(s * 54 + min(kb + k, 53)) * 1024);
  };
  static_for<0, P4_XW>([&](auto K) { ldx(K, IC<0>{}); ldx(K, IC<1>{}); });
  v4u wf[2][5];
  auto rd1 = [&](auto STG, auto I) {
    constexpr int stg = STG, T = I, sl7 = stg % 7;
    wf[stg & 1][T] = lds128(L, wbs[sl7 / 3] + (sl7 % 3) * 20480 + T * 1024);
  };
  static_for<0, 14>([&](auto ST) {
    constexpr int st = ST;
    if constexpr (st == 4 || st == 6) LBAR();
    if constexpr (st == 10 || st == 12) waitbar<p4_wwait(st, N)>();
    if constexpr (st == 1) STAMP(40);
    if constexpr (st == 4) STAMP(41);
    if constexpr (st == 7) STAMP(42);
    if constexpr (st == 10) STAMP(43);
    if constexpr (st == 13) STAMP(44);
    wait_for<p4_xwait(st, N)>(xf[st][0], xf[st][1]);
    SB();
    if (st < 13 || st < nk) {
      static_for<0, 10>([&](auto I) {
        constexpr int i = I;
        if constexpr (st < P4_PRE) mfma_a<(st == 0)>(acc[i >> 1][i & 1], wp[st][i >> 1], xf[st][i & 1]);
        else mfma_a<false>(acc[i >> 1][i & 1], wf[st & 1][i >> 1], xf[st][i & 1]);
        if constexpr (st + P4_XW < 14 && (i == 0 || i == 2)) ldx(IC<st + P4_XW>{}, IC<(i >> 1)>{});
        if constexpr (st == 4 && i == 4) issue(IC<11>{});
        if constexpr (st == 6 && (i == 4 || i == 7)) issue(IC<12 + (i == 7)>{});
        if constexpr (st >= P4_PRE - 1 && st < 13 && i >= 2 && i < 7) rd1(IC<st + 1>{}, IC<i - 2>{});
        SB();
      });
    }
  });
  MFMA_FENCE();
}

template <int NT> struct Frag3 { v4u x[2], g[NT], u[NT]; };

template <int NT, int TILE0, bool LOADER>
__device__ __forceinline__ void p3_body(LAS char* L, char* ws, __amdgpu_buffer_rsrc_t rs, int r, int p, int cs, int w, int l, unsigned tag) {
  const int c = l & 15, g = l >> 4, tg = w & 3;
  const unsigned l16 = l * 16;
  constexpr int NWP = LOADER ? 3 : 4, NP = LOADER ? 7 : 4;
  const int wp0 = LOADER ? 16 + 3 * tg : 4 * tg;
  const char* wsrc = ws + WS_W3 + (size_t)p * 280 * 1024 + wp0 * 1024;
  const char* hx = ws + WS_H + (size_t)(8 * r + 2 * tg) * 20 * 1024;
  const unsigned lw = wp0 * 1024, lx = L_X3 + tg * 4096;
  auto piece = [&](auto STG, auto Q) {
    constexpr int stg = STG, q = Q, slot = stg % 3;
    if constexpr (q < NWP) gldso_sc1<slot * 28672, q * 1024>(l16, wsrc + stg * 28672, lw);
    else gldso_sc1<slot * 16384 + ((q - NWP) >> 1) * 2048, ((q - NWP) & 1) * 1024>(l16, hx + stg * 2048 + ((q - NWP) >> 1) * 20480, lx);
  };
  static_for<0, 3>([&](auto ST) { static_for<0, NWP>([&](auto Q) { piece(ST, Q); }); });
  if constexpr (!LOADER) {
    const int ct = min(w * 64 + l, 223), cm = ct >= 112;
    unsigned csv[20];
#pragma unroll
    for (int i = 0; i < 20; ++i) csv[i] = ldg1_sc1(rs, WS_CS + (p * 40 + 2 * i + cm) * 448 + (ct - 112 * cm) * 4);
    float s = 0.f;
#pragma unroll
    for (int i = 0; i < 20; ++i) s += __uint_as_float(csv[i]);
    *(LAS float*)(L + (w * 64 + l < 224 ? L_CS3 + ct * 4 : L_SYNC + 512)) = s;
  }
  STAMP(32);
  if (w == 7) { wait16(ws, WS_FH + r * 256, tag, l); STAMPW(33, 7); }
  DRAIN();
  LBAR();
  STAMP(10);
  if constexpr (LOADER) static_for<0, 3>([&](auto ST) { static_for<NWP, NP>([&](auto Q) { piece(ST, Q); }); });
  v2u stp[2][4];
#pragma unroll
  for (int s = 0; s < 2; ++s)
#pragma unroll
    for (int i = 0; i < 4; ++i) ldg2_asm_sc1(stp[s][i], (4 * g + i) * 128 + c * 8, ws + WS_HS + (8 * r + 2 * tg + s) * 16 * 128);

  f32x4 ag[NT][2], au[NT][2];
  float rstd[2], mr[2];
  const int xo = L_X3 + tg * 4096 + l16;
  int wb0 = TILE0 * 1024 + l16, wb2 = wb0 + 2 * 28672;
  asm volatile("" : "+v"(wb0), "+v"(wb2));
  auto rd1 = [&](Frag3<NT>& f, auto SLOT, auto KS, auto I) {
    constexpr int slot = SLOT, ks = KS, i = I;
    if constexpr (i < 2) f.x[i] = lds128(L, xo + slot * 16384 + ks * 1024 + i * 2048);
    else {
      constexpr int j = i - 2, t = j >> 1, u = j & 1, off = ks * 14336 + u * 7168 + t * 1024;
      const v4u v = slot < 2 ? lds128(L, wb0 + slot * 28672 + off) : lds128(L, wb2 + off);
      if constexpr (u == 0) f.g[t] = v; else f.u[t] = v;
    }
  };
  auto mma1 = [&](const Frag3<NT>& f, auto I, auto FIRST) {
    constexpr int i = I, t = i >> 2, u = (i >> 1) & 1, s = i & 1;
    if constexpr (u == 0) mfma_a<FIRST>(ag[t][s], f.g[t], f.x[s]); else mfma_a<FIRST>(au[t][s], f.u[t], f.x[s]);
  };
  constexpr int NM = 4 * NT, NR = 2 + 2 * NT;
  Frag3<NT> A, B;
  if constexpr (LOADER) waitbar<8 + 8>(); else LBAR();
  STAMP(35);
  static_for<0, NR>([&](auto I) { rd1(A, IC<0>{}, IC<0>{}, I); });
  static_for<0, 10>([&](auto ST) {
    constexpr int st = ST, slot = st % 3, nslot = (st + 1) % 3;
    static_for<0, NM>([&](auto I) {
      constexpr int i = I;
      mma1(A, I, IC<(st == 0)>{});
      if constexpr (i < NR) rd1(B, IC<slot>{}, IC<1>{}, I);
      SB();
    });
    if constexpr (st < 9) {
      if constexpr (st == 0) { if constexpr (LOADER) waitbar<4 + 8>(); else LBAR(); }
      else if constexpr (st == 1) { if constexpr (LOADER) waitbar<NP>(); else LBAR(); }
      else if constexpr (st < 8) waitbar<NP>(); else waitbar<0>();
    }
    if constexpr (st == 4) {
      asm volatile("" : "+v"(stp[0][0]), "+v"(stp[0][1]), "+v"(stp[0][2]), "+v"(stp[0][3]), "+v"(stp[1][0]), "+v"(stp[1][1]), "+v"(stp[1][2]), "+v"(stp[1][3]));
#pragma unroll
      for (int s = 0; s < 2; ++s) {
        float sm = 0.f, sq = 0.f;
#pragma unroll
        for (int i = 0; i < 4; ++i) { sm += __uint_as_float(stp[s][i].x); sq += __uint_as_float(stp[s][i].y); }
        sm = rowsum4(sm); sq = rowsum4(sq);
        const float mean = sm * (1.0f / 640.0f);
        rstd[s] = __builtin_amdgcn_rsqf(fmaxf(sq * (1.0f / 640.0f) - mean * mean, 0.f) + 1e-5f);
        mr[s] = -mean * rstd[s];
      }
      asm volatile("" : "+v"(rstd[0]), "+v"(rstd[1]), "+v"(mr[0]), "+v"(mr[1]));
    }
    if constexpr (st == 2) STAMP(45);
    if constexpr (st == 5) STAMP(46);
    if constexpr (st == 8) STAMP(47);
    static_for<0, NM>([&](auto I) {
      constexpr int i = I;
      mma1(B, I, IC<0>{});
      if constexpr (LOADER) { if constexpr (st <= 6 && i < NP) piece(IC<st + 3>{}, I); }
      else { if constexpr (st <= 6 && (i % 3) == 0 && i / 3 < NP) piece(IC<st + 3>{}, IC<i / 3>{}); }
      if constexpr (st < 9 && i < NR) rd1(A, IC<nslot>{}, IC<0>{}, I);
      SB();
    });
  });
  MFMA_FENCE();
  STAMP(11);
  LBAR();
  const int kq4 = w >> 1, th4 = w & 1;
  const char* w4src = ws + WS_W4 + (size_t)(cs & 7) * 270 * 1024 + (size_t)(kq4 < 2 ? 14 * kq4 : 28 + 13 * (kq4 - 2)) * 5120;
  const unsigned w4l = kq4 * 5120;
  f32x4 cgv[NT], cuv[NT];
#pragma unroll
  for (int t = 0; t < NT; ++t) {
    cgv[t] = *(const LAS f32x4*)(L + L_CS3 + ((TILE0 + t) * 16 + 4 * g) * 4);
    cuv[t] = *(const LAS f32x4*)(L + L_CS3 + (112 + (TILE0 + t) * 16 + 4 * g) * 4);
  }
  static_for<0, 2>([&](auto S) {
    constexpr int s = S;
    unsigned y[NT][2];
    const unsigned ab = WS_ACT + (8 * r + 2 * tg + s) * 54 * 1024 + l16;
    static_for<0, NT>([&](auto T) {
      constexpr int t = T, step = s * NT + t, j0 = step < NT ? step * P4_EARLY / NT : P4_EARLY, j1 = step < NT ? (step + 1) * P4_EARLY / NT : P4_EARLY;
      y[t][0] = geglu2(lo2(ag[t][s]), lo2(au[t][s]), lo2(cgv[t]), lo2(cuv[t]), rstd[s], mr[s]);
      static_for<j0, (j0 + j1 + 1) / 2>([&](auto J) { p4_pre_piece<J>(w4src, l16, th4, w4l); });
      y[t][1] = geglu2(hi2(ag[t][s]), hi2(au[t][s]), hi2(cgv[t]), hi2(cuv[t]), rstd[s], mr[s]);
      static_for<(j0 + j1 + 1) / 2, j1>([&](auto J) { p4_pre_piece<J>(w4src, l16, th4, w4l); });
      if constexpr (t & 1) {
        if constexpr (TILE0 == 0) stg_sc1(rs, p < 15 ? ab + (3 * p + (t >> 1)) * 1024 : 0x7FFFFFF0u, v4u{y[t - 1][0], y[t - 1][1], y[t][0], y[t][1]});
        else stg_sc1(rs, ab + (p < 15 ? 3 * p + 2 : 45) * 1024, v4u{y[t - 1][0], y[t - 1][1], y[t][0], y[t][1]});
      }
    });
    if constexpr (TILE0 != 0) stg2_sc1(rs, ab + (46 + (p >> 1)) * 1024 + (p & 1) * 8, v2u{y[NT - 1][0], y[NT - 1][1]});
  });
}

struct FragKV { v4u x[2], w[5]; };

__global__ __launch_bounds__(512, 2) void fused_block(const u16* __restrict__ x, const u16* __restrict__ wqkv, const u16* __restrict__ wo,
                                                      const u16* __restrict__ wg, const u16* __restrict__ wu, const u16* __restrict__ wd,
                                                      u16* __restrict__ out, char* __restrict__ ws) {
  extern __shared__ __attribute__((aligned(16))) char smem_[];
  LAS char* L = (LAS char*)smem_;
  const int tid = threadIdx.x, l = tid & 63, c = l & 15, g = l >> 4;
  const int w = __builtin_amdgcn_readfirstlane(tid >> 6);
  const int b = blockIdx.x, xcd = b & 7, jj = b >> 3, rg = xcd >> 1, cgp = xcd & 1;
  const int r = 4 * rg + (jj >> 3), p = 8 * cgp + (jj & 7);
  const int r2 = 4 * xcd + (jj >> 3), cs = jj & 7;
  const int r3 = 8 * (xcd >> 2) + (jj >> 2), c3 = 4 * (xcd & 3) + (jj & 3);
  const __amdgpu_buffer_rsrc_t rs = __builtin_amdgcn_make_buffer_rsrc(ws, 0, WS_END, 0x00020000);
  const v4u ONES = {0x3F803F80u, 0x3F803F80u, 0x3F803F80u, 0x3F803F80u};
  const int q4 = (l & 15) >> 2, pp = l & 3;
  STAMP(0);

  unsigned tag;
  constexpr int S1 = 26624;
  const int xo0 = c * 128 + ((g ^ (c & 7)) << 4);
  const int wrow = 16 * (g >> 1) + 4 * (g & 1) + q4;
  const int kvo = 16384 + wrow * 160 + 8 * pp;
  auto kv_product = [&](int w) {
    const int Ta = w < 2 ? 0 : w < 5 ? 1 : 2;
    const int Tb = (w == 0 || w == 2 || w == 7) ? 2 : (w == 4 || w == 6) ? 4 : 3;
    v4u A[4], B[4];
#pragma unroll
    for (int tg = 0; tg < 4; ++tg) {
      A[tg] = lds128(L, L_KVF + (tg * 5 + Ta) * 1024 + l * 16);
      B[tg] = lds128(L, L_KVF + (tg * 5 + Tb) * 1024 + l * 16);
    }
    f32x4 d = {0, 0, 0, 0};
    const bool one = Tb == 2 && c < 8;
#pragma unroll
    for (int tg = 0; tg < 4; ++tg) d = mfma(A[tg], one ? ONES : B[tg], d);
    const int kc = 16 * Ta + 4 * g;
    const int vc = 16 * Tb + c - 40;
    const int hk = kc >= 20;
    const bool iskv = kc < 40 && vc >= 0 && hk == (vc >= 20), isz = kc < 40 && vc < 0 && c == 0;
    *(LAS f32x4*)(L + (iskv ? L_KVC + (vc * 20 + kc - 20 * hk) * 4 : isz ? L_KVC + (800 + kc) * 4 : L_SYNC + 512 + (l & 31) * 16)) = d;
  };

  if (w < 4) {
    {
      const char* pc;
      asm volatile("s_getpc_b64 %0" : "=s"(pc));
      const int n = (jj * 4 + w) * 4 + (l & 3);
      if (l < 4) touch64(pc + min(n * 128, T0KB * 1024 - 128));
    }
    auto hint1 = [&](int stg, int n) {
      const int idx = jj * 36 + min(n, 35);
      const int wr_ = idx / 10, pc = idx - wr_ * 10, sec = pc / 5;
      touch64(idx < 640 ? (const char*)wqkv + (64 * stg + wr_) * 3840 + (1 + sec) * 1280 + cgp * 640 + (pc - sec * 5) * 128
                        : (const char*)x + ((size_t)rg * 512 + (idx - 640)) * 1280 + stg * 128);
    };
    {
      __builtin_amdgcn_s_sleep(T1SLEEP);
      const int sl_ = l / 9;
      if (l < 63) hint1(2 + sl_, 9 * w + (l - 9 * sl_));
    }
    if (l == 0) asm volatile("s_mov_b32 m0, %1\n\ts_nop 0\n\tglobal_load_lds_dword %0, off sc1" ::"v"(ws + WS_EPOCH), "s"(L_SYNC + 32) : "memory");
    *(LAS v4u*)(L + L_BD + tid * 16) = v4u{0, 0, 0, 0};
    *(LAS v4u*)(L + L_BD + 4096 + tid * 16) = v4u{0, 0, 0, 0};
    if (tid < 2) *(LAS v4u*)(L + L_SYNC + tid * 16) = v4u{0, 0, 0, 0};
    if (tid == 2) *(LAS v4u*)(L + L_SYNC + 48) = v4u{0, 0, 0, 0};
    auto hint2 = [&](int i) {
      const int n = 96 * w + 16 * i + (l & 15);
      const int li = b * 228 + min(n, 227);
      const char* aw_ = li < 6400 ? (const char*)wo + li * 128 : li < 23680 ? (const char*)wg + (li - 6400) * 128
                      : li < 40960 ? (const char*)wu + (li - 23680) * 128 : (const char*)wd + min(li - 40960, 17279) * 128;
      const int m = n - 228;
      const unsigned o5 = m < 32 ? WS_O + ((8 * r + (m >> 2)) * 20 + 16 + (p >> 2)) * 1024 + ((p & 3) >> 1) * 512 + (m & 3) * 128
                        : m < 64 ? WS_H + ((4 * r2 + ((m - 32) >> 3)) * 20 + 16 + (cs >> 1)) * 1024 + (m & 7) * 128
                                 : WS_ACT + ((8 * r3 + (min(m - 64, 63) >> 3)) * 54 + 46 + (c3 >> 1)) * 1024 + (m & 7) * 128;
      const char* a = n < 228 ? aw_ : ws + o5;
      if (l < 16) touch64(a);
    };
    STAMP(1);
    f32x4 acc[2][5];
    {
      const int xw = w * 32 * 128;
      int xbs[3][2], kbs[3];
#pragma unroll
      for (int q = 0; q < 3; ++q) { xbs[q][0] = xw + xo0 + 2 * q * S1; xbs[q][1] = xw + (xo0 ^ 64) + 2 * q * S1; kbs[q] = kvo + 2 * q * S1; }
      asm volatile("" : "+v"(xbs[0][0]), "+v"(xbs[0][1]), "+v"(xbs[1][0]), "+v"(xbs[1][1]), "+v"(xbs[2][0]), "+v"(xbs[2][1]), "+v"(kbs[0]), "+v"(kbs[1]), "+v"(kbs[2]));
      auto rd1 = [&](FragKV& f, auto SLOT, auto KS, auto I) {
        constexpr int slot = SLOT, ks = KS, i = I;
        if constexpr (i < 2) f.x[i] = lds128(L, xbs[slot >> 1][ks] + (slot & 1) * S1 + i * 2048);
        else {
          constexpr int j = i - 2, t = j >> 1, h = j & 1;
          const v2u v = __builtin_bit_cast(v2u, __builtin_amdgcn_ds_read_tr16_b64_v4i16((LAS s16x4*)(L + kbs[slot >> 1] + (slot & 1) * S1 + ks * 32 * 160 + t * 32 + h * 8 * 160)));
          if constexpr (h == 0) { f.w[t].x = v.x; f.w[t].y = v.y; } else { f.w[t].z = v.x; f.w[t].w = v.y; }
        }
      };
      auto mma1 = [&](const FragKV& f, auto I, auto FIRST) { constexpr int i = I, t = i >> 1, s = i & 1; mfma_a<FIRST>(acc[s][t], f.x[s], f.w[t]); };
      FragKV A, B;
      LBAR();
      static_for<0, 12>([&](auto I) { rd1(A, IC<0>{}, IC<0>{}, I); });
      static_for<0, 10>([&](auto ST) {
        constexpr int st = ST;
        static_for<0, 10>([&](auto I) {
          constexpr int i = I;
          mma1(A, I, IC<(st == 0)>{});
          if constexpr (i < 6) { rd1(B, IC<(st % 5)>{}, IC<1>{}, IC<2 * i>{}); rd1(B, IC<(st % 5)>{}, IC<1>{}, IC<2 * i + 1>{}); }
          SB();
        });
        if constexpr (st < 9) LBAR();
        if constexpr (st == 0) STAMP(48);
        if constexpr (st == 3) STAMP(49);
        if constexpr (st == 6) STAMP(50);
        static_for<0, 10>([&](auto I) {
          constexpr int i = I;
          mma1(B, I, IC<0>{});
          if constexpr (st >= 2 && st < 8 && i == 8) hint2(st - 2);
          if constexpr (st < 9 && i < 6) { rd1(A, IC<((st + 1) % 5)>{}, IC<0>{}, IC<2 * i>{}); rd1(A, IC<((st + 1) % 5)>{}, IC<0>{}, IC<2 * i + 1>{}); }
          SB();
        });
      });
      MFMA_FENCE();
    }
    STAMP(2);
    __builtin_amdgcn_s_setprio(2);
    waitvm<(T2MODE == 1 ? 6 : 0)>();
    LBAR();
    STAMP(16);
    tag = __builtin_amdgcn_readfirstlane(*(const LAS unsigned*)(L + L_SYNC + 32)) + 1u;
    {
      f32x4 rstd[2], mr[2];
#pragma unroll
      for (int s = 0; s < 2; ++s) {
        rstd[s] = *(const LAS f32x4*)(L + L_STAT + (32 * w + 16 * s + 4 * g) * 4);
        mr[s] = *(const LAS f32x4*)(L + L_STAT + 512 + (32 * w + 16 * s + 4 * g) * 4);
      }
      float cw[5];
#pragma unroll
      for (int t = 0; t < 5; ++t) cw[t] = *(const LAS float*)(L + L_CSUM + (16 * t + c) * 4);
#pragma unroll
      for (int t = 0; t < 5; ++t) {
        unsigned o[4];
#pragma unroll
        for (int s = 0; s < 2; ++s)
#pragma unroll
          for (int h = 0; h < 2; ++h) {
            const f32x2 a2 = h ? hi2(acc[s][t]) : lo2(acc[s][t]), r2_ = h ? hi2(rstd[s]) : lo2(rstd[s]), m2 = h ? hi2(mr[s]) : lo2(mr[s]);
            f32x2 y;
            if (t < 2) y = phi2(pfma(a2, r2_, pfma(m2, sp2(cw[t]), sp2(1e-6f))));
            else if (t == 2) {
              const f32x2 te = pfma(a2, r2_, pfma(m2, sp2(cw[t]), sp2(1e-6f))), ph = phi2(te), v = te - sp2(1e-6f);
              y = c < 8 ? ph : v;
            } else y = pfma(a2, r2_, m2 * sp2(cw[t]));
            o[2 * s + h] = pk2v(y);
          }
        *(LAS v4u*)(L + L_KVF + (w * 5 + t) * 1024 + l * 16) = v4u{o[0], o[1], o[2], o[3]};
      }
    }
    STAMP(17);
    group_sync(L, 5, l);
    kv_product(w);
    kv_product(w + 4);
    group_sync(L, 6, l);
    STAMP(19);
    if (w == 0) {
      v4u v[4];
#pragma unroll
      for (int i = 0; i < 4; ++i) v[i] = lds128(L, L_KVC + min(i * 64 + l, 209) * 16);
#pragma unroll
      for (int i = 0; i < 4; ++i) stg_sc1(rs, i * 64 + l < 210 ? WS_KVP + (p * 16 + r) * KVP_STRIDE + (i * 64 + l) * 16 : 0x7FFFFFF0u, v[i]);
      STAMP(20);
      DRAIN();
      if (l == 0) set_flag(ws, WS_FKV + p * 256 + r * 4, tag);
      STAMP(3);
    }
    __builtin_amdgcn_s_setprio(0);
    const int f0 = min(7 * c3, 101);
    const int u3 = min(r3 + 16 * max(w - 1, 0), 39), ks3 = u3 >> 1, mat = u3 & 1;
    const int ks4 = min(r2 + 32 * max(w - 1, 0), 53);
    const unsigned area = L_KVF + max(w - 1, 0) * 12288;
    if (w > 0) {
      const char* w3 = (const char*)(mat ? wu : wg);
#pragma unroll
      for (int i = 0; i < 7; ++i) {
        const int n = 64 * i + l, pos = n / 14, ch = n - pos * 14;
        const int feat = ks3 < 16 ? 80 * (ks3 >> 1) + 32 * (ks3 & 1) + pos : 160 * (ks3 - 16) + 64 + (pos & 15) + 80 * (pos >> 4);
        glds(feat * 3456 + (f0 * 16 + 8 * ch) * 2, w3, area + i * 1024);
      }
      if (w < 3) {
#pragma unroll
        for (int i = 0; i < 5; ++i) {
          const int n = 64 * i + l, pos = n / 10, ch = n - pos * 10, t = pos >> 4;
          const int sp = ks4 < 45 ? ks4 / 3 : ks4 == 45 ? 15 : 2 * (ks4 - 46) + t;
          const int lt = ks4 < 45 ? 2 * (ks4 % 3) + t : ks4 == 45 ? 4 + t : 6;
          const int row = 16 * (min(7 * sp, 101) + lt) + (pos & 15);
          glds(row * 1280 + (80 * cs + 8 * ch) * 2, wd, area + 7168 + i * 1024);
        }
      }
    }
    if (w > 0) {
      if (w < 3) waitvm<5>(); else waitvm<0>();
      {
        const int tb = area + (4 * g + q4) * 224 + 8 * pp;
        v4u fr[7];
#pragma unroll
        for (int t = 0; t < 7; ++t) fr[t] = ldtr(L, tb + t * 32, 16 * 224);
        const bool ok = r3 + 16 * (w - 1) < 40;
        const unsigned ob = ok ? WS_W3 + ((c3 * 20 + ks3) * 14 + mat * 7) * 1024 + l * 16 : 0x7FFF0000u;
        float csel0 = 0.f, csel1 = 0.f;
#pragma unroll
        for (int t = 0; t < 7; ++t) {
          stg_sc1(rs, ob + t * 1024, fr[t]);
          const f32x4 cs_ = mfma(ONES, fr[t], f32x4{0, 0, 0, 0});
          if (t < 4) csel0 = (g == t) ? cs_[0] : csel0; else csel1 = (g == t - 4) ? cs_[0] : csel1;
        }
        const unsigned cb = ok ? WS_CS + (c3 * 40 + u3) * 448 + l * 4 : 0x7FFF0000u;
        stg1_sc1(rs, cb, __float_as_uint(csel0));
        stg1_sc1(rs, l < 48 ? cb + 256 : 0x7FFF0000u, __float_as_uint(csel1));
      }
      if (w < 3) {
        waitvm<9>();
        const int tb = area + 7168 + (4 * g + q4) * 160 + 8 * pp;
        v4u fr[5];
#pragma unroll
        for (int t = 0; t < 5; ++t) fr[t] = ldtr(L, tb + t * 32, 16 * 160);
        const unsigned ob = (r2 + 32 * (w - 1) < 54) ? WS_W4 + (cs * 54 + ks4) * 5 * 1024 + l * 16 : 0x7FFF0000u;
#pragma unroll
        for (int t = 0; t < 5; ++t) stg_sc1(rs, ob + t * 1024, fr[t]);
      }
    }
    if (w == 0) wait32(ws, WS_FW + cs * 256, tag, l);
    group_sync(L, 12, l);
    lds_spin(L, 4, 4);
    {
      const char* w2 = ws + WS_W2 + (size_t)cs * 100 * 1024 + w * 25 * 1024;
      static_for<0, 6>([&](auto I) { constexpr int i = I; gldsn_sc1<4, i * 4096>(l * 16, w2 + i * 4096, w * 25 * 1024); });
      gldsn_sc1<1, 24 * 1024>(l * 16, w2 + 24 * 1024, w * 25 * 1024);
    }
    DRAIN();
    if (l == 0 && lds_add(L, 3) == 3) { set_flag(ws, WS_FC + c3 * 256 + r3 * 4, tag); set_flag(ws, WS_FD + cs * 256 + r2 * 4, tag); }
    STAMPW(23, 1);
  } else {
    const int tw = w - 4;
    unsigned vx[4], vw[3];
#pragma unroll
    for (int i = 0; i < 4; ++i) { const int tok = 8 * (4 * tw + i) + (l >> 3); vx[i] = tok * 1280 + (((l & 7) ^ (tok & 7)) << 4); }
    const char* xb = (const char*)x + (size_t)r * 128 * 1280;
    const unsigned lxw = 4 * tw * 1024, lkw = 16384 + 160 * tw * 16;
    auto pieceX = [&](auto ST, auto I) { constexpr int st = ST, i = I; gldsi<(st % 5) * S1 + i * 1024>(vx[i], xb + st * 128, lxw); };
    static_for<0, 4>([&](auto I) { pieceX(IC<0>{}, I); });
    constexpr unsigned AREA_O = 133120;
    if (w == 7) {
      if (l == 0) asm volatile("s_mov_b32 m0, %1\n\ts_nop 0\n\tglobal_load_lds_dword %0, off sc1" ::"v"(ws + WS_EPOCH), "s"(L_SYNC + 36) : "memory");
      const int ks2 = min(r2, 19);
#pragma unroll
      for (int i = 0; i < 5; ++i) {
        const int n = 64 * i + l, pos = n / 10, ch = n - pos * 10, t = pos >> 4, gg = (pos >> 2) & 3;
        const int row = ks2 < 16 ? 40 * ks2 + pos : 40 * (4 * (ks2 - 16) + 2 * (gg >> 1) + t) + 32 + 4 * (gg & 1) + (pos & 3);
        glds(row * 1280 + (80 * cs + 8 * ch) * 2, wo, AREA_O + i * 1024);
      }
    }
#pragma unroll
    for (int j = 0; j < 3; ++j) {
      const int n = min(160 * tw + 64 * j + l, 639), pos = n / 10, ch = n - pos * 10;
      const int col = (ch < 5) ? 640 + 40 * p + 8 * ch : 1280 + 40 * p + 8 * (ch - 5);
      const int k = (pos & ~12) | ((pos & 4) << 1) | ((pos & 8) >> 1);
      vw[j] = k * 3840 + col * 2;
    }
    auto pieceW = [&](auto ST, auto J) {
      constexpr int st = ST, j = J;
      if constexpr (j < 2) gldsi<(st % 5) * S1 + j * 1024>(vw[j], (const char*)wqkv + (size_t)st * 64 * 3840, lkw);
      else { if (l < 32) gldsi<(st % 5) * S1 + j * 1024>(vw[j], (const char*)wqkv + (size_t)st * 64 * 3840, lkw); }
    };
    auto piece = [&](auto ST, auto Q) { constexpr int q = Q; if constexpr (q < 4) pieceX(ST, Q); else pieceW(ST, IC<q - 4>{}); };
    static_for<0, 3>([&](auto J) { pieceW(IC<0>{}, J); });
    static_for<0, 7>([&](auto Q) { piece(IC<1>{}, Q); });
    auto pieceQ = [&](auto QS, auto J) {
      constexpr int qs = QS, j = J, npc = qs < 2 ? 6 : 3;
      const int n = 64 * (tw * npc + j) + l, pos = n / 6, ch = n - pos * 6;
      const int k = 256 * qs + ((pos & ~12) | ((pos & 4) << 1) | ((pos & 8) >> 1));
      if (ch < 5) gldsi<qs * S1 + j * 1024>(k * 3840 + (40 * p + 8 * ch) * 2, wqkv, tw * npc * 1024);
    };
    STAMPW(51, 4);
    v4u xs[20][2];
    f32x4 sq[2], sm[2], cs0, cs1;
    v4u ones_ = ONES;
    asm volatile("" : "+v"(ones_));
    {
      const int xw = tw * 32 * 128;
      int xbs[3][2], ebs[3][2];
#pragma unroll
      for (int q = 0; q < 3; ++q) { xbs[q][0] = xw + xo0 + 2 * q * S1; xbs[q][1] = xw + (xo0 ^ 64) + 2 * q * S1; ebs[q][0] = kvo + tw * 32 + 2 * q * S1; ebs[q][1] = kvo + 4 * 32 + 2 * q * S1; }
      asm volatile("" : "+v"(xbs[0][0]), "+v"(xbs[0][1]), "+v"(xbs[1][0]), "+v"(xbs[1][1]), "+v"(xbs[2][0]), "+v"(xbs[2][1]),
                        "+v"(ebs[0][0]), "+v"(ebs[0][1]), "+v"(ebs[1][0]), "+v"(ebs[1][1]), "+v"(ebs[2][0]), "+v"(ebs[2][1]));
      v4u e[2][2];
      auto rd1 = [&](auto KK, auto I) {
        constexpr int kk = KK, i = I, slot = (kk >> 1) % 5, ks = kk & 1;
        if constexpr (i < 2) xs[kk][i] = lds128(L, xbs[slot >> 1][ks] + (slot & 1) * S1 + i * 2048);
        else {
          constexpr int t = (i - 2) >> 1, h = i & 1;
          const v2u v = __builtin_bit_cast(v2u, __builtin_amdgcn_ds_read_tr16_b64_v4i16((LAS s16x4*)(L + ebs[slot >> 1][t] + (slot & 1) * S1 + ks * 32 * 160 + h * 8 * 160)));
          if constexpr (h == 0) { e[kk & 1][t].x = v.x; e[kk & 1][t].y = v.y; } else { e[kk & 1][t].z = v.x; e[kk & 1][t].w = v.y; }
        }
      };
      auto mma1 = [&](auto KK, auto I) {
        constexpr int kk = KK, i = I;
        if constexpr (i < 2) mfma_a<(kk == 0)>(sq[i], xs[kk][i], xs[kk][i]);
        else if constexpr (i < 4) mfma_a<(kk == 0)>(sm[i - 2], ones_, xs[kk][i - 2]);
        else if constexpr (i == 4) mfma_a<(kk == 0)>(cs0, ones_, e[kk & 1][0]);
        else mfma_a<(kk == 0)>(cs1, ones_, e[kk & 1][1]);
      };
      waitbar<7>();
      if (w == 7) {
        v4u fr[5];
#pragma unroll
        for (int t = 0; t < 5; ++t) fr[t] = ldtr(L, AREA_O + (4 * g + q4) * 160 + 8 * pp + t * 32, 16 * 160);
        const unsigned ob = r2 < 20 ? WS_W2 + (cs * 20 + min(r2, 19)) * 5 * 1024 + l * 16 : 0x7FFF0000u;
#pragma unroll
        for (int t = 0; t < 5; ++t) stg_sc1(rs, ob + t * 1024, fr[t]);
      }
      static_for<0, 6>([&](auto I) { rd1(IC<0>{}, I); });
      static_for<0, 10>([&](auto ST) {
        constexpr int st = ST;
        static_for<0, 6>([&](auto I) {
          constexpr int i = I;
          mma1(IC<2 * st>{}, I);
          rd1(IC<2 * st + 1>{}, I);
          if constexpr (st == 0) { piece(IC<2>{}, I); if constexpr (i == 5) piece(IC<2>{}, IC<6>{}); }
          if constexpr (st == 1) { piece(IC<4>{}, I); if constexpr (i == 5) piece(IC<4>{}, IC<6>{}); }
          SB();
        });
        if constexpr (st < 9) { if constexpr (st == 0) waitbar<7>(); else if constexpr (st <= 6) waitbar<14>(); else if constexpr (st == 7) waitbar<13>(); else waitbar<12>(); }
        if constexpr (st == 6) {
          if (w == 7 && l == 0) set_flag(ws, WS_FW + cs * 256 + r2 * 4, *(const LAS unsigned*)(L + L_SYNC + 36) + 1u);
        }
        static_for<0, 6>([&](auto I) {
          constexpr int i = I;
          mma1(IC<2 * st + 1>{}, I);
          if constexpr (st == 0) { piece(IC<3>{}, I); if constexpr (i == 5) piece(IC<3>{}, IC<6>{}); }
          if constexpr (st >= 1 && st <= 5) { piece(IC<st + 4>{}, I); if constexpr (i == 5) piece(IC<st + 4>{}, IC<6>{}); }
          if constexpr (st == 6 || st == 7) pieceQ(IC<st - 6>{}, I);
          if constexpr (st == 8 && i < 3) pieceQ(IC<2>{}, I);
          if constexpr (st < 9) rd1(IC<2 * st + 2>{}, I);
          SB();
        });
      });
      MFMA_FENCE();
    }
#pragma unroll
    for (int s = 0; s < 2; ++s) {
      const int e_ = c & 3;
      const float v = e_ == 0 ? sq[s][0] : e_ == 1 ? sq[s][1] : e_ == 2 ? sq[s][2] : sq[s][3];
      const float m = sm[s][0] * (1.0f / 640.0f);
      const float rs_ = __builtin_amdgcn_rsqf(fmaxf(v * (1.0f / 640.0f) - m * m + 1e-5f, 1e-5f));
      const bool own = g == (c >> 2);
      *(LAS float*)(L + (own ? L_STAT + (32 * tw + 16 * s + c) * 4 : L_SYNC + 512 + l * 4)) = rs_;
      *(LAS float*)(L + (own ? L_STAT + 512 + (32 * tw + 16 * s + c) * 4 : L_SYNC + 768 + l * 4)) = -m * rs_;
    }
    *(LAS float*)(L + (g == 0 ? L_CSUM + (16 * tw + c) * 4 : L_SYNC + 512 + l * 4)) = cs0[0];
    *(LAS float*)(L + (g == 0 ? L_CSUM + (64 + c) * 4 : L_SYNC + 512 + l * 4)) = cs1[0];
    LBAR();
    tag = __builtin_amdgcn_readfirstlane(*(const LAS unsigned*)(L + L_SYNC + 32)) + 1u;
    DRAIN();
    group_sync(L, 7, l);
    lds_spin(L, 5, 4);
    f32x4 qacc[3][2], csq;
    {
      int qbs[3];
#pragma unroll
      for (int q = 0; q < 3; ++q) qbs[q] = q * S1 + wrow * 96 + 8 * pp;
      asm volatile("" : "+v"(qbs[0]), "+v"(qbs[1]), "+v"(qbs[2]));
      int cbs[3];
#pragma unroll
      for (int q = 0; q < 3; ++q) cbs[q] = qbs[q] + (tw < 3 ? tw : 2) * 32;
      asm volatile("" : "+v"(cbs[0]), "+v"(cbs[1]), "+v"(cbs[2]));
      v4u f[2][4];
      auto rd1 = [&](auto KK, auto I) {
        constexpr int kk = KK, i = I, t = i >> 1, h = i & 1;
        const int base = t < 3 ? qbs[kk >> 3] + t * 32 : cbs[kk >> 3];
        const v2u v = __builtin_bit_cast(v2u, __builtin_amdgcn_ds_read_tr16_b64_v4i16((LAS s16x4*)(L + base + (kk & 7) * 32 * 96 + h * 8 * 96)));
        if constexpr (h == 0) { f[kk & 1][t].x = v.x; f[kk & 1][t].y = v.y; } else { f[kk & 1][t].z = v.x; f[kk & 1][t].w = v.y; }
      };
      static_for<0, 8>([&](auto I) { rd1(IC<0>{}, I); });
      static_for<0, 20>([&](auto KK) {
        constexpr int kk = KK;
        static_for<0, 7>([&](auto I) {
          constexpr int i = I;
          if constexpr (i < 6) mfma_a<(kk == 0)>(qacc[i >> 1][i & 1], f[kk & 1][i >> 1], xs[kk][i & 1]);
          else mfma_a<(kk == 0)>(csq, ones_, f[kk & 1][3]);
          if constexpr (kk < 19) { rd1(IC<kk + 1>{}, I); if constexpr (i == 6) rd1(IC<kk + 1>{}, IC<7>{}); }
          SB();
        });
      });
      MFMA_FENCE();
    }
    *(LAS float*)(L + (g == 0 && tw < 3 ? L_CSUM + (80 + 16 * tw + c) * 4 : L_SYNC + 512 + l * 4)) = csq[0];
    asm volatile("s_waitcnt lgkmcnt(0)" ::: "memory");
    if (l == 0) lds_add(L, 4);
    lds_spin(L, 4, 4);
    v4u pqA[2], pqB[2];
#pragma unroll
    for (int s = 0; s < 2; ++s) {
      const float rs_ = *(const LAS float*)(L + L_STAT + (32 * tw + 16 * s + c) * 4);
      const float mr = *(const LAS float*)(L + L_STAT + 512 + (32 * tw + 16 * s + c) * 4);
      unsigned pq[6];
#pragma unroll
      for (int t = 0; t < 3; ++t) {
        const f32x4 cw = *(const LAS f32x4*)(L + L_CSUM + (80 + 16 * t + 4 * g) * 4);
        pq[2 * t] = pk2v(phi2(pfma(lo2(qacc[t][s]), sp2(rs_), pfma(lo2(cw), sp2(mr), sp2(1e-6f)))));
        pq[2 * t + 1] = pk2v(phi2(pfma(hi2(qacc[t][s]), sp2(rs_), pfma(hi2(cw), sp2(mr), sp2(1e-6f)))));
      }
      pqA[s] = v4u{pq[0], pq[1], pq[2], pq[3]};
      pqB[s] = v4u{g < 2 ? pq[4] : 0u, g < 2 ? pq[5] : 0u, 0u, 0u};
    }
    asm volatile("" : "+v"(pqA[0]), "+v"(pqA[1]), "+v"(pqB[0]), "+v"(pqB[1]));
    STAMPW(22, 4);
    if (w == 4) { wait16(ws, WS_FKV + p * 256, tag, l); if (l == 0) lds_add(L, 0); } else lds_spin(L, 0, 1);
    STAMPW(21, 4);
    {
      const int t = min(tid - 256, 209);
      v4u v[16];
#pragma unroll
      for (int i = 0; i < 16; ++i) v[i] = ldg_sc1(rs, WS_KVP + (p * 16 + i) * KVP_STRIDE + t * 16);
      f32x4 s = {0, 0, 0, 0};
#pragma unroll
      for (int i = 0; i < 16; ++i) s += __builtin_bit_cast(f32x4, v[i]);
      const v2u pk = {pk2v(lo2(s)), pk2v(hi2(s))};
      const int fo = t / 5, f = t < 200 ? 4 * (t - fo * 5) + (fo >= 20 ? 20 : 0) : 4 * (t - 200);
      const int kst = f >> 5, tt = (f >> 4) & 1, gg = (f >> 2) & 3;
      if (t < 200) *(LAS v2u*)(L + L_BD + ((fo >> 4) * 2 + kst) * 1024 + (gg * 16 + (fo & 15)) * 16 + tt * 8) = pk;
      else {
#pragma unroll
        for (int i = 0; i < 8; ++i) *(LAS v2u*)(L + L_BD + (6 + kst) * 1024 + (gg * 16 + 2 * i + (f >= 20)) * 16 + tt * 8) = pk;
      }
    }
    asm volatile("s_waitcnt lgkmcnt(0)" ::: "memory");
    if (l == 0) lds_add(L, 1);
    lds_spin(L, 1, 4);
    STAMPW(24, 4);
    v4u bd[8];
#pragma unroll
    for (int i = 0; i < 8; ++i) bd[i] = lds128(L, L_BD + i * 1024 + l * 16);
#pragma unroll
    for (int s = 0; s < 2; ++s) {
      f32x4 den = mfma(bd[6], pqA[s], f32x4{0, 0, 0, 0}); den = mfma(bd[7], pqB[s], den);
      f32x4 num[3];
#pragma unroll
      for (int ft = 0; ft < 3; ++ft) { num[ft] = mfma(bd[2 * ft], pqA[s], f32x4{0, 0, 0, 0}); num[ft] = mfma(bd[2 * ft + 1], pqB[s], num[ft]); }
      const float r0 = __builtin_amdgcn_rcpf(den[0] + 1e-6f), r1 = __builtin_amdgcn_rcpf(den[1] + 1e-6f);
      const float rm = g == 0 ? r0 : r1;
      const unsigned base = WS_O + (8 * r + 2 * tw + s) * 20 * 1024;
      stg_sc1(rs, base + p * 1024 + l * 16, v4u{pk2v(lo2(num[0]) * r0), pk2v(hi2(num[0]) * r0), pk2v(lo2(num[1]) * rm), pk2v(hi2(num[1]) * rm)});
      stg2_sc1(rs, g < 2 ? base + (16 + (p >> 2)) * 1024 + ((2 * ((p & 3) >> 1) + g) * 16 + c) * 16 + (p & 1) * 8 : 0x7FFFFFF0u,
               v2u{pk2v(lo2(num[2]) * r1), pk2v(hi2(num[2]) * r1)});
    }
    STAMPW(26, 4);
    DRAIN();
    STAMPW(27, 4);
    if (l == 0 && lds_add(L, 2) == 3) set_flag(ws, WS_FO + r * 256 + p * 4, tag);
  }
  LBAR();
  STAMP(5);

  const int th = w & 1, kq = w >> 1;
  const int sl = 2 * th + (kq & 1);
  const int tok = 16 * (4 * r2 + sl) + c;
  {
    v2u xr[3];
    const __amdgpu_buffer_rsrc_t rsx = __builtin_amdgcn_make_buffer_rsrc((void*)x, 0, 2048 * 640 * 2, 0x00020000);
#pragma unroll
    for (int i = 0; i < 3; ++i) {
      const int T = kq < 2 ? (i == 2 ? 4 : i) : (i == 2 ? 2 : 2 + i);
      xr[i] = __builtin_amdgcn_raw_buffer_load_b64(rsx, (tok * 640 + 80 * cs + 16 * T + 4 * g) * 2, 0, 0);
    }
    v4u wf[5][5];
#pragma unroll
    for (int k = 0; k < 5; ++k)
#pragma unroll
      for (int T = 0; T < 5; ++T) wf[k][T] = lds128(L, ((5 * kq + k) * 5 + T) * 1024 + l * 16);
#pragma unroll
    for (int k = 0; k < 5; ++k) asm volatile("" : "+v"(wf[k][0]), "+v"(wf[k][1]), "+v"(wf[k][2]), "+v"(wf[k][3]), "+v"(wf[k][4]));
    STAMP(28);
    if (w == 0) wait16(ws, WS_FO + (r2 >> 1) * 256, tag, l);
    if (w == 1) wait16(ws, WS_FC + c3 * 256, tag, l);
    if (w == 2) wait32(ws, WS_FD + cs * 256, tag, l);
    LBAR();
    STAMP(6);
    if (b == 0 && tid == 0) set_flag(ws, WS_EPOCH, tag);
    v4u xf[5][2];
#pragma unroll
    for (int k = 0; k < 5; ++k)
#pragma unroll
      for (int s = 0; s < 2; ++s) xf[k][s] = ldg_sc1(rs, WS_O + ((4 * r2 + 2 * th + s) * 20 + 5 * kq + k) * 1024 + l * 16);
    f32x4 acc[5][2];
#pragma unroll
    for (int T = 0; T < 5; ++T) { acc[T][0] = f32x4{0, 0, 0, 0}; acc[T][1] = f32x4{0, 0, 0, 0}; }
    SB();
#pragma unroll
    for (int k = 0; k < 5; ++k)
#pragma unroll
      for (int T = 0; T < 5; ++T)
#pragma unroll
        for (int s = 0; s < 2; ++s) acc[T][s] = mfma(wf[k][T], xf[k][s], acc[T][s]);
    STAMP(7);
    f32x4 own[3];
    splitk_exchange(L, acc, own, th, kq, l);
    STAMP(29);
    const bool hi = kq >= 2;
    unsigned hp[3][2];
    f32x2 sm2 = {0.f, 0.f}, sq2 = {0.f, 0.f};
#pragma unroll
    for (int i = 0; i < 3; ++i) {
      hp[i][0] = pk2v(f32x2{bflo(xr[i].x), bfhi(xr[i].x)} + lo2(own[i]));
      hp[i][1] = pk2v(f32x2{bflo(xr[i].y), bfhi(xr[i].y)} + hi2(own[i]));
      if (i == 2 && hi) { hp[2][0] = 0u; hp[2][1] = 0u; }
      const f32x2 a0 = {bflo(hp[i][0]), bfhi(hp[i][0])}, a1 = {bflo(hp[i][1]), bfhi(hp[i][1])};
      sm2 += a0 + a1;
      sq2 = pfma(a0, a0, pfma(a1, a1, sq2));
    }
    float sm = sm2.x + sm2.y, sq = sq2.x + sq2.y;
    const unsigned hb = WS_H + (4 * r2 + sl) * 20 * 1024;
    stg_sc1(rs, hb + (2 * cs + (kq >> 1)) * 1024 + l * 16, v4u{hp[0][0], hp[0][1], hp[1][0], hp[1][1]});
    stg2_sc1(rs, hi ? 0x7FFFFFF0u : hb + (16 + (cs >> 1)) * 1024 + l * 16 + (cs & 1) * 8, v2u{hp[2][0], hp[2][1]});
    sm = rowsum4(sm); sq = rowsum4(sq);
    stg2_sc1(rs, g == 0 ? WS_HS + ((4 * r2 + sl) * 16 + cs * 2 + (kq >> 1)) * 128 + c * 8 : 0x7FFFFFF0u, v2u{__float_as_uint(sm), __float_as_uint(sq)});
    *(LAS v2u*)(L + L_HT + (sl * 5 + (hi ? 2 : 0)) * 512 + l * 8) = v2u{hp[0][0], hp[0][1]};
    *(LAS v2u*)(L + L_HT + (sl * 5 + (hi ? 3 : 1)) * 512 + l * 8) = v2u{hp[1][0], hp[1][1]};
    *(LAS v2u*)(L + (hi ? L_SYNC + 512 : L_HT + (sl * 5 + 4) * 512) + l * 8) = v2u{hp[2][0], hp[2][1]};
    STAMP(30);
    DRAIN();
    STAMP(31);
    LBAR();
    if (tid == 0) set_flag(ws, WS_FH + (r2 >> 1) * 256 + ((r2 & 1) * 8 + cs) * 4, tag);
    STAMP(8);
  }
  {
    if (w < 4) p3_body<4, 0, false>(L, ws, rs, r3, c3, cs | ((r2 & 3) << 8), w, l, tag);
    else       p3_body<3, 4, true>(L, ws, rs, r3, c3, cs | ((r2 & 3) << 8), w, l, tag);
    STAMP(36);
    DRAIN();
    STAMP(37);
    LBAR();
    if (tid == 0) set_flag(ws, WS_FA + r3 * 256 + c3 * 4, tag);
    STAMP(12);
  }
  {
    f32x4 acc[5][2];
    if (th == 0) p4_main<0>(L, ws, r2 >> 1, r2, cs, kq, w, l, tag, acc); else p4_main<1>(L, ws, r2 >> 1, r2, cs, kq, w, l, tag, acc);
    LBAR();
    STAMP(14);
    f32x4 own[3];
    splitk_exchange(L, acc, own, th, kq, l);
    const bool hi = kq >= 2;
    LBAR();
#pragma unroll
    for (int i = 0; i < 3; ++i) {
      const int T = i == 2 ? 4 : (hi ? 2 : 0) + i;
      const v2u hh = *(const LAS v2u*)(L + L_HT + (sl * 5 + T) * 512 + l * 8);
      const v2u o = {pk2v(f32x2{bflo(hh.x), bfhi(hh.x)} + lo2(own[i])), pk2v(f32x2{bflo(hh.y), bfhi(hh.y)} + hi2(own[i]))};
      *(LAS v2u*)(L + ((i < 2 || !hi) ? (16 * sl + c) * 176 + T * 32 + g * 8 : 16384 + l * 8)) = o;
    }
    LBAR();
    {
      const __amdgpu_buffer_rsrc_t rso = __builtin_amdgcn_make_buffer_rsrc((void*)out, 0, 2048 * 640 * 2, 0x00020000);
#pragma unroll
      for (int i = 0; i < 2; ++i) {
        const int n = i * 512 + tid, row = n / 10, ch = n - row * 10;
        const v4u v = lds128(L, min(row, 63) * 176 + ch * 16);
        __builtin_amdgcn_raw_buffer_store_b128(v, rso, n < 640 ? ((64 * r2 + row) * 640 + 80 * cs) * 2 + ch * 16 : 0x7FFFFFF0u, 0, OUTAUX);
      }
    }
    STAMP(15);
  }
}

extern "C" void kernel_launch(void* const* d_in, const int* in_sizes, int n_in, void* d_out, int out_size, void* d_ws, size_t ws_size,
                              hipStream_t stream) {
  (void)in_sizes; (void)n_in; (void)out_size; (void)ws_size;
  (void)hipFuncSetAttribute((const void*)fused_block, hipFuncAttributeMaxDynamicSharedMemorySize, LDS_BYTES);
  fused_block<<<256, 512, LDS_BYTES, stream>>>((const u16*)d_in[0], (const u16*)d_in[1], (const u16*)d_in[2], (const u16*)d_in[4],
                                                (const u16*)d_in[5], (const u16*)d_in[6], (u16*)d_out, (char*)d_ws);
}
```

```cpp
#include <hip/hip_runtime.h>
#include <hip/hip_bf16.h>
#include <stdint.h>
#include <type_traits>

#define LAS __attribute__((address_space(3)))
#define GAS __attribute__((address_space(1)))
typedef __bf16 bf16x8 __attribute__((ext_vector_type(8)));
typedef __bf16 bf16x2 __attribute__((ext_vector_type(2)));
typedef short s16x4 __attribute__((ext_vector_type(4)));
typedef float f32x4 __attribute__((ext_vector_type(4)));
typedef float f32x2 __attribute__((ext_vector_type(2)));
typedef unsigned v4u __attribute__((ext_vector_type(4)));
typedef unsigned v2u __attribute__((ext_vector_type(2)));
typedef GAS unsigned gu32;
typedef unsigned short u16;
#define RLX_AGENT __ATOMIC_RELAXED, __HIP_MEMORY_SCOPE_AGENT

constexpr unsigned WS_EPOCH = 0, WS_TMO = 256;
constexpr unsigned WS_FKV = 4096, WS_FO = 8192, WS_FH = 12288, WS_FA = 16384, WS_FC = 20480, WS_FD = 24576, WS_FW = 28672;
constexpr unsigned WS_KVP = 32768, KVP_STRIDE = 3456;
constexpr unsigned WS_O = WS_KVP + 256 * KVP_STRIDE;
constexpr unsigned WS_H = WS_O + 2621440;
constexpr unsigned WS_HS = WS_H + 2621440;
constexpr unsigned WS_ACT = WS_HS + 262144;
constexpr unsigned WS_CS = WS_ACT + 7077888;
constexpr unsigned WS_W3 = WS_CS + 16 * 40 * 448;
constexpr unsigned WS_W4 = WS_W3 + 16 * 280 * 1024;
constexpr unsigned WS_W2 = WS_W4 + 8 * 270 * 1024;
constexpr unsigned WS_STAMP = WS_W2 + 8 * 100 * 1024;
constexpr unsigned WS_END = WS_STAMP + 256 * 64 * 8;

constexpr int LDS_BYTES = 163840;
constexpr int L_KVF = 102400;
constexpr int L_KVC = 135168;
constexpr int L_BD = 138752;
constexpr int L_STAT = 146944;
constexpr int L_CSUM = 147968;
constexpr int L_SIDE = 148480;
constexpr int L_CS3 = 151168;
constexpr int L_SYNC = 152064;
constexpr int L_HT = 153600;
constexpr int L_RED = 0;
constexpr int L_X3 = 86016;

#define STAMPS 0
#define T2MODE 1
#define WOMODE 1
#define T1MODE 4
#define T2POS 0
#define T1SLEEP 16
#define OUTAUX 16
#define T3MODE 1
#define T0KB 52

__device__ __forceinline__ void glds(unsigned voff, const void* sbase, unsigned ldsaddr) {
  asm volatile("s_mov_b32 m0, %2\n\ts_nop 0\n\tglobal_load_lds_dwordx4 %0, %1" ::"v"(voff), "s"(sbase), "s"(ldsaddr) : "memory");
}
__device__ __forceinline__ void glds2_sc1(unsigned voff, const void* sbase, unsigned ldsaddr) {
  asm volatile("s_mov_b32 m0, %2\n\ts_nop 0\n\tglobal_load_lds_dwordx4 %0, %1 sc1\n\tglobal_load_lds_dwordx4 %0, %1 offset:1024 sc1" ::"v"(voff), "s"(sbase), "s"(ldsaddr) : "memory");
}
template <int IMM> __device__ __forceinline__ void gldsi(unsigned voff, const void* sbase, unsigned lbase) {
  asm volatile("s_add_u32 m0, %2, %3\n\ts_nop 0\n\tglobal_load_lds_dwordx4 %0, %1" ::"v"(voff), "s"(sbase), "s"(lbase), "i"(IMM) : "memory", "scc");
}
template <int IMM> __device__ __forceinline__ void glds2i_sc1(unsigned voff, const void* sbase, unsigned lbase) {
  asm volatile("s_add_u32 m0, %2, %3\n\ts_nop 0\n\tglobal_load_lds_dwordx4 %0, %1 sc1\n\tglobal_load_lds_dwordx4 %0, %1 offset:1024 sc1" ::"v"(voff), "s"(sbase), "s"(lbase), "i"(IMM) : "memory", "scc");
}
template <int N, int IMM> __device__ __forceinline__ void gldsn_sc1(unsigned voff, const void* sbase, unsigned lbase) {
  static_assert(N >= 1 && N <= 4, "");
  if constexpr (N == 1) asm volatile("s_add_u32 m0, %2, %3\n\ts_nop 0\n\tglobal_load_lds_dwordx4 %0, %1 sc1" ::"v"(voff), "s"(sbase), "s"(lbase), "i"(IMM) : "memory", "scc");
  if constexpr (N == 2) asm volatile("s_add_u32 m0, %2, %3\n\ts_nop 0\n\tglobal_load_lds_dwordx4 %0, %1 sc1\n\tglobal_load_lds_dwordx4 %0, %1 offset:1024 sc1" ::"v"(voff), "s"(sbase), "s"(lbase), "i"(IMM) : "memory", "scc");
  if constexpr (N == 3) asm volatile("s_add_u32 m0, %2, %3\n\ts_nop 0\n\tglobal_load_lds_dwordx4 %0, %1 sc1\n\tglobal_load_lds_dwordx4 %0, %1 offset:1024 sc1\n\tglobal_load_lds_dwordx4 %0, %1 offset:2048 sc1" ::"v"(voff), "s"(sbase), "s"(lbase), "i"(IMM) : "memory", "scc");
  if constexpr (N == 4) asm volatile("s_add_u32 m0, %2, %3\n\ts_nop 0\n\tglobal_load_lds_dwordx4 %0, %1 sc1\n\tglobal_load_lds_dwordx4 %0, %1 offset:1024 sc1\n\tglobal_load_lds_dwordx4 %0, %1 offset:2048 sc1\n\tglobal_load_lds_dwordx4 %0, %1 offset:3072 sc1" ::"v"(voff), "s"(sbase), "s"(lbase), "i"(IMM) : "memory", "scc");
}
template <int IMM, int OFF> __device__ __forceinline__ void gldso_sc1(unsigned voff, const void* sbase, unsigned lbase) {
  asm volatile("s_add_u32 m0, %2, %3\n\ts_nop 0\n\tglobal_load_lds_dwordx4 %0, %1 offset:%4 sc1" ::"v"(voff), "s"(sbase), "s"(lbase), "i"(IMM), "i"(OFF) : "memory", "scc");
}
__device__ __forceinline__ void ldg_asm_sc1(v4u& d, unsigned voff, const void* sbase) {
  asm volatile("global_load_dwordx4 %0, %1, %2 sc1" : "=v"(d) : "v"(voff), "s"(sbase) : "memory");
}
__device__ __forceinline__ void ldg2_asm_sc1(v2u& d, unsigned voff, const void* sbase) {
  asm volatile("global_load_dwordx2 %0, %1, %2 sc1" : "=v"(d) : "v"(voff), "s"(sbase) : "memory");
}
template <int N> __device__ __forceinline__ void wait_for(v4u& a, v4u& b) { asm volatile("s_waitcnt vmcnt(%2)" : "+v"(a), "+v"(b) : "i"(N) : "memory"); }
template <int N> __device__ __forceinline__ void waitvm() { asm volatile("s_waitcnt vmcnt(%0)" ::"i"(N) : "memory"); }
__device__ __forceinline__ f32x2 pfma(f32x2 a, f32x2 b, f32x2 c) { return __builtin_elementwise_fma(a, b, c); }
__device__ __forceinline__ f32x2 sp2(float a) { return f32x2{a, a}; }
__device__ __forceinline__ f32x2 lo2(f32x4 v) { return __builtin_shufflevector(v, v, 0, 1); }
__device__ __forceinline__ f32x2 hi2(f32x4 v) { return __builtin_shufflevector(v, v, 2, 3); }
__device__ __forceinline__ unsigned pk2v(f32x2 v) { return __builtin_bit_cast(unsigned, __builtin_convertvector(v, bf16x2)); }
__device__ __forceinline__ f32x2 phi2(f32x2 te) {
  const f32x2 y = pfma(te, sp2(1.44269504088896f), sp2(-1.44269504088896e-6f));
  const f32x2 e = {__builtin_amdgcn_fmed3f(__builtin_amdgcn_exp2f(y.x), 0.f, 1.f), __builtin_amdgcn_fmed3f(__builtin_amdgcn_exp2f(y.y), 0.f, 1.f)};
  const f32x2 m = {fmaxf(te.x, 1e-6f), fmaxf(te.y, 1e-6f)};
  return m + e;
}
__device__ __forceinline__ unsigned geglu2(f32x2 ag, f32x2 au, f32x2 cg, f32x2 cu, float rstd, float mr) {
  const f32x2 gv = pfma(ag, sp2(rstd), cg * sp2(mr)), uv = pfma(au, sp2(rstd), cu * sp2(mr));
  const f32x2 z = gv * pfma(gv * gv, sp2(-0.1029432397f), sp2(-2.302208198f));
  const f32x2 d = {1.0f + __builtin_amdgcn_exp2f(z.x), 1.0f + __builtin_amdgcn_exp2f(z.y)};
  const f32x2 rc = {__builtin_amdgcn_rcpf(d.x), __builtin_amdgcn_rcpf(d.y)};
  return pk2v(gv * uv * rc);
}
template <int V> using IC = std::integral_constant<int, V>;
template <int I, int N, class F> __device__ __forceinline__ void static_for(F&& f) {
  if constexpr (I < N) { f(std::integral_constant<int, I>{}); static_for<I + 1, N>(f); }
}
#define LBAR() asm volatile("s_waitcnt lgkmcnt(0)\n\ts_barrier" ::: "memory")
#define DRAIN() asm volatile("s_waitcnt vmcnt(0)" ::: "memory")
#define SB() __builtin_amdgcn_sched_barrier(0)
template <int N> __device__ __forceinline__ void waitbar() { asm volatile("s_waitcnt vmcnt(%0) lgkmcnt(0)\n\ts_barrier" ::"i"(N) : "memory"); }

__device__ __forceinline__ unsigned pk2(float a, float b) {
  bf16x2 v = {(__bf16)a, (__bf16)b};
  return __builtin_bit_cast(unsigned, v);
}
__device__ __forceinline__ float bflo(unsigned u) { return __uint_as_float(u << 16); }
__device__ __forceinline__ float bfhi(unsigned u) { return __uint_as_float(u & 0xffff0000u); }
__device__ __forceinline__ f32x4 mfma(v4u a, v4u b, f32x4 c) {
  return __builtin_amdgcn_mfma_f32_16x16x32_bf16(__builtin_bit_cast(bf16x8, a), __builtin_bit_cast(bf16x8, b), c, 0, 0, 0);
}
__device__ __forceinline__ void mfma_acc(f32x4& acc, v4u a, v4u b) {
  asm volatile("v_mfma_f32_16x16x32_bf16 %0, %1, %2, %0" : "+v"(acc) : "v"(a), "v"(b));
}
__device__ __forceinline__ void mfma_first(f32x4& acc, v4u a, v4u b) {
  asm volatile("v_mfma_f32_16x16x32_bf16 %0, %1, %2, 0" : "=&v"(acc) : "v"(a), "v"(b));
}
template <bool FIRST> __device__ __forceinline__ void mfma_a(f32x4& acc, v4u a, v4u b) { if constexpr (FIRST) mfma_first(acc, a, b); else mfma_acc(acc, a, b); }
#define MFMA_FENCE() asm volatile("s_nop 7\n\ts_nop 1" ::: "memory")
__device__ __forceinline__ v4u lds128(LAS char* L, int off) { return *(const LAS v4u*)(L + off); }
__device__ __forceinline__ v4u ldtr(LAS char* L, int off, int d) {
  s16x4 a = __builtin_amdgcn_ds_read_tr16_b64_v4i16((LAS s16x4*)(L + off));
  s16x4 b = __builtin_amdgcn_ds_read_tr16_b64_v4i16((LAS s16x4*)(L + off + d));
  v2u x = __builtin_bit_cast(v2u, a), y = __builtin_bit_cast(v2u, b);
  return v4u{x.x, x.y, y.x, y.y};
}
__device__ __forceinline__ float phi_eps(float te) {
  const float e = __builtin_amdgcn_fmed3f(__builtin_amdgcn_exp2f((te - 1e-6f) * 1.44269504088896f), 0.f, 1.f);
  return fmaxf(te, 1e-6f) + e;
}
__device__ __forceinline__ float rowsum4(float v) {
  auto a = __builtin_amdgcn_permlane16_swap(__float_as_uint(v), __float_as_uint(v), false, false);
  const float u = __uint_as_float(a[0]) + __uint_as_float(a[1]);
  auto b = __builtin_amdgcn_permlane32_swap(__float_as_uint(u), __float_as_uint(u), false, false);
  return __uint_as_float(b[0]) + __uint_as_float(b[1]);
}
__device__ __forceinline__ v4u ldg_sc1(__amdgpu_buffer_rsrc_t rs, unsigned off) { return __builtin_amdgcn_raw_buffer_load_b128(rs, off, 0, 16); }
__device__ __forceinline__ v2u ldg2_sc1(__amdgpu_buffer_rsrc_t rs, unsigned off) { return __builtin_amdgcn_raw_buffer_load_b64(rs, off, 0, 16); }
__device__ __forceinline__ unsigned ldg1_sc1(__amdgpu_buffer_rsrc_t rs, unsigned off) { return __builtin_amdgcn_raw_buffer_load_b32(rs, off, 0, 16); }
__device__ __forceinline__ void stg_sc1(__amdgpu_buffer_rsrc_t rs, unsigned off, v4u v) { __builtin_amdgcn_raw_buffer_store_b128(v, rs, off, 0, 16); }
__device__ __forceinline__ void stg2_sc1(__amdgpu_buffer_rsrc_t rs, unsigned off, v2u v) { __builtin_amdgcn_raw_buffer_store_b64(v, rs, off, 0, 16); }
__device__ __forceinline__ void stg1_sc1(__amdgpu_buffer_rsrc_t rs, unsigned off, unsigned v) { __builtin_amdgcn_raw_buffer_store_b32(v, rs, off, 0, 16); }

__device__ __forceinline__ void wait16(char* ws, unsigned off, unsigned tag, int lane) {
  gu32* f = (gu32*)(ws + off) + (lane & 15);
  for (unsigned sp = 0; sp < (1u << 15); ++sp) {
    unsigned v = __hip_atomic_load(f, RLX_AGENT);
    if (__all(v == tag)) return;
    __builtin_amdgcn_s_sleep(1);
  }
  if (lane == 0) __hip_atomic_store((gu32*)(ws + WS_TMO), 1u, RLX_AGENT);
}
__device__ __forceinline__ void wait32(char* ws, unsigned off, unsigned tag, int lane) {
  gu32* f = (gu32*)(ws + off) + (lane & 31);
  for (unsigned sp = 0; sp < (1u << 15); ++sp) {
    unsigned v = __hip_atomic_load(f, RLX_AGENT);
    if (__all(v == tag)) return;
    __builtin_amdgcn_s_sleep(1);
  }
  if (lane == 0) __hip_atomic_store((gu32*)(ws + WS_TMO), 1u, RLX_AGENT);
}
typedef LAS unsigned lu32;
__device__ __forceinline__ unsigned lds_add(LAS char* L, int word) { return __hip_atomic_fetch_add((lu32*)(L + L_SYNC) + word, 1u, __ATOMIC_RELAXED, __HIP_MEMORY_SCOPE_WORKGROUP); }
__device__ __forceinline__ void lds_spin(LAS char* L, int word, unsigned want) {
  for (unsigned sp = 0; sp < (1u << 20); ++sp) {
    if (__builtin_amdgcn_readfirstlane(__hip_atomic_load((lu32*)(L + L_SYNC) + word, __ATOMIC_RELAXED, __HIP_MEMORY_SCOPE_WORKGROUP)) >= want) break;
  }
  asm volatile("" ::: "memory");
}
__device__ __forceinline__ void group_sync(LAS char* L, int word, int l) {
  asm volatile("s_waitcnt lgkmcnt(0)" ::: "memory");
  if (l == 0) lds_add(L, word);
  lds_spin(L, word, 4);
}
__device__ __forceinline__ void touch(unsigned voff, const void* sbase) {
  asm volatile("s_mov_b32 m0, %2\n\ts_nop 0\n\tglobal_load_lds_dword %0, %1" ::"v"(voff), "s"(sbase), "s"(L_SYNC + 64) : "memory");
}
__device__ __forceinline__ void touch64(const void* addr) {
  asm volatile("s_mov_b32 m0, %1\n\ts_nop 0\n\tglobal_load_lds_dword %0, off" ::"v"(addr), "s"(L_SYNC + 64) : "memory");
}
__device__ __forceinline__ void set_flag(char* ws, unsigned off, unsigned tag) { __hip_atomic_store((gu32*)(ws + off), tag, RLX_AGENT); }
#define STAMPW(i, wv) do {} while (0)
#define STAMP(i) STAMPW(i, 0)

__device__ __forceinline__ void splitk_exchange(LAS char* L, f32x4 (&acc)[5][2], f32x4 (&own)[3], int th, int kq, int l) {
  const int wb = L_RED + (th * 40 + kq) * 1024 + l * 16;
#pragma unroll
  for (int s = 0; s < 2; ++s)
#pragma unroll
    for (int T = 0; T < 5; ++T) *(LAS f32x4*)(L + wb + (s * 5 + T) * 4096) = acc[T][s];
  LBAR();
  const int rb = L_RED + (th * 2 + (kq & 1)) * 5 * 4096 + l * 16, t0 = rb + (kq >> 1) * 8192;
  f32x4 v[3][4];
#pragma unroll
  for (int k = 0; k < 4; ++k) {
    v[0][k] = *(const LAS f32x4*)(L + t0 + k * 1024);
    v[1][k] = *(const LAS f32x4*)(L + t0 + 4096 + k * 1024);
    v[2][k] = *(const LAS f32x4*)(L + rb + 4 * 4096 + k * 1024);
  }
#pragma unroll
  for (int i = 0; i < 3; ++i) own[i] = (v[i][0] + v[i][1]) + (v[i][2] + v[i][3]);
}

#define P4XW 4
constexpr int P4_XW = P4XW;
constexpr int P4_PRE = 4;
#define P4EARLY 10
constexpr int P4_EARLY = P4EARLY;
constexpr int p4_g(int it, int n) { return it == 4 ? n : it == 6 ? 2 * n : 0; }
constexpr int p4_x(int it) { return it + P4_XW < 14 ? 2 : 0; }
constexpr int p4_xwait(int st, int n) {
  int y = 0;
  if (st < P4_XW) { y = 2 * (P4_XW - 1 - st); for (int j = 0; j < st; ++j) y += p4_x(j) + p4_g(j, n); }
  else { y = p4_g(st - P4_XW, n); for (int j = st - P4_XW + 1; j < st; ++j) y += p4_x(j) + p4_g(j, n); }
  return y;
}
constexpr int p4_wwait(int st, int n) {
  int y = st == 10 ? n : 0;
  for (int j = 7; j < st; ++j) y += p4_x(j);
  return y;
}
template <int TH, int stg> __device__ __forceinline__ void p4_issue(char* ws, int cs, int kq, int l) {
  constexpr int N = TH == 0 ? 3 : 2;
  const int kb = kq < 2 ? 14 * kq : 28 + 13 * (kq - 2);
  gldsn_sc1<N, (stg % 7) * 20480>(l * 16, ws + WS_W4 + (size_t)cs * 270 * 1024 + TH * 3072 + (size_t)min(kb + stg, 53) * 5120, kq * 5120 + TH * 3072);
}
template <int J> __device__ __forceinline__ void p4_pre_piece(const char* src, unsigned l16, int th, unsigned lbase) {
  constexpr int q0 = 2 * J, q1 = (2 * J + 1 < 35) ? 2 * J + 1 : 34;
  constexpr int f0 = (q0 / 5) * 20480 + (q0 % 5) * 1024, f1 = (q1 / 5) * 20480 + (q1 % 5) * 1024;
  asm volatile("s_mov_b32 m0, %2\n\ts_nop 0\n\tglobal_load_lds_dwordx4 %0, %1 sc1" ::"v"(l16), "s"(src + (th ? q1 : q0) * 1024), "s"(lbase + (th ? f1 : f0)) : "memory");
}
template <int TH>
__device__ __forceinline__ void p4_main(LAS char* L, char* ws, int r, int r2, int cs, int kq, int w, int l, unsigned tag, f32x4 (&acc)[5][2]) {
  constexpr int N = TH == 0 ? 3 : 2;
  const int kb = kq < 2 ? 14 * kq : 28 + 13 * (kq - 2);
  const int nk = kq < 2 ? 14 : 13;
  const unsigned l16 = l * 16;
  auto issue = [&](auto STG) { p4_issue<TH, STG>(ws, cs, kq, l); };
  int wbs[3];
  wbs[0] = kq * 5120 + l16; wbs[1] = wbs[0] + 3 * 20480; wbs[2] = wbs[0] + 6 * 20480;
  asm volatile("" : "+v"(wbs[0]), "+v"(wbs[1]), "+v"(wbs[2]));
  {
    const char* w4src = ws + WS_W4 + (size_t)cs * 270 * 1024 + (size_t)kb * 5120;
    static_for<P4_EARLY, 18>([&](auto J) { p4_pre_piece<J>(w4src, l16, TH, kq * 5120); });
    static_assert(P4_EARLY >= 10, "");
  }
  v4u wp[P4_PRE][5];
  static_for<0, P4_PRE>([&](auto ST) { static_for<0, 5>([&](auto I) { wp[ST][I] = lds128(L, wbs[ST / 3] + (ST % 3) * 20480 + I * 1024); }); });
  LBAR();
  static_for<7, 7 + P4_PRE>([&](auto ST) { issue(ST); });
  STAMP(38);
  if (w == 0) wait16(ws, WS_FA + r * 256, tag, l);
  STAMP(39);
  waitbar<P4_PRE * N>();
  STAMP(13);
  v4u xf[14][2];
  const char* xa = ws + WS_ACT + (size_t)(4 * r2 + 2 * TH) * 54 * 1024;
  auto ldx = [&](auto K, auto S) {
    constexpr int k = K, s = S;
    ldg_asm_sc1(xf[k][s], l16, xa + (size_t)(s * 54 + min(kb + k, 53)) * 1024);
  };
  static_for<0, P4_XW>([&](auto K) { ldx(K, IC<0>{}); ldx(K, IC<1>{}); });
  v4u wf[2][5];
  auto rd1 = [&](auto STG, auto I) {
    constexpr int stg = STG, T = I, sl7 = stg % 7;
    wf[stg & 1][T] = lds128(L, wbs[sl7 / 3] + (sl7 % 3) * 20480 + T * 1024);
  };
  static_for<0, 14>([&](auto ST) {
    constexpr int st = ST;
    if constexpr (st == 4 || st == 6) LBAR();
    if constexpr (st == 10 || st == 12) waitbar<p4_wwait(st, N)>();
    if constexpr (st == 1) STAMP(40);
    if constexpr (st == 4) STAMP(41);
    if constexpr (st == 7) STAMP(42);
    if constexpr (st == 10) STAMP(43);
    if constexpr (st == 13) STAMP(44);
    wait_for<p4_xwait(st, N)>(xf[st][0], xf[st][1]);
    SB();
    if (st < 13 || st < nk) {
      static_for<0, 10>([&](auto I) {
        constexpr int i = I;
        if constexpr (st < P4_PRE) mfma_a<(st == 0)>(acc[i >> 1][i & 1], wp[st][i >> 1], xf[st][i & 1]);
        else mfma_a<false>(acc[i >> 1][i & 1], wf[st & 1][i >> 1], xf[st][i & 1]);
        if constexpr (st + P4_XW < 14 && (i == 0 || i == 2)) ldx(IC<st + P4_XW>{}, IC<(i >> 1)>{});
        if constexpr (st == 4 && i == 4) issue(IC<11>{});
        if constexpr (st == 6 && (i == 4 || i == 7)) issue(IC<12 + (i == 7)>{});
        if constexpr (st >= P4_PRE - 1 && st < 13 && i >= 2 && i < 7) rd1(IC<st + 1>{}, IC<i - 2>{});
        SB();
      });
    }
  });
  MFMA_FENCE();
}

template <int NT> struct Frag3 { v4u x[2], g[NT], u[NT]; };

template <int NT, int TILE0, bool LOADER>
__device__ __forceinline__ void p3_body(LAS char* L, char* ws, __amdgpu_buffer_rsrc_t rs, int r, int p, int cs, int w, int l, unsigned tag) {
  const int c = l & 15, g = l >> 4, tg = w & 3;
  const unsigned l16 = l * 16;
  constexpr int NWP = LOADER ? 3 : 4, NP = LOADER ? 7 : 4;
  const int wp0 = LOADER ? 16 + 3 * tg : 4 * tg;
  const char* wsrc = ws + WS_W3 + (size_t)p * 280 * 1024 + wp0 * 1024;
  const char* hx = ws + WS_H + (size_t)(8 * r + 2 * tg) * 20 * 1024;
  const unsigned lw = wp0 * 1024, lx = L_X3 + tg * 4096;
  auto piece = [&](auto STG, auto Q) {
    constexpr int stg = STG, q = Q, slot = stg % 3;
    if constexpr (q < NWP) gldso_sc1<slot * 28672, q * 1024>(l16, wsrc + stg * 28672, lw);
    else gldso_sc1<slot * 16384 + ((q - NWP) >> 1) * 2048, ((q - NWP) & 1) * 1024>(l16, hx + stg * 2048 + ((q - NWP) >> 1) * 20480, lx);
  };
  static_for<0, 3>([&](auto ST) { static_for<0, NWP>([&](auto Q) { piece(ST, Q); }); });
  if constexpr (!LOADER) {
    const int ct = min(w * 64 + l, 223), cm = ct >= 112;
    unsigned csv[20];
#pragma unroll
    for (int i = 0; i < 20; ++i) csv[i] = ldg1_sc1(rs, WS_CS + (p * 40 + 2 * i + cm) * 448 + (ct - 112 * cm) * 4);
    float s = 0.f;
#pragma unroll
    for (int i = 0; i < 20; ++i) s += __uint_as_float(csv[i]);
    *(LAS float*)(L + (w * 64 + l < 224 ? L_CS3 + ct * 4 : L_SYNC + 512)) = s;
  }
  STAMP(32);
  if (w == 7) { wait16(ws, WS_FH + r * 256, tag, l); STAMPW(33, 7); }
  DRAIN();
  LBAR();
  STAMP(10);
  if constexpr (LOADER) static_for<0, 3>([&](auto ST) { static_for<NWP, NP>([&](auto Q) { piece(ST, Q); }); });
  v2u stp[2][4];
#pragma unroll
  for (int s = 0; s < 2; ++s)
#pragma unroll
    for (int i = 0; i < 4; ++i) ldg2_asm_sc1(stp[s][i], (4 * g + i) * 128 + c * 8, ws + WS_HS + (8 * r + 2 * tg + s) * 16 * 128);

  f32x4 ag[NT][2], au[NT][2];
  float rstd[2], mr[2];
  const int xo = L_X3 + tg * 4096 + l16;
  int wb0 = TILE0 * 1024 + l16, wb2 = wb0 + 2 * 28672;
  asm volatile("" : "+v"(wb0), "+v"(wb2));
  auto rd1 = [&](Frag3<NT>& f, auto SLOT, auto KS, auto I) {
    constexpr int slot = SLOT, ks = KS, i = I;
    if constexpr (i < 2) f.x[i] = lds128(L, xo + slot * 16384 + ks * 1024 + i * 2048);
    else {
      constexpr int j = i - 2, t = j >> 1, u = j & 1, off = ks * 14336 + u * 7168 + t * 1024;
      const v4u v = slot < 2 ? lds128(L, wb0 + slot * 28672 + off) : lds128(L, wb2 + off);
      if constexpr (u == 0) f.g[t] = v; else f.u[t] = v;
    }
  };
  auto mma1 = [&](const Frag3<NT>& f, auto I, auto FIRST) {
    constexpr int i = I, t = i >> 2, u = (i >> 1) & 1, s = i & 1;
    if constexpr (u == 0) mfma_a<FIRST>(ag[t][s], f.g[t], f.x[s]); else mfma_a<FIRST>(au[t][s], f.u[t], f.x[s]);
  };
  constexpr int NM = 4 * NT, NR = 2 + 2 * NT;
  Frag3<NT> A, B;
  if constexpr (LOADER) waitbar<8 + 8>(); else LBAR();
  STAMP(35);
  static_for<0, NR>([&](auto I) { rd1(A, IC<0>{}, IC<0>{}, I); });
  static_for<0, 10>([&](auto ST) {
    constexpr int st = ST, slot = st % 3, nslot = (st + 1) % 3;
    static_for<0, NM>([&](auto I) {
      constexpr int i = I;
      mma1(A, I, IC<(st == 0)>{});
      if constexpr (i < NR) rd1(B, IC<slot>{}, IC<1>{}, I);
      SB();
    });
    if constexpr (st < 9) {
      if constexpr (st == 0) { if constexpr (LOADER) waitbar<4 + 8>(); else LBAR(); }
      else if constexpr (st == 1) { if constexpr (LOADER) waitbar<NP>(); else LBAR(); }
      else if constexpr (st < 8) waitbar<NP>(); else waitbar<0>();
    }
    if constexpr (st == 4) {
      asm volatile("" : "+v"(stp[0][0]), "+v"(stp[0][1]), "+v"(stp[0][2]), "+v"(stp[0][3]), "+v"(stp[1][0]), "+v"(stp[1][1]), "+v"(stp[1][2]), "+v"(stp[1][3]));
#pragma unroll
      for (int s = 0; s < 2; ++s) {
        float sm = 0.f, sq = 0.f;
#pragma unroll
        for (int i = 0; i < 4; ++i) { sm += __uint_as_float(stp[s][i].x); sq += __uint_as_float(stp[s][i].y); }
        sm = rowsum4(sm); sq = rowsum4(sq);
        const float mean = sm * (1.0f / 640.0f);
        rstd[s] = __builtin_amdgcn_rsqf(fmaxf(sq * (1.0f / 640.0f) - mean * mean, 0.f) + 1e-5f);
        mr[s] = -mean * rstd[s];
      }
      asm volatile("" : "+v"(rstd[0]), "+v"(rstd[1]), "+v"(mr[0]), "+v"(mr[1]));
    }
    if constexpr (st == 2) STAMP(45);
    if constexpr (st == 5) STAMP(46);
    if constexpr (st == 8) STAMP(47);
    static_for<0, NM>([&](auto I) {
      constexpr int i = I;
      mma1(B, I, IC<0>{});
      if constexpr (LOADER) { if constexpr (st <= 6 && i < NP) piece(IC<st + 3>{}, I); }
      else { if constexpr (st <= 6 && (i % 3) == 0 && i / 3 < NP) piece(IC<st + 3>{}, IC<i / 3>{}); }
      if constexpr (st < 9 && i < NR) rd1(A, IC<nslot>{}, IC<0>{}, I);
      SB();
    });
  });
  MFMA_FENCE();
  STAMP(11);
  LBAR();
  const int kq4 = w >> 1, th4 = w & 1;
  const char* w4src = ws + WS_W4 + (size_t)(cs & 7) * 270 * 1024 + (size_t)(kq4 < 2 ? 14 * kq4 : 28 + 13 * (kq4 - 2)) * 5120;
  const unsigned w4l = kq4 * 5120;
  f32x4 cgv[NT], cuv[NT];
#pragma unroll
  for (int t = 0; t < NT; ++t) {
    cgv[t] = *(const LAS f32x4*)(L + L_CS3 + ((TILE0 + t) * 16 + 4 * g) * 4);
    cuv[t] = *(const LAS f32x4*)(L + L_CS3 + (112 + (TILE0 + t) * 16 + 4 * g) * 4);
  }
  static_for<0, 2>([&](auto S) {
    constexpr int s = S;
    unsigned y[NT][2];
    const unsigned ab = WS_ACT + (8 * r + 2 * tg + s) * 54 * 1024 + l16;
    static_for<0, NT>([&](auto T) {
      constexpr int t = T, step = s * NT + t, j0 = step < NT ? step * P4_EARLY / NT : P4_EARLY, j1 = step < NT ? (step + 1) * P4_EARLY / NT : P4_EARLY;
      y[t][0] = geglu2(lo2(ag[t][s]), lo2(au[t][s]), lo2(cgv[t]), lo2(cuv[t]), rstd[s], mr[s]);
      static_for<j0, (j0 + j1 + 1) / 2>([&](auto J) { p4_pre_piece<J>(w4src, l16, th4, w4l); });
      y[t][1] = geglu2(hi2(ag[t][s]), hi2(au[t][s]), hi2(cgv[t]), hi2(cuv[t]), rstd[s], mr[s]);
      static_for<(j0 + j1 + 1) / 2, j1>([&](auto J) { p4_pre_piece<J>(w4src, l16, th4, w4l); });
      if constexpr (t & 1) {
        if constexpr (TILE0 == 0) stg_sc1(rs, p < 15 ? ab + (3 * p + (t >> 1)) * 1024 : 0x7FFFFFF0u, v4u{y[t - 1][0], y[t - 1][1], y[t][0], y[t][1]});
        else stg_sc1(rs, ab + (p < 15 ? 3 * p + 2 : 45) * 1024, v4u{y[t - 1][0], y[t - 1][1], y[t][0], y[t][1]});
      }
    });
    if constexpr (TILE0 != 0) stg2_sc1(rs, ab + (46 + (p >> 1)) * 1024 + (p & 1) * 8, v2u{y[NT - 1][0], y[NT - 1][1]});
  });
}

struct FragKV { v4u x[2], w[5]; };

__global__ __launch_bounds__(512, 2) void fused_block(const u16* __restrict__ x, const u16* __restrict__ wqkv, const u16* __restrict__ wo,
                                                      const u16* __restrict__ wg, const u16* __restrict__ wu, const u16* __restrict__ wd,
                                                      u16* __restrict__ out, char* __restrict__ ws) {
  extern __shared__ __attribute__((aligned(16))) char smem_[];
  LAS char* L = (LAS char*)smem_;
  const int tid = threadIdx.x, l = tid & 63, c = l & 15, g = l >> 4;
  const int w = __builtin_amdgcn_readfirstlane(tid >> 6);
  const int b = blockIdx.x, xcd = b & 7, jj = b >> 3, rg = xcd >> 1, cgp = xcd & 1;
  const int r = 4 * rg + (jj >> 3), p = 8 * cgp + (jj & 7);
  const int r2 = 4 * xcd + (jj >> 3), cs = jj & 7;
  const int r3 = 8 * (xcd >> 2) + (jj >> 2), c3 = 4 * (xcd & 3) + (jj & 3);
  const __amdgpu_buffer_rsrc_t rs = __builtin_amdgcn_make_buffer_rsrc(ws, 0, WS_END, 0x00020000);
  const v4u ONES = {0x3F803F80u, 0x3F803F80u, 0x3F803F80u, 0x3F803F80u};
  const int q4 = (l & 15) >> 2, pp = l & 3;
  STAMP(0);

  unsigned tag;
  constexpr int S1 = 26624;
  const int xo0 = c * 128 + ((g ^ (c & 7)) << 4);
  const int wrow = 16 * (g >> 1) + 4 * (g & 1) + q4;
  const int kvo = 16384 + wrow * 160 + 8 * pp;
  auto kv_product = [&](int w) {
    const int Ta = w < 2 ? 0 : w < 5 ? 1 : 2;
    const int Tb = (w == 0 || w == 2 || w == 7) ? 2 : (w == 4 || w == 6) ? 4 : 3;
    v4u A[4], B[4];
#pragma unroll
    for (int tg = 0; tg < 4; ++tg) {
      A[tg] = lds128(L, L_KVF + (tg * 5 + Ta) * 1024 + l * 16);
      B[tg] = lds128(L, L_KVF + (tg * 5 + Tb) * 1024 + l * 16);
    }
    f32x4 d = {0, 0, 0, 0};
    const bool one = Tb == 2 && c < 8;
#pragma unroll
    for (int tg = 0; tg < 4; ++tg) d = mfma(A[tg], one ? ONES : B[tg], d);
    const int kc = 16 * Ta + 4 * g;
    const int vc = 16 * Tb + c - 40;
    const int hk = kc >= 20;
    const bool iskv = kc < 40 && vc >= 0 && hk == (vc >= 20), isz = kc < 40 && vc < 0 && c == 0;
    *(LAS f32x4*)(L + (iskv ? L_KVC + (vc * 20 + kc - 20 * hk) * 4 : isz ? L_KVC + (800 + kc) * 4 : L_SYNC + 512 + (l & 31) * 16)) = d;
  };

  if (w < 4) {
    {
      const char* pc;
      asm volatile("s_getpc_b64 %0" : "=s"(pc));
      const int n = (jj * 4 + w) * 4 + (l & 3);
      if (l < 4) touch64(pc + min(n * 128, T0KB * 1024 - 128));
    }
    if (w == 3) {
      const int n = jj * 16 + (l & 15);
      touch64(l < 16 ? (const char*)ws + min(n * 65536, (int)WS_END - 4) : (const char*)out + min((jj * 2 + (l & 1)) * 65536, 2048 * 640 * 2 - 4));
    }
    auto hint1 = [&](int stg, int n) {
      const int idx = jj * 36 + min(n, 35);
      const int wr_ = idx / 10, pc = idx - wr_ * 10, sec = pc / 5;
      touch64(idx < 640 ? (const char*)wqkv + (64 * stg + wr_) * 3840 + (1 + sec) * 1280 + cgp * 640 + (pc - sec * 5) * 128
                        : (const char*)x + ((size_t)rg * 512 + (idx - 640)) * 1280 + stg * 128);
    };
    {
      __builtin_amdgcn_s_sleep(T1SLEEP);
      const int sl_ = l / 9;
      if (l < 63) hint1(2 + sl_, 9 * w + (l - 9 * sl_));
    }
    if (l == 0) asm volatile("s_mov_b32 m0, %1\n\ts_nop 0\n\tglobal_load_lds_dword %0, off sc1" ::"v"(ws + WS_EPOCH), "s"(L_SYNC + 32) : "memory");
    *(LAS v4u*)(L + L_BD + tid * 16) = v4u{0, 0, 0, 0};
    *(LAS v4u*)(L + L_BD + 4096 + tid * 16) = v4u{0, 0, 0, 0};
    if (tid < 2) *(LAS v4u*)(L + L_SYNC + tid * 16) = v4u{0, 0, 0, 0};
    if (tid == 2) *(LAS v4u*)(L + L_SYNC + 48) = v4u{0, 0, 0, 0};
    auto hint2 = [&](int i) {
      const int n = 96 * w + 16 * i + (l & 15);
      const int li = b * 228 + min(n, 227);
      const char* aw_ = li < 6400 ? (const char*)wo + li * 128 : li < 23680 ? (const char*)wg + (li - 6400) * 128
                      : li < 40960 ? (const char*)wu + (li - 23680) * 128 : (const char*)wd + min(li - 40960, 17279) * 128;
      const int m = n - 228;
      const unsigned o5 = m < 32 ? WS_O + ((8 * r + (m >> 2)) * 20 + 16 + (p >> 2)) * 1024 + ((p & 3) >> 1) * 512 + (m & 3) * 128
                        : m < 64 ? WS_H + ((4 * r2 + ((m - 32) >> 3)) * 20 + 16 + (cs >> 1)) * 1024 + (m & 7) * 128
                                 : WS_ACT + ((8 * r3 + (min(m - 64, 63) >> 3)) * 54 + 46 + (c3 >> 1)) * 1024 + (m & 7) * 128;
      const char* a = n < 228 ? aw_ : ws + o5;
      if (l < 16) touch64(a);
    };
    STAMP(1);
    f32x4 acc[2][5];
    {
      const int xw = w * 32 * 128;
      int xbs[3][2], kbs[3];
#pragma unroll
      for (int q = 0; q < 3; ++q) { xbs[q][0] = xw + xo0 + 2 * q * S1; xbs[q][1] = xw + (xo0 ^ 64) + 2 * q * S1; kbs[q] = kvo + 2 * q * S1; }
      asm volatile("" : "+v"(xbs[0][0]), "+v"(xbs[0][1]), "+v"(xbs[1][0]), "+v"(xbs[1][1]), "+v"(xbs[2][0]), "+v"(xbs[2][1]), "+v"(kbs[0]), "+v"(kbs[1]), "+v"(kbs[2]));
      auto rd1 = [&](FragKV& f, auto SLOT, auto KS, auto I) {
        constexpr int slot = SLOT, ks = KS, i = I;
        if constexpr (i < 2) f.x[i] = lds128(L, xbs[slot >> 1][ks] + (slot & 1) * S1 + i * 2048);
        else {
          constexpr int j = i - 2, t = j >> 1, h = j & 1;
          const v2u v = __builtin_bit_cast(v2u, __builtin_amdgcn_ds_read_tr16_b64_v4i16((LAS s16x4*)(L + kbs[slot >> 1] + (slot & 1) * S1 + ks * 32 * 160 + t * 32 + h * 8 * 160)));
          if constexpr (h == 0) { f.w[t].x = v.x; f.w[t].y = v.y; } else { f.w[t].z = v.x; f.w[t].w = v.y; }
        }
      };
      auto mma1 = [&](const FragKV& f, auto I, auto FIRST) { constexpr int i = I, t = i >> 1, s = i & 1; mfma_a<FIRST>(acc[s][t], f.x[s], f.w[t]); };
      FragKV A, B;
      LBAR();
      static_for<0, 12>([&](auto I) { rd1(A, IC<0>{}, IC<0>{}, I); });
      static_for<0, 10>([&](auto ST) {
        constexpr int st = ST;
        static_for<0, 10>([&](auto I) {
          constexpr int i = I;
          mma1(A, I, IC<(st == 0)>{});
          if constexpr (i < 6) { rd1(B, IC<(st % 5)>{}, IC<1>{}, IC<2 * i>{}); rd1(B, IC<(st % 5)>{}, IC<1>{}, IC<2 * i + 1>{}); }
          SB();
        });
        if constexpr (st < 9) LBAR();
        if constexpr (st == 0) STAMP(48);
        if constexpr (st == 3) STAMP(49);
        if constexpr (st == 6) STAMP(50);
        static_for<0, 10>([&](auto I) {
          constexpr int i = I;
          mma1(B, I, IC<0>{});
          if constexpr (st >= 2 && st < 8 && i == 8) hint2(st - 2);
          if constexpr (st < 9 && i < 6) { rd1(A, IC<((st + 1) % 5)>{}, IC<0>{}, IC<2 * i>{}); rd1(A, IC<((st + 1) % 5)>{}, IC<0>{}, IC<2 * i + 1>{}); }
          SB();
        });
      });
      MFMA_FENCE();
    }
    STAMP(2);
    __builtin_amdgcn_s_setprio(2);
    waitvm<(T2MODE == 1 ? 6 : 0)>();
    LBAR();
    STAMP(16);
    tag = __builtin_amdgcn_readfirstlane(*(const LAS unsigned*)(L + L_SYNC + 32)) + 1u;
    {
      f32x4 rstd[2], mr[2];
#pragma unroll
      for (int s = 0; s < 2; ++s) {
        rstd[s] = *(const LAS f32x4*)(L + L_STAT + (32 * w + 16 * s + 4 * g) * 4);
        mr[s] = *(const LAS f32x4*)(L + L_STAT + 512 + (32 * w + 16 * s + 4 * g) * 4);
      }
      float cw[5];
#pragma unroll
      for (int t = 0; t < 5; ++t) cw[t] = *(const LAS float*)(L + L_CSUM + (16 * t + c) * 4);
#pragma unroll
      for (int t = 0; t < 5; ++t) {
        unsigned o[4];
#pragma unroll
        for (int s = 0; s < 2; ++s)
#pragma unroll
          for (int h = 0; h < 2; ++h) {
            const f32x2 a2 = h ? hi2(acc[s][t]) : lo2(acc[s][t]), r2_ = h ? hi2(rstd[s]) : lo2(rstd[s]), m2 = h ? hi2(mr[s]) : lo2(mr[s]);
            f32x2 y;
            if (t < 2) y = phi2(pfma(a2, r2_, pfma(m2, sp2(cw[t]), sp2(1e-6f))));
            else if (t == 2) {
              const f32x2 te = pfma(a2, r2_, pfma(m2, sp2(cw[t]), sp2(1e-6f))), ph = phi2(te), v = te - sp2(1e-6f);
              y = c < 8 ? ph : v;
            } else y = pfma(a2, r2_, m2 * sp2(cw[t]));
            o[2 * s + h] = pk2v(y);
          }
        *(LAS v4u*)(L + L_KVF + (w * 5 + t) * 1024 + l * 16) = v4u{o[0], o[1], o[2], o[3]};
      }
    }
    STAMP(17);
    group_sync(L, 5, l);
    kv_product(w);
    kv_product(w + 4);
    group_sync(L, 6, l);
    STAMP(19);
    if (w == 0) {
      v4u v[4];
#pragma unroll
      for (int i = 0; i < 4; ++i) v[i] = lds128(L, L_KVC + min(i * 64 + l, 209) * 16);
#pragma unroll
      for (int i = 0; i < 4; ++i) stg_sc1(rs, i * 64 + l < 210 ? WS_KVP + (p * 16 + r) * KVP_STRIDE + (i * 64 + l) * 16 : 0x7FFFFFF0u, v[i]);
      STAMP(20);
      DRAIN();
      if (l == 0) set_flag(ws, WS_FKV + p * 256 + r * 4, tag);
      STAMP(3);
    }
    __builtin_amdgcn_s_setprio(0);
    const int f0 = min(7 * c3, 101);
    const int u3 = min(r3 + 16 * max(w - 1, 0), 39), ks3 = u3 >> 1, mat = u3 & 1;
    const int ks4 = min(r2 + 32 * max(w - 1, 0), 53);
    const unsigned area = L_KVF + max(w - 1, 0) * 12288;
    if (w > 0) {
      const char* w3 = (const char*)(mat ? wu : wg);
#pragma unroll
      for (int i = 0; i < 7; ++i) {
        const int n = 64 * i + l, pos = n / 14, ch = n - pos * 14;
        const int feat = ks3 < 16 ? 80 * (ks3 >> 1) + 32 * (ks3 & 1) + pos : 160 * (ks3 - 16) + 64 + (pos & 15) + 80 * (pos >> 4);
        glds(feat * 3456 + (f0 * 16 + 8 * ch) * 2, w3, area + i * 1024);
      }
      if (w < 3) {
#pragma unroll
        for (int i = 0; i < 5; ++i) {
          const int n = 64 * i + l, pos = n / 10, ch = n - pos * 10, t = pos >> 4;
          const int sp = ks4 < 45 ? ks4 / 3 : ks4 == 45 ? 15 : 2 * (ks4 - 46) + t;
          const int lt = ks4 < 45 ? 2 * (ks4 % 3) + t : ks4 == 45 ? 4 + t : 6;
          const int row = 16 * (min(7 * sp, 101) + lt) + (pos & 15);
          glds(row * 1280 + (80 * cs + 8 * ch) * 2, wd, area + 7168 + i * 1024);
        }
      }
    }
    if (w > 0) {
      if (w < 3) waitvm<5>(); else waitvm<0>();
      {
        const int tb = area + (4 * g + q4) * 224 + 8 * pp;
        v4u fr[7];
#pragma unroll
        for (int t = 0; t < 7; ++t) fr[t] = ldtr(L, tb + t * 32, 16 * 224);
        const bool ok = r3 + 16 * (w - 1) < 40;
        const unsigned ob = ok ? WS_W3 + ((c3 * 20 + ks3) * 14 + mat * 7) * 1024 + l * 16 : 0x7FFF0000u;
        float csel0 = 0.f, csel1 = 0.f;
#pragma unroll
        for (int t = 0; t < 7; ++t) {
          stg_sc1(rs, ob + t * 1024, fr[t]);
          const f32x4 cs_ = mfma(ONES, fr[t], f32x4{0, 0, 0, 0});
          if (t < 4) csel0 = (g == t) ? cs_[0] : csel0; else csel1 = (g == t - 4) ? cs_[0] : csel1;
        }
        const unsigned cb = ok ? WS_CS + (c3 * 40 + u3) * 448 + l * 4 : 0x7FFF0000u;
        stg1_sc1(rs, cb, __float_as_uint(csel0));
        stg1_sc1(rs, l < 48 ? cb + 256 : 0x7FFF0000u, __float_as_uint(csel1));
      }
      if (w < 3) {
        waitvm<9>();
        const int tb = area + 7168 + (4 * g + q4) * 160 + 8 * pp;
        v4u fr[5];
#pragma unroll
        for (int t = 0; t < 5; ++t) fr[t] = ldtr(L, tb + t * 32, 16 * 160);
        const unsigned ob = (r2 + 32 * (w - 1) < 54) ? WS_W4 + (cs * 54 + ks4) * 5 * 1024 + l * 16 : 0x7FFF0000u;
#pragma unroll
        for (int t = 0; t < 5; ++t) stg_sc1(rs, ob + t * 1024, fr[t]);
      }
    }
    if (w == 0) wait32(ws, WS_FW + cs * 256, tag, l);
    group_sync(L, 12, l);
    lds_spin(L, 4, 4);
    {
      const char* w2 = ws + WS_W2 + (size_t)cs * 100 * 1024 + w * 25 * 1024;
      static_for<0, 6>([&](auto I) { constexpr int i = I; gldsn_sc1<4, i * 4096>(l * 16, w2 + i * 4096, w * 25 * 1024); });
      gldsn_sc1<1, 24 * 1024>(l * 16, w2 + 24 * 1024, w * 25 * 1024);
    }
    DRAIN();
    if (l == 0 && lds_add(L, 3) == 3) { set_flag(ws, WS_FC + c3 * 256 + r3 * 4, tag); set_flag(ws, WS_FD + cs * 256 + r2 * 4, tag); }
    STAMPW(23, 1);
  } else {
    const int tw = w - 4;
    unsigned vx[4], vw[3];
#pragma unroll
    for (int i = 0; i < 4; ++i) { const int tok = 8 * (4 * tw + i) + (l >> 3); vx[i] = tok * 1280 + (((l & 7) ^ (tok & 7)) << 4); }
    const char* xb = (const char*)x + (size_t)r * 128 * 1280;
    const unsigned lxw = 4 * tw * 1024, lkw = 16384 + 160 * tw * 16;
    auto pieceX = [&](auto ST, auto I) { constexpr int st = ST, i = I; gldsi<(st % 5) * S1 + i * 1024>(vx[i], xb + st * 128, lxw); };
    static_for<0, 4>([&](auto I) { pieceX(IC<0>{}, I); });
    constexpr unsigned AREA_O = 133120;
    if (w == 7) {
      if (l == 0) asm volatile("s_mov_b32 m0, %1\n\ts_nop 0\n\tglobal_load_lds_dword %0, off sc1" ::"v"(ws + WS_EPOCH), "s"(L_SYNC + 36) : "memory");
      const int ks2 = min(r2, 19);
#pragma unroll
      for (int i = 0; i < 5; ++i) {
        const int n = 64 * i + l, pos = n / 10, ch = n - pos * 10, t = pos >> 4, gg = (pos >> 2) & 3;
        const int row = ks2 < 16 ? 40 * ks2 + pos : 40 * (4 * (ks2 - 16) + 2 * (gg >> 1) + t) + 32 + 4 * (gg & 1) + (pos & 3);
        glds(row * 1280 + (80 * cs + 8 * ch) * 2, wo, AREA_O + i * 1024);
      }
    }
#pragma unroll
    for (int j = 0; j < 3; ++j) {
      const int n = min(160 * tw + 64 * j + l, 639), pos = n / 10, ch = n - pos * 10;
      const int col = (ch < 5) ? 640 + 40 * p + 8 * ch : 1280 + 40 * p + 8 * (ch - 5);
      const int k = (pos & ~12) | ((pos & 4) << 1) | ((pos & 8) >> 1);
      vw[j] = k * 3840 + col * 2;
    }
    auto pieceW = [&](auto ST, auto J) {
      constexpr int st = ST, j = J;
      if constexpr (j < 2) gldsi<(st % 5) * S1 + j * 1024>(vw[j], (const char*)wqkv + (size_t)st * 64 * 3840, lkw);
      else { if (l < 32) gldsi<(st % 5) * S1 + j * 1024>(vw[j], (const char*)wqkv + (size_t)st * 64 * 3840, lkw); }
    };
    auto piece = [&](auto ST, auto Q) { constexpr int q = Q; if constexpr (q < 4) pieceX(ST, Q); else pieceW(ST, IC<q - 4>{}); };
    static_for<0, 3>([&](auto J) { pieceW(IC<0>{}, J); });
    static_for<0, 7>([&](auto Q) { piece(IC<1>{}, Q); });
    auto pieceQ = [&](auto QS, auto J) {
      constexpr int qs = QS, j = J, npc = qs < 2 ? 6 : 3;
      const int n = 64 * (tw * npc + j) + l, pos = n / 6, ch = n - pos * 6;
      const int k = 256 * qs + ((pos & ~12) | ((pos & 4) << 1) | ((pos & 8) >> 1));
      if (ch < 5) gldsi<qs * S1 + j * 1024>(k * 3840 + (40 * p + 8 * ch) * 2, wqkv, tw * npc * 1024);
    };
    STAMPW(51, 4);
    v4u xs[20][2];
    f32x4 sq[2], sm[2], cs0, cs1;
    v4u ones_ = ONES;
    asm volatile("" : "+v"(ones_));
    {
      const int xw = tw * 32 * 128;
      int xbs[3][2], ebs[3][2];
#pragma unroll
      for (int q = 0; q < 3; ++q) { xbs[q][0] = xw + xo0 + 2 * q * S1; xbs[q][1] = xw + (xo0 ^ 64) + 2 * q * S1; ebs[q][0] = kvo + tw * 32 + 2 * q * S1; ebs[q][1] = kvo + 4 * 32 + 2 * q * S1; }
      asm volatile("" : "+v"(xbs[0][0]), "+v"(xbs[0][1]), "+v"(xbs[1][0]), "+v"(xbs[1][1]), "+v"(xbs[2][0]), "+v"(xbs[2][1]),
                        "+v"(ebs[0][0]), "+v"(ebs[0][1]), "+v"(ebs[1][0]), "+v"(ebs[1][1]), "+v"(ebs[2][0]), "+v"(ebs[2][1]));
      v4u e[2][2];
      auto rd1 = [&](auto KK, auto I) {
        constexpr int kk = KK, i = I, slot = (kk >> 1) % 5, ks = kk & 1;
        if constexpr (i < 2) xs[kk][i] = lds128(L, xbs[slot >> 1][ks] + (slot & 1) * S1 + i * 2048);
        else {
          constexpr int t = (i - 2) >> 1, h = i & 1;
          const v2u v = __builtin_bit_cast(v2u, __builtin_amdgcn_ds_read_tr16_b64_v4i16((LAS s16x4*)(L + ebs[slot >> 1][t] + (slot & 1) * S1 + ks * 32 * 160 + h * 8 * 160)));
          if constexpr (h == 0) { e[kk & 1][t].x = v.x; e[kk & 1][t].y = v.y; } else { e[kk & 1][t].z = v.x; e[kk & 1][t].w = v.y; }
        }
      };
      auto mma1 = [&](auto KK, auto I) {
        constexpr int kk = KK, i = I;
        if constexpr (i < 2) mfma_a<(kk == 0)>(sq[i], xs[kk][i], xs[kk][i]);
        else if constexpr (i < 4) mfma_a<(kk == 0)>(sm[i - 2], ones_, xs[kk][i - 2]);
        else if constexpr (i == 4) mfma_a<(kk == 0)>(cs0, ones_, e[kk & 1][0]);
        else mfma_a<(kk == 0)>(cs1, ones_, e[kk & 1][1]);
      };
      waitbar<7>();
      if (w == 7) {
        v4u fr[5];
#pragma unroll
        for (int t = 0; t < 5; ++t) fr[t] = ldtr(L, AREA_O + (4 * g + q4) * 160 + 8 * pp + t * 32, 16 * 160);
        const unsigned ob = r2 < 20 ? WS_W2 + (cs * 20 + min(r2, 19)) * 5 * 1024 + l * 16 : 0x7FFF0000u;
#pragma unroll
        for (int t = 0; t < 5; ++t) stg_sc1(rs, ob + t * 1024, fr[t]);
      }
      static_for<0, 6>([&](auto I) { rd1(IC<0>{}, I); });
      static_for<0, 10>([&](auto ST) {
        constexpr int st = ST;
        static_for<0, 6>([&](auto I) {
          constexpr int i = I;
          mma1(IC<2 * st>{}, I);
          rd1(IC<2 * st + 1>{}, I);
          if constexpr (st == 0) { piece(IC<2>{}, I); if constexpr (i == 5) piece(IC<2>{}, IC<6>{}); }
          if constexpr (st == 1) { piece(IC<4>{}, I); if constexpr (i == 5) piece(IC<4>{}, IC<6>{}); }
          SB();
        });
        if constexpr (st < 9) { if constexpr (st == 0) waitbar<7>(); else if constexpr (st <= 6) waitbar<14>(); else if constexpr (st == 7) waitbar<13>(); else waitbar<12>(); }
        if constexpr (st == 6) {
          if (w == 7 && l == 0) set_flag(ws, WS_FW + cs * 256 + r2 * 4, *(const LAS unsigned*)(L + L_SYNC + 36) + 1u);
        }
        static_for<0, 6>([&](auto I) {
          constexpr int i = I;
          mma1(IC<2 * st + 1>{}, I);
          if constexpr (st == 0) { piece(IC<3>{}, I); if constexpr (i == 5) piece(IC<3>{}, IC<6>{}); }
          if constexpr (st >= 1 && st <= 5) { piece(IC<st + 4>{}, I); if constexpr (i == 5) piece(IC<st + 4>{}, IC<6>{}); }
          if constexpr (st == 6 || st == 7) pieceQ(IC<st - 6>{}, I);
          if constexpr (st == 8 && i < 3) pieceQ(IC<2>{}, I);
          if constexpr (st < 9) rd1(IC<2 * st + 2>{}, I);
          SB();
        });
      });
      MFMA_FENCE();
    }
#pragma unroll
    for (int s = 0; s < 2; ++s) {
      const int e_ = c & 3;
      const float v = e_ == 0 ? sq[s][0] : e_ == 1 ? sq[s][1] : e_ == 2 ? sq[s][2] : sq[s][3];
      const float m = sm[s][0] * (1.0f / 640.0f);
      const float rs_ = __builtin_amdgcn_rsqf(fmaxf(v * (1.0f / 640.0f) - m * m + 1e-5f, 1e-5f));
      const bool own = g == (c >> 2);
      *(LAS float*)(L + (own ? L_STAT + (32 * tw + 16 * s + c) * 4 : L_SYNC + 512 + l * 4)) = rs_;
      *(LAS float*)(L + (own ? L_STAT + 512 + (32 * tw + 16 * s + c) * 4 : L_SYNC + 768 + l * 4)) = -m * rs_;
    }
    *(LAS float*)(L + (g == 0 ? L_CSUM + (16 * tw + c) * 4 : L_SYNC + 512 + l * 4)) = cs0[0];
    *(LAS float*)(L + (g == 0 ? L_CSUM + (64 + c) * 4 : L_SYNC + 512 + l * 4)) = cs1[0];
    LBAR();
    tag = __builtin_amdgcn_readfirstlane(*(const LAS unsigned*)(L + L_SYNC + 32)) + 1u;
    DRAIN();
    group_sync(L, 7, l);
    lds_spin(L, 5, 4);
    f32x4 qacc[3][2], csq;
    {
      int qbs[3];
#pragma unroll
      for (int q = 0; q < 3; ++q) qbs[q] = q * S1 + wrow * 96 + 8 * pp;
      asm volatile("" : "+v"(qbs[0]), "+v"(qbs[1]), "+v"(qbs[2]));
      int cbs[3];
#pragma unroll
      for (int q = 0; q < 3; ++q) cbs[q] = qbs[q] + (tw < 3 ? tw : 2) * 32;
      asm volatile("" : "+v"(cbs[0]), "+v"(cbs[1]), "+v"(cbs[2]));
      v4u f[2][4];
      auto rd1 = [&](auto KK, auto I) {
        constexpr int kk = KK, i = I, t = i >> 1, h = i & 1;
        const int base = t < 3 ? qbs[kk >> 3] + t * 32 : cbs[kk >> 3];
        const v2u v = __builtin_bit_cast(v2u, __builtin_amdgcn_ds_read_tr16_b64_v4i16((LAS s16x4*)(L + base + (kk & 7) * 32 * 96 + h * 8 * 96)));
        if constexpr (h == 0) { f[kk & 1][t].x = v.x; f[kk & 1][t].y = v.y; } else { f[kk & 1][t].z = v.x; f[kk & 1][t].w = v.y; }
      };
      static_for<0, 8>([&](auto I) { rd1(IC<0>{}, I); });
      static_for<0, 20>([&](auto KK) {
        constexpr int kk = KK;
        static_for<0, 7>([&](auto I) {
          constexpr int i = I;
          if constexpr (i < 6) mfma_a<(kk == 0)>(qacc[i >> 1][i & 1], f[kk & 1][i >> 1], xs[kk][i & 1]);
          else mfma_a<(kk == 0)>(csq, ones_, f[kk & 1][3]);
          if constexpr (kk < 19) { rd1(IC<kk + 1>{}, I); if constexpr (i == 6) rd1(IC<kk + 1>{}, IC<7>{}); }
          SB();
        });
      });
      MFMA_FENCE();
    }
    *(LAS float*)(L + (g == 0 && tw < 3 ? L_CSUM + (80 + 16 * tw + c) * 4 : L_SYNC + 512 + l * 4)) = csq[0];
    asm volatile("s_waitcnt lgkmcnt(0)" ::: "memory");
    if (l == 0) lds_add(L, 4);
    lds_spin(L, 4, 4);
    v4u pqA[2], pqB[2];
#pragma unroll
    for (int s = 0; s < 2; ++s) {
      const float rs_ = *(const LAS float*)(L + L_STAT + (32 * tw + 16 * s + c) * 4);
      const float mr = *(const LAS float*)(L + L_STAT + 512 + (32 * tw + 16 * s + c) * 4);
      unsigned pq[6];
#pragma unroll
      for (int t = 0; t < 3; ++t) {
        const f32x4 cw = *(const LAS f32x4*)(L + L_CSUM + (80 + 16 * t + 4 * g) * 4);
        pq[2 * t] = pk2v(phi2(pfma(lo2(qacc[t][s]), sp2(rs_), pfma(lo2(cw), sp2(mr), sp2(1e-6f)))));
        pq[2 * t + 1] = pk2v(phi2(pfma(hi2(qacc[t][s]), sp2(rs_), pfma(hi2(cw), sp2(mr), sp2(1e-6f)))));
      }
      pqA[s] = v4u{pq[0], pq[1], pq[2], pq[3]};
      pqB[s] = v4u{g < 2 ? pq[4] : 0u, g < 2 ? pq[5] : 0u, 0u, 0u};
    }
    asm volatile("" : "+v"(pqA[0]), "+v"(pqA[1]), "+v"(pqB[0]), "+v"(pqB[1]));
    STAMPW(22, 4);
    if (w == 4) { wait16(ws, WS_FKV + p * 256, tag, l); if (l == 0) lds_add(L, 0); } else lds_spin(L, 0, 1);
    STAMPW(21, 4);
    {
      const int t = min(tid - 256, 209);
      v4u v[16];
#pragma unroll
      for (int i = 0; i < 16; ++i) v[i] = ldg_sc1(rs, WS_KVP + (p * 16 + i) * KVP_STRIDE + t * 16);
      f32x4 s = {0, 0, 0, 0};
#pragma unroll
      for (int i = 0; i < 16; ++i) s += __builtin_bit_cast(f32x4, v[i]);
      const v2u pk = {pk2v(lo2(s)), pk2v(hi2(s))};
      const int fo = t / 5, f = t < 200 ? 4 * (t - fo * 5) + (fo >= 20 ? 20 : 0) : 4 * (t - 200);
      const int kst = f >> 5, tt = (f >> 4) & 1, gg = (f >> 2) & 3;
      if (t < 200) *(LAS v2u*)(L + L_BD + ((fo >> 4) * 2 + kst) * 1024 + (gg * 16 + (fo & 15)) * 16 + tt * 8) = pk;
      else {
#pragma unroll
        for (int i = 0; i < 8; ++i) *(LAS v2u*)(L + L_BD + (6 + kst) * 1024 + (gg * 16 + 2 * i + (f >= 20)) * 16 + tt * 8) = pk;
      }
    }
    asm volatile("s_waitcnt lgkmcnt(0)" ::: "memory");
    if (l == 0) lds_add(L, 1);
    lds_spin(L, 1, 4);
    STAMPW(24, 4);
    v4u bd[8];
#pragma unroll
    for (int i = 0; i < 8; ++i) bd[i] = lds128(L, L_BD + i * 1024 + l * 16);
#pragma unroll
    for (int s = 0; s < 2; ++s) {
      f32x4 den = mfma(bd[6], pqA[s], f32x4{0, 0, 0, 0}); den = mfma(bd[7], pqB[s], den);
      f32x4 num[3];
#pragma unroll
      for (int ft = 0; ft < 3; ++ft) { num[ft] = mfma(bd[2 * ft], pqA[s], f32x4{0, 0, 0, 0}); num[ft] = mfma(bd[2 * ft + 1], pqB[s], num[ft]); }
      const float r0 = __builtin_amdgcn_rcpf(den[0] + 1e-6f), r1 = __builtin_amdgcn_rcpf(den[1] + 1e-6f);
      const float rm = g == 0 ? r0 : r1;
      const unsigned base = WS_O + (8 * r + 2 * tw + s) * 20 * 1024;
      stg_sc1(rs, base + p * 1024 + l * 16, v4u{pk2v(lo2(num[0]) * r0), pk2v(hi2(num[0]) * r0), pk2v(lo2(num[1]) * rm), pk2v(hi2(num[1]) * rm)});
      stg2_sc1(rs, g < 2 ? base + (16 + (p >> 2)) * 1024 + ((2 * ((p & 3) >> 1) + g) * 16 + c) * 16 + (p & 1) * 8 : 0x7FFFFFF0u,
               v2u{pk2v(lo2(num[2]) * r1), pk2v(hi2(num[2]) * r1)});
    }
    STAMPW(26, 4);
    DRAIN();
    STAMPW(27, 4);
    if (l == 0 && lds_add(L, 2) == 3) set_flag(ws, WS_FO + r * 256 + p * 4, tag);
  }
  LBAR();
  STAMP(5);

  const int th = w & 1, kq = w >> 1;
  const int sl = 2 * th + (kq & 1);
  const int tok = 16 * (4 * r2 + sl) + c;
  {
    v2u xr[3];
    const __amdgpu_buffer_rsrc_t rsx = __builtin_amdgcn_make_buffer_rsrc((void*)x, 0, 2048 * 640 * 2, 0x00020000);
#pragma unroll
    for (int i = 0; i < 3; ++i) {
      const int T = kq < 2 ? (i == 2 ? 4 : i) : (i == 2 ? 2 : 2 + i);
      xr[i] = __builtin_amdgcn_raw_buffer_load_b64(rsx, (tok * 640 + 80 * cs + 16 * T + 4 * g) * 2, 0, 0);
    }
    v4u wf[5][5];
#pragma unroll
    for (int k = 0; k < 5; ++k)
#pragma unroll
      for (int T = 0; T < 5; ++T) wf[k][T] = lds128(L, ((5 * kq + k) * 5 + T) * 1024 + l * 16);
#pragma unroll
    for (int k = 0; k < 5; ++k) asm volatile("" : "+v"(wf[k][0]), "+v"(wf[k][1]), "+v"(wf[k][2]), "+v"(wf[k][3]), "+v"(wf[k][4]));
    STAMP(28);
    if (w == 0) wait16(ws, WS_FO + (r2 >> 1) * 256, tag, l);
    if (w == 1) wait16(ws, WS_FC + c3 * 256, tag, l);
    if (w == 2) wait32(ws, WS_FD + cs * 256, tag, l);
    LBAR();
    STAMP(6);
    if (b == 0 && tid == 0) set_flag(ws, WS_EPOCH, tag);
    v4u xf[5][2];
#pragma unroll
    for (int k = 0; k < 5; ++k)
#pragma unroll
      for (int s = 0; s < 2; ++s) xf[k][s] = ldg_sc1(rs, WS_O + ((4 * r2 + 2 * th + s) * 20 + 5 * kq + k) * 1024 + l * 16);
    f32x4 acc[5][2];
#pragma unroll
    for (int T = 0; T < 5; ++T) { acc[T][0] = f32x4{0, 0, 0, 0}; acc[T][1] = f32x4{0, 0, 0, 0}; }
    SB();
#pragma unroll
    for (int k = 0; k < 5; ++k)
#pragma unroll
      for (int T = 0; T < 5; ++T)
#pragma unroll
        for (int s = 0; s < 2; ++s) acc[T][s] = mfma(wf[k][T], xf[k][s], acc[T][s]);
    STAMP(7);
    f32x4 own[3];
    splitk_exchange(L, acc, own, th, kq, l);
    STAMP(29);
    const bool hi = kq >= 2;
    unsigned hp[3][2];
    f32x2 sm2 = {0.f, 0.f}, sq2 = {0.f, 0.f};
#pragma unroll
    for (int i = 0; i < 3; ++i) {
      hp[i][0] = pk2v(f32x2{bflo(xr[i].x), bfhi(xr[i].x)} + lo2(own[i]));
      hp[i][1] = pk2v(f32x2{bflo(xr[i].y), bfhi(xr[i].y)} + hi2(own[i]));
      if (i == 2 && hi) { hp[2][0] = 0u; hp[2][1] = 0u; }
      const f32x2 a0 = {bflo(hp[i][0]), bfhi(hp[i][0])}, a1 = {bflo(hp[i][1]), bfhi(hp[i][1])};
      sm2 += a0 + a1;
      sq2 = pfma(a0, a0, pfma(a1, a1, sq2));
    }
    float sm = sm2.x + sm2.y, sq = sq2.x + sq2.y;
    const unsigned hb = WS_H + (4 * r2 + sl) * 20 * 1024;
    stg_sc1(rs, hb + (2 * cs + (kq >> 1)) * 1024 + l * 16, v4u{hp[0][0], hp[0][1], hp[1][0], hp[1][1]});
    stg2_sc1(rs, hi ? 0x7FFFFFF0u : hb + (16 + (cs >> 1)) * 1024 + l * 16 + (cs & 1) * 8, v2u{hp[2][0], hp[2][1]});
    sm = rowsum4(sm); sq = rowsum4(sq);
    stg2_sc1(rs, g == 0 ? WS_HS + ((4 * r2 + sl) * 16 + cs * 2 + (kq >> 1)) * 128 + c * 8 : 0x7FFFFFF0u, v2u{__float_as_uint(sm), __float_as_uint(sq)});
    *(LAS v2u*)(L + L_HT + (sl * 5 + (hi ? 2 : 0)) * 512 + l * 8) = v2u{hp[0][0], hp[0][1]};
    *(LAS v2u*)(L + L_HT + (sl * 5 + (hi ? 3 : 1)) * 512 + l * 8) = v2u{hp[1][0], hp[1][1]};
    *(LAS v2u*)(L + (hi ? L_SYNC + 512 : L_HT + (sl * 5 + 4) * 512) + l * 8) = v2u{hp[2][0], hp[2][1]};
    STAMP(30);
    DRAIN();
    STAMP(31);
    LBAR();
    if (tid == 0) set_flag(ws, WS_FH + (r2 >> 1) * 256 + ((r2 & 1) * 8 + cs) * 4, tag);
    STAMP(8);
  }
  {
    if (w < 4) p3_body<4, 0, false>(L, ws, rs, r3, c3, cs | ((r2 & 3) << 8), w, l, tag);
    else       p3_body<3, 4, true>(L, ws, rs, r3, c3, cs | ((r2 & 3) << 8), w, l, tag);
    STAMP(36);
    DRAIN();
    STAMP(37);
    LBAR();
    if (tid == 0) set_flag(ws, WS_FA + r3 * 256 + c3 * 4, tag);
    STAMP(12);
  }
  {
    f32x4 acc[5][2];
    if (th == 0) p4_main<0>(L, ws, r2 >> 1, r2, cs, kq, w, l, tag, acc); else p4_main<1>(L, ws, r2 >> 1, r2, cs, kq, w, l, tag, acc);
    LBAR();
    STAMP(14);
    f32x4 own[3];
    splitk_exchange(L, acc, own, th, kq, l);
    const bool hi = kq >= 2;
    LBAR();
#pragma unroll
    for (int i = 0; i < 3; ++i) {
      const int T = i == 2 ? 4 : (hi ? 2 : 0) + i;
      const v2u hh = *(const LAS v2u*)(L + L_HT + (sl * 5 + T) * 512 + l * 8);
      const v2u o = {pk2v(f32x2{bflo(hh.x), bfhi(hh.x)} + lo2(own[i])), pk2v(f32x2{bflo(hh.y), bfhi(hh.y)} + hi2(own[i]))};
      *(LAS v2u*)(L + ((i < 2 || !hi) ? (16 * sl + c) * 176 + T * 32 + g * 8 : 16384 + l * 8)) = o;
    }
    LBAR();
    {
      const __amdgpu_buffer_rsrc_t rso = __builtin_amdgcn_make_buffer_rsrc((void*)out, 0, 2048 * 640 * 2, 0x00020000);
#pragma unroll
      for (int i = 0; i < 2; ++i) {
        const int n = i * 512 + tid, row = n / 10, ch = n - row * 10;
        const v4u v = lds128(L, min(row, 63) * 176 + ch * 16);
        __builtin_amdgcn_raw_buffer_store_b128(v, rso, n < 640 ? ((64 * r2 + row) * 640 + 80 * cs) * 2 + ch * 16 : 0x7FFFFFF0u, 0, OUTAUX);
      }
    }
    STAMP(15);
  }
}

extern "C" void kernel_launch(void* const* d_in, const int* in_sizes, int n_in, void* d_out, int out_size, void* d_ws, size_t ws_size,
                              hipStream_t stream) {
  (void)in_sizes; (void)n_in; (void)out_size; (void)ws_size;
  (void)hipFuncSetAttribute((const void*)fused_block, hipFuncAttributeMaxDynamicSharedMemorySize, LDS_BYTES);
  fused_block<<<256, 512, LDS_BYTES, stream>>>((const u16*)d_in[0], (const u16*)d_in[1], (const u16*)d_in[2], (const u16*)d_in[4],
                                                (const u16*)d_in[5], (const u16*)d_in[6], (u16*)d_out, (char*)d_ws);
}
```

```cpp
#include <hip/hip_runtime.h>
#include <hip/hip_bf16.h>
#include <stdint.h>
#include <type_traits>

#define LAS __attribute__((address_space(3)))
#define GAS __attribute__((address_space(1)))
typedef __bf16 bf16x8 __attribute__((ext_vector_type(8)));
typedef __bf16 bf16x2 __attribute__((ext_vector_type(2)));
typedef short s16x4 __attribute__((ext_vector_type(4)));
typedef float f32x4 __attribute__((ext_vector_type(4)));
typedef float f32x2 __attribute__((ext_vector_type(2)));
typedef unsigned v4u __attribute__((ext_vector_type(4)));
typedef unsigned v2u __attribute__((ext_vector_type(2)));
typedef GAS unsigned gu32;
typedef unsigned short u16;
#define RLX_AGENT __ATOMIC_RELAXED, __HIP_MEMORY_SCOPE_AGENT

constexpr unsigned WS_EPOCH = 0, WS_TMO = 256;
constexpr unsigned WS_FKV = 4096, WS_FO = 8192, WS_FH = 12288, WS_FA = 16384, WS_FC = 20480, WS_FD = 24576, WS_FW = 28672;
constexpr unsigned WS_KVP = 32768, KVP_STRIDE = 3456;
constexpr unsigned WS_O = WS_KVP + 256 * KVP_STRIDE;
constexpr unsigned WS_H = WS_O + 2621440;
constexpr unsigned WS_HS = WS_H + 2621440;
constexpr unsigned WS_ACT = WS_HS + 262144;
constexpr unsigned WS_CS = WS_ACT + 7077888;
constexpr unsigned WS_W3 = WS_CS + 16 * 40 * 448;
constexpr unsigned WS_W4 = WS_W3 + 16 * 280 * 1024;
constexpr unsigned WS_W2 = WS_W4 + 8 * 270 * 1024;
constexpr unsigned WS_END = WS_W2 + 8 * 100 * 1024;

constexpr int CODE_HINT_BYTES = 60 * 1024;

constexpr int LDS_BYTES = 163840;
constexpr int L_KVF = 102400;
constexpr int L_KVC = 135168;
constexpr int L_BD = 138752;
constexpr int L_STAT = 146944;
constexpr int L_CSUM = 147968;
constexpr int L_CS3 = 151168;
constexpr int L_SYNC = 152064;
constexpr int L_HT = 153600;
constexpr int L_RED = 0;
constexpr int L_X3 = 86016;

__device__ __forceinline__ void glds(unsigned voff, const void* sbase, unsigned ldsaddr) {
  asm volatile("s_mov_b32 m0, %2\n\ts_nop 0\n\tglobal_load_lds_dwordx4 %0, %1" ::"v"(voff), "s"(sbase), "s"(ldsaddr) : "memory");
}
template <int IMM> __device__ __forceinline__ void gldsi(unsigned voff, const void* sbase, unsigned lbase) {
  asm volatile("s_add_u32 m0, %2, %3\n\ts_nop 0\n\tglobal_load_lds_dwordx4 %0, %1" ::"v"(voff), "s"(sbase), "s"(lbase), "i"(IMM) : "memory", "scc");
}
template <int N, int IMM> __device__ __forceinline__ void gldsn_sc1(unsigned voff, const void* sbase, unsigned lbase) {
  static_assert(N >= 1 && N <= 4, "");
  if constexpr (N == 1) asm volatile("s_add_u32 m0, %2, %3\n\ts_nop 0\n\tglobal_load_lds_dwordx4 %0, %1 sc1" ::"v"(voff), "s"(sbase), "s"(lbase), "i"(IMM) : "memory", "scc");
  if constexpr (N == 2) asm volatile("s_add_u32 m0, %2, %3\n\ts_nop 0\n\tglobal_load_lds_dwordx4 %0, %1 sc1\n\tglobal_load_lds_dwordx4 %0, %1 offset:1024 sc1" ::"v"(voff), "s"(sbase), "s"(lbase), "i"(IMM) : "memory", "scc");
  if constexpr (N == 3) asm volatile("s_add_u32 m0, %2, %3\n\ts_nop 0\n\tglobal_load_lds_dwordx4 %0, %1 sc1\n\tglobal_load_lds_dwordx4 %0, %1 offset:1024 sc1\n\tglobal_load_lds_dwordx4 %0, %1 offset:2048 sc1" ::"v"(voff), "s"(sbase), "s"(lbase), "i"(IMM) : "memory", "scc");
  if constexpr (N == 4) asm volatile("s_add_u32 m0, %2, %3\n\ts_nop 0\n\tglobal_load_lds_dwordx4 %0, %1 sc1\n\tglobal_load_lds_dwordx4 %0, %1 offset:1024 sc1\n\tglobal_load_lds_dwordx4 %0, %1 offset:2048 sc1\n\tglobal_load_lds_dwordx4 %0, %1 offset:3072 sc1" ::"v"(voff), "s"(sbase), "s"(lbase), "i"(IMM) : "memory", "scc");
}
template <int IMM, int OFF> __device__ __forceinline__ void gldso_sc1(unsigned voff, const void* sbase, unsigned lbase) {
  asm volatile("s_add_u32 m0, %2, %3\n\ts_nop 0\n\tglobal_load_lds_dwordx4 %0, %1 offset:%4 sc1" ::"v"(voff), "s"(sbase), "s"(lbase), "i"(IMM), "i"(OFF) : "memory", "scc");
}
__device__ __forceinline__ void ldg_asm_sc1(v4u& d, unsigned voff, const void* sbase) {
  asm volatile("global_load_dwordx4 %0, %1, %2 sc1" : "=v"(d) : "v"(voff), "s"(sbase) : "memory");
}
__device__ __forceinline__ void ldg2_asm_sc1(v2u& d, unsigned voff, const void* sbase) {
  asm volatile("global_load_dwordx2 %0, %1, %2 sc1" : "=v"(d) : "v"(voff), "s"(sbase) : "memory");
}
template <int N> __device__ __forceinline__ void wait_for(v4u& a, v4u& b) { asm volatile("s_waitcnt vmcnt(%2)" : "+v"(a), "+v"(b) : "i"(N) : "memory"); }
template <int N> __device__ __forceinline__ void waitvm() { asm volatile("s_waitcnt vmcnt(%0)" ::"i"(N) : "memory"); }
__device__ __forceinline__ f32x2 pfma(f32x2 a, f32x2 b, f32x2 c) { return __builtin_elementwise_fma(a, b, c); }
__device__ __forceinline__ f32x2 sp2(float a) { return f32x2{a, a}; }
__device__ __forceinline__ f32x2 lo2(f32x4 v) { return __builtin_shufflevector(v, v, 0, 1); }
__device__ __forceinline__ f32x2 hi2(f32x4 v) { return __builtin_shufflevector(v, v, 2, 3); }
__device__ __forceinline__ unsigned pk2v(f32x2 v) { return __builtin_bit_cast(unsigned, __builtin_convertvector(v, bf16x2)); }
__device__ __forceinline__ f32x2 phi2(f32x2 te) {
  const f32x2 y = pfma(te, sp2(1.44269504088896f), sp2(-1.44269504088896e-6f));
  const f32x2 e = {__builtin_amdgcn_fmed3f(__builtin_amdgcn_exp2f(y.x), 0.f, 1.f), __builtin_amdgcn_fmed3f(__builtin_amdgcn_exp2f(y.y), 0.f, 1.f)};
  const f32x2 m = {fmaxf(te.x, 1e-6f), fmaxf(te.y, 1e-6f)};
  return m + e;
}
struct TokC { float nmean, k0, k1, k2; };
__device__ __forceinline__ unsigned geglu2(f32x2 ag, f32x2 au, f32x2 cg, f32x2 cu, const TokC& k) {
  const f32x2 a = pfma(cg, sp2(k.nmean), ag), b = pfma(cu, sp2(k.nmean), au);
  const f32x2 z = a * pfma(a * a, sp2(k.k1), sp2(k.k0));
  const f32x2 d = f32x2{__builtin_amdgcn_exp2f(z.x), __builtin_amdgcn_exp2f(z.y)} + sp2(1.0f);
  const f32x2 rc = {__builtin_amdgcn_rcpf(d.x), __builtin_amdgcn_rcpf(d.y)};
  return pk2v((a * sp2(k.k2)) * b * rc);
}
template <int V> using IC = std::integral_constant<int, V>;
template <int I, int N, class F> __device__ __forceinline__ void static_for(F&& f) {
  if constexpr (I < N) { f(std::integral_constant<int, I>{}); static_for<I + 1, N>(f); }
}
#define LBAR() asm volatile("s_waitcnt lgkmcnt(0)\n\ts_barrier" ::: "memory")
#define DRAIN() asm volatile("s_waitcnt vmcnt(0)" ::: "memory")
#define SB() __builtin_amdgcn_sched_barrier(0)
template <int N> __device__ __forceinline__ void waitbar() { asm volatile("s_waitcnt vmcnt(%0) lgkmcnt(0)\n\ts_barrier" ::"i"(N) : "memory"); }

__device__ __forceinline__ float bflo(unsigned u) { return __uint_as_float(u << 16); }
__device__ __forceinline__ float bfhi(unsigned u) { return __uint_as_float(u & 0xffff0000u); }
__device__ __forceinline__ f32x4 mfma(v4u a, v4u b, f32x4 c) {
  return __builtin_amdgcn_mfma_f32_16x16x32_bf16(__builtin_bit_cast(bf16x8, a), __builtin_bit_cast(bf16x8, b), c, 0, 0, 0);
}
__device__ __forceinline__ void mfma_acc(f32x4& acc, v4u a, v4u b) {
  asm volatile("v_mfma_f32_16x16x32_bf16 %0, %1, %2, %0" : "+v"(acc) : "v"(a), "v"(b));
}
__device__ __forceinline__ void mfma_first(f32x4& acc, v4u a, v4u b) {
  asm volatile("v_mfma_f32_16x16x32_bf16 %0, %1, %2, 0" : "=&v"(acc) : "v"(a), "v"(b));
}
template <bool FIRST> __device__ __forceinline__ void mfma_a(f32x4& acc, v4u a, v4u b) { if constexpr (FIRST) mfma_first(acc, a, b); else mfma_acc(acc, a, b); }
#define MFMA_FENCE() asm volatile("s_nop 7\n\ts_nop 1" ::: "memory")
__device__ __forceinline__ v4u lds128(LAS char* L, int off) { return *(const LAS v4u*)(L + off); }
__device__ __forceinline__ v4u ldtr(LAS char* L, int off, int d) {
  s16x4 a = __builtin_amdgcn_ds_read_tr16_b64_v4i16((LAS s16x4*)(L + off));
  s16x4 b = __builtin_amdgcn_ds_read_tr16_b64_v4i16((LAS s16x4*)(L + off + d));
  v2u x = __builtin_bit_cast(v2u, a), y = __builtin_bit_cast(v2u, b);
  return v4u{x.x, x.y, y.x, y.y};
}
__device__ __forceinline__ float rowsum4(float v) {
  auto a = __builtin_amdgcn_permlane16_swap(__float_as_uint(v), __float_as_uint(v), false, false);
  const float u = __uint_as_float(a[0]) + __uint_as_float(a[1]);
  auto b = __builtin_amdgcn_permlane32_swap(__float_as_uint(u), __float_as_uint(u), false, false);
  return __uint_as_float(b[0]) + __uint_as_float(b[1]);
}
__device__ __forceinline__ v4u ldg_sc1(__amdgpu_buffer_rsrc_t rs, unsigned off) { return __builtin_amdgcn_raw_buffer_load_b128(rs, off, 0, 16); }
__device__ __forceinline__ unsigned ldg1_sc1(__amdgpu_buffer_rsrc_t rs, unsigned off) { return __builtin_amdgcn_raw_buffer_load_b32(rs, off, 0, 16); }
__device__ __forceinline__ void stg_sc1(__amdgpu_buffer_rsrc_t rs, unsigned off, v4u v) { __builtin_amdgcn_raw_buffer_store_b128(v, rs, off, 0, 16); }
__device__ __forceinline__ void stg2_sc1(__amdgpu_buffer_rsrc_t rs, unsigned off, v2u v) { __builtin_amdgcn_raw_buffer_store_b64(v, rs, off, 0, 16); }
__device__ __forceinline__ void stg1_sc1(__amdgpu_buffer_rsrc_t rs, unsigned off, unsigned v) { __builtin_amdgcn_raw_buffer_store_b32(v, rs, off, 0, 16); }

typedef GAS unsigned long long gu64;
__device__ __forceinline__ unsigned long long flag_value(unsigned tag) { return ((unsigned long long)tag << 32) | (tag * 0x9E3779B1u + 0x7F4A7C15u); }
__device__ __forceinline__ void set_flag(char* ws, unsigned line, int idx, unsigned tag) { __hip_atomic_store((gu64*)(ws + line) + idx, flag_value(tag), RLX_AGENT); }
template <int NF> __device__ __forceinline__ void wait_flags(char* ws, unsigned line, unsigned tag, int lane) {
  gu64* f = (gu64*)(ws + line) + (lane & (NF - 1));
  const unsigned long long want = flag_value(tag);
  for (unsigned sp = 0; sp < (1u << 15); ++sp) {
    if (__all(__hip_atomic_load(f, RLX_AGENT) == want)) return;
    __builtin_amdgcn_s_sleep(1);
  }
  if (lane == 0) __hip_atomic_store((gu32*)(ws + WS_TMO), 1u, RLX_AGENT);
}
__device__ __forceinline__ void wait16(char* ws, unsigned line, unsigned tag, int lane) { wait_flags<16>(ws, line, tag, lane); }
__device__ __forceinline__ void wait32(char* ws, unsigned line, unsigned tag, int lane) { wait_flags<32>(ws, line, tag, lane); }
typedef LAS unsigned lu32;
__device__ __forceinline__ unsigned lds_add(LAS char* L, int word) { return __hip_atomic_fetch_add((lu32*)(L + L_SYNC) + word, 1u, __ATOMIC_RELAXED, __HIP_MEMORY_SCOPE_WORKGROUP); }
__device__ __forceinline__ void lds_spin(LAS char* L, int word, unsigned want) {
  for (unsigned sp = 0; sp < (1u << 20); ++sp) {
    if (__builtin_amdgcn_readfirstlane(__hip_atomic_load((lu32*)(L + L_SYNC) + word, __ATOMIC_RELAXED, __HIP_MEMORY_SCOPE_WORKGROUP)) >= want) break;
  }
  asm volatile("" ::: "memory");
}
__device__ __forceinline__ void group_sync(LAS char* L, int word, int l) {
  asm volatile("s_waitcnt lgkmcnt(0)" ::: "memory");
  if (l == 0) lds_add(L, word);
  lds_spin(L, word, 4);
}
__device__ __forceinline__ void touch64(const void* addr) {
  asm volatile("s_mov_b32 m0, %1\n\ts_nop 0\n\tglobal_load_lds_dword %0, off sc1" ::"v"(addr), "s"(L_SYNC + 64) : "memory");
}

template <int KQ> __device__ __forceinline__ void splitk_exchange_t(LAS char* L, f32x4 (&acc)[5][2], f32x4 (&own)[3], int th, int l) {
  const int wb = L_RED + (th * 40 + KQ) * 1024 + l * 16;
  constexpr int so = KQ & 1, T0 = KQ < 2 ? 0 : 2;
#pragma unroll
  for (int s = 0; s < 2; ++s)
#pragma unroll
    for (int T = 0; T < 5; ++T) {
      const bool mine = s == so && (KQ < 2 ? (T == 0 || T == 1 || T == 4) : (T == 2 || T == 3));
      if (!mine) *(LAS f32x4*)(L + wb + (s * 5 + T) * 4096) = acc[T][s];
    }
  LBAR();
  const int rb = L_RED + (th * 2 + so) * 5 * 4096 + l * 16;
  f32x4 v[3][3];
#pragma unroll
  for (int i = 0; i < (KQ < 2 ? 3 : 2); ++i) {
    const int T = i == 2 ? 4 : T0 + i;
#pragma unroll
    for (int k = 0; k < 3; ++k) v[i][k] = *(const LAS f32x4*)(L + rb + T * 4096 + (k < KQ ? k : k + 1) * 1024);
  }
#pragma unroll
  for (int i = 0; i < 3; ++i) {
    if (i == 2 && KQ >= 2) { own[2] = f32x4{0, 0, 0, 0}; continue; }
    const int T = i == 2 ? 4 : T0 + i;
    const f32x4 o = acc[T][so];
    const f32x4 q0 = KQ == 0 ? o : v[i][0], q1 = KQ == 1 ? o : v[i][KQ == 0 ? 0 : 1], q2 = KQ == 2 ? o : v[i][KQ < 2 ? 1 : 2], q3 = KQ == 3 ? o : v[i][2];
    own[i] = (q0 + q1) + (q2 + q3);
  }
}
__device__ __forceinline__ void splitk_exchange(LAS char* L, f32x4 (&acc)[5][2], f32x4 (&own)[3], int th, int kq, int l) {
  if (kq == 0) splitk_exchange_t<0>(L, acc, own, th, l);
  else if (kq == 1) splitk_exchange_t<1>(L, acc, own, th, l);
  else if (kq == 2) splitk_exchange_t<2>(L, acc, own, th, l);
  else splitk_exchange_t<3>(L, acc, own, th, l);
}

constexpr int P4_XW = 4;
constexpr int P4_PRE = 4;
constexpr int P4_EARLY = 10;
constexpr int p4_g(int it, int n) { return it == 4 ? n : it == 6 ? 2 * n : 0; }
constexpr int p4_x(int it) { return it + P4_XW < 14 ? 2 : 0; }
constexpr int p4_xwait(int st, int n) {
  int y = 0;
  if (st < P4_XW) { y = 2 * (P4_XW - 1 - st); for (int j = 0; j < st; ++j) y += p4_x(j) + p4_g(j, n); }
  else { y = p4_g(st - P4_XW, n); for (int j = st - P4_XW + 1; j < st; ++j) y += p4_x(j) + p4_g(j, n); }
  return y;
}
constexpr int p4_wwait(int st, int n) {
  int y = st == 10 ? n : 0;
  for (int j = 7; j < st; ++j) y += p4_x(j);
  return y;
}
template <int TH, int stg> __device__ __forceinline__ void p4_issue(char* ws, int cs, int kq, int l) {
  constexpr int N = TH == 0 ? 3 : 2;
  const int kb = kq < 2 ? 14 * kq : 28 + 13 * (kq - 2);
  gldsn_sc1<N, (stg % 7) * 20480>(l * 16, ws + WS_W4 + (size_t)cs * 270 * 1024 + TH * 3072 + (size_t)min(kb + stg, 53) * 5120, kq * 5120 + TH * 3072);
}
template <int J> __device__ __forceinline__ void p4_pre_piece(const char* src, unsigned l16, int th, unsigned lbase) {
  constexpr int q0 = 2 * J, q1 = (2 * J + 1 < 35) ? 2 * J + 1 : 34;
  constexpr int f0 = (q0 / 5) * 20480 + (q0 % 5) * 1024, f1 = (q1 / 5) * 20480 + (q1 % 5) * 1024;
  asm volatile("s_mov_b32 m0, %2\n\ts_nop 0\n\tglobal_load_lds_dwordx4 %0, %1 sc1" ::"v"(l16), "s"(src + (th ? q1 : q0) * 1024), "s"(lbase + (th ? f1 : f0)) : "memory");
}
template <int TH>
__device__ __forceinline__ void p4_main(LAS char* L, char* ws, int r, int r2, int cs, int kq, int w, int l, unsigned tag, f32x4 (&acc)[5][2]) {
  constexpr int N = TH == 0 ? 3 : 2;
  const int kb = kq < 2 ? 14 * kq : 28 + 13 * (kq - 2);
  const int nk = kq < 2 ? 14 : 13;
  const unsigned l16 = l * 16;
  auto issue = [&](auto STG) { p4_issue<TH, STG>(ws, cs, kq, l); };
  int wbs[3];
  wbs[0] = kq * 5120 + l16; wbs[1] = wbs[0] + 3 * 20480; wbs[2] = wbs[0] + 6 * 20480;
  asm volatile("" : "+v"(wbs[0]), "+v"(wbs[1]), "+v"(wbs[2]));
  {
    const char* w4src = ws + WS_W4 + (size_t)cs * 270 * 1024 + (size_t)kb * 5120;
    static_for<P4_EARLY, 18>([&](auto J) { p4_pre_piece<J>(w4src, l16, TH, kq * 5120); });
    static_assert(P4_EARLY >= 10, "");
  }
  v4u wp[P4_PRE][5];
  static_for<0, P4_PRE>([&](auto ST) { static_for<0, 5>([&](auto I) { wp[ST][I] = lds128(L, wbs[ST / 3] + (ST % 3) * 20480 + I * 1024); }); });
  LBAR();
  static_for<7, 7 + P4_PRE>([&](auto ST) { issue(ST); });
  if (w == 0) wait16(ws, WS_FA + r * 256, tag, l);
  waitbar<P4_PRE * N>();
  v4u xf[14][2];
  const char* xa = ws + WS_ACT + (size_t)(4 * r2 + 2 * TH) * 54 * 1024;
  auto ldx = [&](auto K, auto S) {
    constexpr int k = K, s = S;
    ldg_asm_sc1(xf[k][s], l16, xa + (size_t)(s * 54 + min(kb + k, 53)) * 1024);
  };
  static_for<0, P4_XW>([&](auto K) { ldx(K, IC<0>{}); ldx(K, IC<1>{}); });
  v4u wf[2][5];
  auto rd1 = [&](auto STG, auto I) {
    constexpr int stg = STG, T = I, sl7 = stg % 7;
    wf[stg & 1][T] = lds128(L, wbs[sl7 / 3] + (sl7 % 3) * 20480 + T * 1024);
  };
  static_for<0, 14>([&](auto ST) {
    constexpr int st = ST;
    if constexpr (st == 4 || st == 6) LBAR();
    if constexpr (st == 10 || st == 12) waitbar<p4_wwait(st, N)>();
    wait_for<p4_xwait(st, N)>(xf[st][0], xf[st][1]);
    SB();
    if (st < 13 || st < nk) {
      static_for<0, 10>([&](auto I) {
        constexpr int i = I;
        if constexpr (st < P4_PRE) mfma_a<(st == 0)>(acc[i >> 1][i & 1], wp[st][i >> 1], xf[st][i & 1]);
        else mfma_a<false>(acc[i >> 1][i & 1], wf[st & 1][i >> 1], xf[st][i & 1]);
        if constexpr (st + P4_XW < 14 && (i == 0 || i == 2)) ldx(IC<st + P4_XW>{}, IC<(i >> 1)>{});
        if constexpr (st == 4 && i == 4) issue(IC<11>{});
        if constexpr (st == 6 && (i == 4 || i == 7)) issue(IC<12 + (i == 7)>{});
        if constexpr (st >= P4_PRE - 1 && st < 13 && i >= 2 && i < 7) rd1(IC<st + 1>{}, IC<i - 2>{});
        SB();
      });
    }
  });
  MFMA_FENCE();
}

template <int NT> struct Frag3 { v4u x[2], g[NT], u[NT]; };

template <int NT, int TILE0, bool LOADER>
__device__ __forceinline__ void p3_body(LAS char* L, char* ws, __amdgpu_buffer_rsrc_t rs, int r, int p, int cs, int w, int l, unsigned tag) {
  const int c = l & 15, g = l >> 4, tg = w & 3;
  const unsigned l16 = l * 16;
  constexpr int NWP = LOADER ? 3 : 4, NP = LOADER ? 7 : 4;
  const int wp0 = LOADER ? 16 + 3 * tg : 4 * tg;
  const char* wsrc = ws + WS_W3 + (size_t)p * 280 * 1024 + wp0 * 1024;
  const char* hx = ws + WS_H + (size_t)(8 * r + 2 * tg) * 20 * 1024;
  const unsigned lw = wp0 * 1024, lx = L_X3 + tg * 4096;
  auto piece = [&](auto STG, auto Q) {
    constexpr int stg = STG, q = Q, slot = stg % 3;
    if constexpr (q < NWP) gldso_sc1<slot * 28672, q * 1024>(l16, wsrc + stg * 28672, lw);
    else gldso_sc1<slot * 16384 + ((q - NWP) >> 1) * 2048, ((q - NWP) & 1) * 1024>(l16, hx + stg * 2048 + ((q - NWP) >> 1) * 20480, lx);
  };
  static_for<0, 3>([&](auto ST) { static_for<0, NWP>([&](auto Q) { piece(ST, Q); }); });
  if constexpr (!LOADER) {
    const int ct = min(w * 64 + l, 223), cm = ct >= 112;
    unsigned csv[20];
#pragma unroll
    for (int i = 0; i < 20; ++i) csv[i] = ldg1_sc1(rs, WS_CS + (p * 40 + 2 * i + cm) * 448 + (ct - 112 * cm) * 4);
    float s = 0.f;
#pragma unroll
    for (int i = 0; i < 20; ++i) s += __uint_as_float(csv[i]);
    *(LAS float*)(L + (w * 64 + l < 224 ? L_CS3 + ct * 4 : L_SYNC + 512)) = s;
  }
  if (w == 7) { wait16(ws, WS_FH + r * 256, tag, l); }
  DRAIN();
  LBAR();
  if constexpr (LOADER) static_for<0, 3>([&](auto ST) { static_for<NWP, NP>([&](auto Q) { piece(ST, Q); }); });
  v2u stp[2][4];
#pragma unroll
  for (int s = 0; s < 2; ++s)
#pragma unroll
    for (int i = 0; i < 4; ++i) ldg2_asm_sc1(stp[s][i], (4 * g + i) * 128 + c * 8, ws + WS_HS + (8 * r + 2 * tg + s) * 16 * 128);

  f32x4 ag[NT][2], au[NT][2];
  TokC tk[2];
  const int xo = L_X3 + tg * 4096 + l16;
  int wb0 = TILE0 * 1024 + l16, wb2 = wb0 + 2 * 28672;
  asm volatile("" : "+v"(wb0), "+v"(wb2));
  auto rd1 = [&](Frag3<NT>& f, auto SLOT, auto KS, auto I) {
    constexpr int slot = SLOT, ks = KS, i = I;
    if constexpr (i < 2) f.x[i] = lds128(L, xo + slot * 16384 + ks * 1024 + i * 2048);
    else {
      constexpr int j = i - 2, t = j >> 1, u = j & 1, off = ks * 14336 + u * 7168 + t * 1024;
      const v4u v = slot < 2 ? lds128(L, wb0 + slot * 28672 + off) : lds128(L, wb2 + off);
      if constexpr (u == 0) f.g[t] = v; else f.u[t] = v;
    }
  };
  auto mma1 = [&](const Frag3<NT>& f, auto I, auto FIRST) {
    constexpr int i = I, t = i >> 2, u = (i >> 1) & 1, s = i & 1;
    if constexpr (u == 0) mfma_a<FIRST>(ag[t][s], f.g[t], f.x[s]); else mfma_a<FIRST>(au[t][s], f.u[t], f.x[s]);
  };
  constexpr int NM = 4 * NT, NR = 2 + 2 * NT;
  Frag3<NT> A, B;
  if constexpr (LOADER) waitbar<8 + 8>(); else LBAR();
  static_for<0, NR>([&](auto I) { rd1(A, IC<0>{}, IC<0>{}, I); });
  static_for<0, 10>([&](auto ST) {
    constexpr int st = ST, slot = st % 3, nslot = (st + 1) % 3;
    static_for<0, NM>([&](auto I) {
      constexpr int i = I;
      mma1(A, I, IC<(st == 0)>{});
      if constexpr (i < NR) rd1(B, IC<slot>{}, IC<1>{}, I);
      SB();
    });
    if constexpr (st < 9) {
      if constexpr (st == 0) { if constexpr (LOADER) waitbar<4 + 8>(); else LBAR(); }
      else if constexpr (st == 1) { if constexpr (LOADER) waitbar<NP>(); else LBAR(); }
      else if constexpr (st < 8) waitbar<NP>(); else waitbar<0>();
    }
    if constexpr (st == 4) {
      asm volatile("" : "+v"(stp[0][0]), "+v"(stp[0][1]), "+v"(stp[0][2]), "+v"(stp[0][3]), "+v"(stp[1][0]), "+v"(stp[1][1]), "+v"(stp[1][2]), "+v"(stp[1][3]));
#pragma unroll
      for (int s = 0; s < 2; ++s) {
        float sm = 0.f, sq = 0.f;
#pragma unroll
        for (int i = 0; i < 4; ++i) { sm += __uint_as_float(stp[s][i].x); sq += __uint_as_float(stp[s][i].y); }
        sm = rowsum4(sm); sq = rowsum4(sq);
        const float mean = sm * (1.0f / 640.0f);
        const float rstd = __builtin_amdgcn_rsqf(fmaxf(sq * (1.0f / 640.0f) - mean * mean, 0.f) + 1e-5f);
        tk[s].nmean = -mean; tk[s].k0 = -2.302208198f * rstd; tk[s].k2 = rstd * rstd; tk[s].k1 = -0.1029432397f * rstd * tk[s].k2;
        asm volatile("" : "+v"(tk[s].nmean), "+v"(tk[s].k0), "+v"(tk[s].k1), "+v"(tk[s].k2));
      }
    }
    static_for<0, NM>([&](auto I) {
      constexpr int i = I;
      mma1(B, I, IC<0>{});
      if constexpr (LOADER) { if constexpr (st <= 6 && i < NP) piece(IC<st + 3>{}, I); }
      else { if constexpr (st <= 6 && (i % 3) == 0 && i / 3 < NP) piece(IC<st + 3>{}, IC<i / 3>{}); }
      if constexpr (st < 9 && i < NR) rd1(A, IC<nslot>{}, IC<0>{}, I);
      SB();
    });
  });
  MFMA_FENCE();
  LBAR();
  const int kq4 = w >> 1, th4 = w & 1;
  const char* w4src = ws + WS_W4 + (size_t)(cs & 7) * 270 * 1024 + (size_t)(kq4 < 2 ? 14 * kq4 : 28 + 13 * (kq4 - 2)) * 5120;
  const unsigned w4l = kq4 * 5120;
  f32x4 cgv[NT], cuv[NT];
#pragma unroll
  for (int t = 0; t < NT; ++t) {
    cgv[t] = *(const LAS f32x4*)(L + L_CS3 + ((TILE0 + t) * 16 + 4 * g) * 4);
    cuv[t] = *(const LAS f32x4*)(L + L_CS3 + (112 + (TILE0 + t) * 16 + 4 * g) * 4);
  }
  static_for<0, 2>([&](auto S) {
    constexpr int s = S;
    unsigned y[NT][2];
    const unsigned ab = WS_ACT + (8 * r + 2 * tg + s) * 54 * 1024 + l16;
    static_for<0, NT>([&](auto T) {
      constexpr int t = T, step = s * NT + t, j0 = step < NT ? step * P4_EARLY / NT : P4_EARLY, j1 = step < NT ? (step + 1) * P4_EARLY / NT : P4_EARLY;
      y[t][0] = geglu2(lo2(ag[t][s]), lo2(au[t][s]), lo2(cgv[t]), lo2(cuv[t]), tk[s]);
      static_for<j0, (j0 + j1 + 1) / 2>([&](auto J) { p4_pre_piece<J>(w4src, l16, th4, w4l); });
      y[t][1] = geglu2(hi2(ag[t][s]), hi2(au[t][s]), hi2(cgv[t]), hi2(cuv[t]), tk[s]);
      static_for<(j0 + j1 + 1) / 2, j1>([&](auto J) { p4_pre_piece<J>(w4src, l16, th4, w4l); });
      if constexpr (t & 1) {
        if constexpr (TILE0 == 0) stg_sc1(rs, p < 15 ? ab + (3 * p + (t >> 1)) * 1024 : 0x7FFFFFF0u, v4u{y[t - 1][0], y[t - 1][1], y[t][0], y[t][1]});
        else stg_sc1(rs, ab + (p < 15 ? 3 * p + 2 : 45) * 1024, v4u{y[t - 1][0], y[t - 1][1], y[t][0], y[t][1]});
      }
    });
    if constexpr (TILE0 != 0) stg2_sc1(rs, ab + (46 + (p >> 1)) * 1024 + (p & 1) * 8, v2u{y[NT - 1][0], y[NT - 1][1]});
  });
}

struct FragKV { v4u x[2], w[5]; };

__global__ __launch_bounds__(512, 2) void fused_block(const u16* __restrict__ x, const u16* __restrict__ wqkv, const u16* __restrict__ wo,
                                                      const u16* __restrict__ wg, const u16* __restrict__ wu, const u16* __restrict__ wd,
                                                      u16* __restrict__ out, char* __restrict__ ws) {
  extern __shared__ __attribute__((aligned(16))) char smem_[];
  LAS char* L = (LAS char*)smem_;
  const char* pc0;
  asm volatile("s_getpc_b64 %0" : "=s"(pc0));
  const int tid = threadIdx.x, l = tid & 63, c = l & 15, g = l >> 4;
  const int w = __builtin_amdgcn_readfirstlane(tid >> 6);
  const int b = blockIdx.x, xcd = b & 7, jj = b >> 3, rg = xcd >> 1, cgp = xcd & 1;
  const int r = 4 * rg + (jj >> 3), p = 8 * cgp + (jj & 7);
  const int r2 = 4 * xcd + (jj >> 3), cs = jj & 7;
  const int r3 = 8 * (xcd >> 2) + (jj >> 2), c3 = 4 * (xcd & 3) + (jj & 3);
  const __amdgpu_buffer_rsrc_t rs = __builtin_amdgcn_make_buffer_rsrc(ws, 0, WS_END, 0x00020000);
  const v4u ONES = {0x3F803F80u, 0x3F803F80u, 0x3F803F80u, 0x3F803F80u};
  const int q4 = (l & 15) >> 2, pp = l & 3;

  unsigned tag;
  constexpr int S1 = 26624;
  const int xo0 = c * 128 + ((g ^ (c & 7)) << 4);
  const int wrow = 16 * (g >> 1) + 4 * (g & 1) + q4;
  const int kvo = 16384 + wrow * 160 + 8 * pp;
  auto kv_product = [&](int w) {
    const int Ta = w < 2 ? 0 : w < 5 ? 1 : 2;
    const int Tb = (w == 0 || w == 2 || w == 7) ? 2 : (w == 4 || w == 6) ? 4 : 3;
    v4u A[4], B[4];
#pragma unroll
    for (int tg = 0; tg < 4; ++tg) {
      A[tg] = lds128(L, L_KVF + (tg * 5 + Ta) * 1024 + l * 16);
      B[tg] = lds128(L, L_KVF + (tg * 5 + Tb) * 1024 + l * 16);
    }
    f32x4 d = {0, 0, 0, 0};
    const bool one = Tb == 2 && c < 8;
#pragma unroll
    for (int tg = 0; tg < 4; ++tg) d = mfma(A[tg], one ? ONES : B[tg], d);
    const int kc = 16 * Ta + 4 * g;
    const int vc = 16 * Tb + c - 40;
    const int hk = kc >= 20;
    const bool iskv = kc < 40 && vc >= 0 && hk == (vc >= 20), isz = kc < 40 && vc < 0 && c == 0;
    *(LAS f32x4*)(L + (iskv ? L_KVC + (vc * 20 + kc - 20 * hk) * 4 : isz ? L_KVC + (800 + kc) * 4 : L_SYNC + 512 + (l & 31) * 16)) = d;
  };

  if (w < 4) {
    {
      const int n = (jj * 4 + w) * 4 + (l & 3);
      if (l < 4) touch64(pc0 + min(n * 128, CODE_HINT_BYTES - 128));
    }
    auto hint1 = [&](int stg, int n) {
      const int idx = jj * 36 + min(n, 35);
      const int wr_ = idx / 10, pc = idx - wr_ * 10, sec = pc / 5;
      touch64(idx < 640 ? (const char*)wqkv + (64 * stg + wr_) * 3840 + (1 + sec) * 1280 + cgp * 640 + (pc - sec * 5) * 128
                        : (const char*)x + ((size_t)rg * 512 + (idx - 640)) * 1280 + stg * 128);
    };
    {
      __builtin_amdgcn_s_sleep(16);
      const int sl_ = l / 9;
      if (l < 63) hint1(2 + sl_, 9 * w + (l - 9 * sl_));
    }
    if (l == 0) asm volatile("s_mov_b32 m0, %1\n\ts_nop 0\n\tglobal_load_lds_dword %0, off sc1" ::"v"(ws + WS_EPOCH), "s"(L_SYNC + 32) : "memory");
    *(LAS v4u*)(L + L_BD + tid * 16) = v4u{0, 0, 0, 0};
    *(LAS v4u*)(L + L_BD + 4096 + tid * 16) = v4u{0, 0, 0, 0};
    if (tid < 2) *(LAS v4u*)(L + L_SYNC + tid * 16) = v4u{0, 0, 0, 0};
    if (tid == 2) *(LAS v4u*)(L + L_SYNC + 48) = v4u{0, 0, 0, 0};
    auto hint2 = [&](int i) {
      const int n = 96 * w + 16 * i + (l & 15);
      const int li = b * 228 + min(n, 227);
      const char* aw_ = li < 6400 ? (const char*)wo + li * 128 : li < 23680 ? (const char*)wg + (li - 6400) * 128
                      : li < 40960 ? (const char*)wu + (li - 23680) * 128 : (const char*)wd + min(li - 40960, 17279) * 128;
      const int m = n - 228;
      const unsigned o5 = m < 32 ? WS_O + ((8 * r + (m >> 2)) * 20 + 16 + (p >> 2)) * 1024 + ((p & 3) >> 1) * 512 + (m & 3) * 128
                        : m < 64 ? WS_H + ((4 * r2 + ((m - 32) >> 3)) * 20 + 16 + (cs >> 1)) * 1024 + (m & 7) * 128
                                 : WS_ACT + ((8 * r3 + (min(m - 64, 63) >> 3)) * 54 + 46 + (c3 >> 1)) * 1024 + (m & 7) * 128;
      const char* a = n < 228 ? aw_ : ws + o5;
      if (l < 16) touch64(a);
    };
    f32x4 acc[2][5];
    {
      const int xw = w * 32 * 128;
      int xbs[3][2], kbs[3];
#pragma unroll
      for (int q = 0; q < 3; ++q) { xbs[q][0] = xw + xo0 + 2 * q * S1; xbs[q][1] = xw + (xo0 ^ 64) + 2 * q * S1; kbs[q] = kvo + 2 * q * S1; }
      asm volatile("" : "+v"(xbs[0][0]), "+v"(xbs[0][1]), "+v"(xbs[1][0]), "+v"(xbs[1][1]), "+v"(xbs[2][0]), "+v"(xbs[2][1]), "+v"(kbs[0]), "+v"(kbs[1]), "+v"(kbs[2]));
      auto rd1 = [&](FragKV& f, auto SLOT, auto KS, auto I) {
        constexpr int slot = SLOT, ks = KS, i = I;
        if constexpr (i < 2) f.x[i] = lds128(L, xbs[slot >> 1][ks] + (slot & 1) * S1 + i * 2048);
        else {
          constexpr int j = i - 2, t = j >> 1, h = j & 1;
          const v2u v = __builtin_bit_cast(v2u, __builtin_amdgcn_ds_read_tr16_b64_v4i16((LAS s16x4*)(L + kbs[slot >> 1] + (slot & 1) * S1 + ks * 32 * 160 + t * 32 + h * 8 * 160)));
          if constexpr (h == 0) { f.w[t].x = v.x; f.w[t].y = v.y; } else { f.w[t].z = v.x; f.w[t].w = v.y; }
        }
      };
      auto mma1 = [&](const FragKV& f, auto I, auto FIRST) { constexpr int i = I, t = i >> 1, s = i & 1; mfma_a<FIRST>(acc[s][t], f.x[s], f.w[t]); };
      FragKV A, B;
      LBAR();
      static_for<0, 12>([&](auto I) { rd1(A, IC<0>{}, IC<0>{}, I); });
      static_for<0, 10>([&](auto ST) {
        constexpr int st = ST;
        static_for<0, 10>([&](auto I) {
          constexpr int i = I;
          mma1(A, I, IC<(st == 0)>{});
          if constexpr (i < 6) { rd1(B, IC<(st % 5)>{}, IC<1>{}, IC<2 * i>{}); rd1(B, IC<(st % 5)>{}, IC<1>{}, IC<2 * i + 1>{}); }
          SB();
        });
        if constexpr (st < 9) LBAR();
        static_for<0, 10>([&](auto I) {
          constexpr int i = I;
          mma1(B, I, IC<0>{});
          if constexpr (st >= 2 && st < 8 && i == 8) hint2(st - 2);
          if constexpr (st < 9 && i < 6) { rd1(A, IC<((st + 1) % 5)>{}, IC<0>{}, IC<2 * i>{}); rd1(A, IC<((st + 1) % 5)>{}, IC<0>{}, IC<2 * i + 1>{}); }
          SB();
        });
      });
      MFMA_FENCE();
    }
    __builtin_amdgcn_s_setprio(2);
    waitvm<6>();
    LBAR();
    tag = __builtin_amdgcn_readfirstlane(*(const LAS unsigned*)(L + L_SYNC + 32)) + 1u;
    {
      f32x4 rstd[2], mr[2];
#pragma unroll
      for (int s = 0; s < 2; ++s) {
        rstd[s] = *(const LAS f32x4*)(L + L_STAT + (32 * w + 16 * s + 4 * g) * 4);
        mr[s] = *(const LAS f32x4*)(L + L_STAT + 512 + (32 * w + 16 * s + 4 * g) * 4);
      }
      float cw[5];
#pragma unroll
      for (int t = 0; t < 5; ++t) cw[t] = *(const LAS float*)(L + L_CSUM + (16 * t + c) * 4);
#pragma unroll
      for (int t = 0; t < 5; ++t) {
        unsigned o[4];
#pragma unroll
        for (int s = 0; s < 2; ++s)
#pragma unroll
          for (int h = 0; h < 2; ++h) {
            const f32x2 a2 = h ? hi2(acc[s][t]) : lo2(acc[s][t]), r2_ = h ? hi2(rstd[s]) : lo2(rstd[s]), m2 = h ? hi2(mr[s]) : lo2(mr[s]);
            f32x2 y;
            if (t < 2) y = phi2(pfma(a2, r2_, pfma(m2, sp2(cw[t]), sp2(1e-6f))));
            else if (t == 2) {
              const f32x2 te = pfma(a2, r2_, pfma(m2, sp2(cw[t]), sp2(1e-6f))), ph = phi2(te), v = te - sp2(1e-6f);
              y = c < 8 ? ph : v;
            } else y = pfma(a2, r2_, m2 * sp2(cw[t]));
            o[2 * s + h] = pk2v(y);
          }
        *(LAS v4u*)(L + L_KVF + (w * 5 + t) * 1024 + l * 16) = v4u{o[0], o[1], o[2], o[3]};
      }
    }
    group_sync(L, 5, l);
    kv_product(w);
    kv_product(w + 4);
    group_sync(L, 6, l);
    if (w == 0) {
      v4u v[4];
#pragma unroll
      for (int i = 0; i < 4; ++i) v[i] = lds128(L, L_KVC + min(i * 64 + l, 209) * 16);
#pragma unroll
      for (int i = 0; i < 4; ++i) stg_sc1(rs, i * 64 + l < 210 ? WS_KVP + (p * 16 + r) * KVP_STRIDE + (i * 64 + l) * 16 : 0x7FFFFFF0u, v[i]);
      DRAIN();
      if (l == 0) set_flag(ws, WS_FKV + p * 256, r, tag);
    }
    __builtin_amdgcn_s_setprio(0);
    const int f0 = min(7 * c3, 101);
    const int u3 = min(r3 + 16 * max(w - 1, 0), 39), ks3 = u3 >> 1, mat = u3 & 1;
    const int ks4 = min(r2 + 32 * max(w - 1, 0), 53);
    const unsigned area = L_KVF + max(w - 1, 0) * 12288;
    if (w > 0) {
      const char* w3 = (const char*)(mat ? wu : wg);
#pragma unroll
      for (int i = 0; i < 7; ++i) {
        const int n = 64 * i + l, pos = n / 14, ch = n - pos * 14;
        const int feat = ks3 < 16 ? 80 * (ks3 >> 1) + 32 * (ks3 & 1) + pos : 160 * (ks3 - 16) + 64 + (pos & 15) + 80 * (pos >> 4);
        glds(feat * 3456 + (f0 * 16 + 8 * ch) * 2, w3, area + i * 1024);
      }
      if (w < 3) {
#pragma unroll
        for (int i = 0; i < 5; ++i) {
          const int n = 64 * i + l, pos = n / 10, ch = n - pos * 10, t = pos >> 4;
          const int sp = ks4 < 45 ? ks4 / 3 : ks4 == 45 ? 15 : 2 * (ks4 - 46) + t;
          const int lt = ks4 < 45 ? 2 * (ks4 % 3) + t : ks4 == 45 ? 4 + t : 6;
          const int row = 16 * (min(7 * sp, 101) + lt) + (pos & 15);
          glds(row * 1280 + (80 * cs + 8 * ch) * 2, wd, area + 7168 + i * 1024);
        }
      }
    }
    if (w > 0) {
      if (w < 3) waitvm<5>(); else waitvm<0>();
      {
        const int tb = area + (4 * g + q4) * 224 + 8 * pp;
        v4u fr[7];
#pragma unroll
        for (int t = 0; t < 7; ++t) fr[t] = ldtr(L, tb + t * 32, 16 * 224);
        const bool ok = r3 + 16 * (w - 1) < 40;
        const unsigned ob = ok ? WS_W3 + ((c3 * 20 + ks3) * 14 + mat * 7) * 1024 + l * 16 : 0x7FFF0000u;
        float csel0 = 0.f, csel1 = 0.f;
#pragma unroll
        for (int t = 0; t < 7; ++t) {
          stg_sc1(rs, ob + t * 1024, fr[t]);
          const f32x4 cs_ = mfma(ONES, fr[t], f32x4{0, 0, 0, 0});
          if (t < 4) csel0 = (g == t) ? cs_[0] : csel0; else csel1 = (g == t - 4) ? cs_[0] : csel1;
        }
        const unsigned cb = ok ? WS_CS + (c3 * 40 + u3) * 448 + l * 4 : 0x7FFF0000u;
        stg1_sc1(rs, cb, __float_as_uint(csel0));
        stg1_sc1(rs, l < 48 ? cb + 256 : 0x7FFF0000u, __float_as_uint(csel1));
      }
      if (w < 3) {
        waitvm<9>();
        const int tb = area + 7168 + (4 * g + q4) * 160 + 8 * pp;
        v4u fr[5];
#pragma unroll
        for (int t = 0; t < 5; ++t) fr[t] = ldtr(L, tb + t * 32, 16 * 160);
        const unsigned ob = (r2 + 32 * (w - 1) < 54) ? WS_W4 + (cs * 54 + ks4) * 5 * 1024 + l * 16 : 0x7FFF0000u;
#pragma unroll
        for (int t = 0; t < 5; ++t) stg_sc1(rs, ob + t * 1024, fr[t]);
      }
    }
    if (w == 0) wait32(ws, WS_FW + cs * 256, tag, l);
    group_sync(L, 12, l);
    lds_spin(L, 4, 4);
    {
      const char* w2 = ws + WS_W2 + (size_t)cs * 100 * 1024 + w * 25 * 1024;
      static_for<0, 6>([&](auto I) { constexpr int i = I; gldsn_sc1<4, i * 4096>(l * 16, w2 + i * 4096, w * 25 * 1024); });
      gldsn_sc1<1, 24 * 1024>(l * 16, w2 + 24 * 1024, w * 25 * 1024);
    }
    DRAIN();
    if (l == 0 && lds_add(L, 3) == 3) { set_flag(ws, WS_FC + c3 * 256, r3, tag); set_flag(ws, WS_FD + cs * 256, r2, tag); }
  } else {
    const int tw = w - 4;
    unsigned vx[4], vw[3];
#pragma unroll
    for (int i = 0; i < 4; ++i) { const int tok = 8 * (4 * tw + i) + (l >> 3); vx[i] = tok * 1280 + (((l & 7) ^ (tok & 7)) << 4); }
    const char* xb = (const char*)x + (size_t)r * 128 * 1280;
    const unsigned lxw = 4 * tw * 1024, lkw = 16384 + 160 * tw * 16;
    auto pieceX = [&](auto ST, auto I) { constexpr int st = ST, i = I; gldsi<(st % 5) * S1 + i * 1024>(vx[i], xb + st * 128, lxw); };
    static_for<0, 4>([&](auto I) { pieceX(IC<0>{}, I); });
    constexpr unsigned AREA_O = 133120;
    if (w == 7) {
      if (l == 0) asm volatile("s_mov_b32 m0, %1\n\ts_nop 0\n\tglobal_load_lds_dword %0, off sc1" ::"v"(ws + WS_EPOCH), "s"(L_SYNC + 36) : "memory");
      const int ks2 = min(r2, 19);
#pragma unroll
      for (int i = 0; i < 5; ++i) {
        const int n = 64 * i + l, pos = n / 10, ch = n - pos * 10, t = pos >> 4, gg = (pos >> 2) & 3;
        const int row = ks2 < 16 ? 40 * ks2 + pos : 40 * (4 * (ks2 - 16) + 2 * (gg >> 1) + t) + 32 + 4 * (gg & 1) + (pos & 3);
        glds(row * 1280 + (80 * cs + 8 * ch) * 2, wo, AREA_O + i * 1024);
      }
    }
#pragma unroll
    for (int j = 0; j < 3; ++j) {
      const int n = min(160 * tw + 64 * j + l, 639), pos = n / 10, ch = n - pos * 10;
      const int col = (ch < 5) ? 640 + 40 * p + 8 * ch : 1280 + 40 * p + 8 * (ch - 5);
      const int k = (pos & ~12) | ((pos & 4) << 1) | ((pos & 8) >> 1);
      vw[j] = k * 3840 + col * 2;
    }
    auto pieceW = [&](auto ST, auto J) {
      constexpr int st = ST, j = J;
      if constexpr (j < 2) gldsi<(st % 5) * S1 + j * 1024>(vw[j], (const char*)wqkv + (size_t)st * 64 * 3840, lkw);
      else { if (l < 32) gldsi<(st % 5) * S1 + j * 1024>(vw[j], (const char*)wqkv + (size_t)st * 64 * 3840, lkw); }
    };
    auto piece = [&](auto ST, auto Q) { constexpr int q = Q; if constexpr (q < 4) pieceX(ST, Q); else pieceW(ST, IC<q - 4>{}); };
    static_for<0, 3>([&](auto J) { pieceW(IC<0>{}, J); });
    static_for<0, 7>([&](auto Q) { piece(IC<1>{}, Q); });
    auto pieceQ = [&](auto QS, auto J) {
      constexpr int qs = QS, j = J, npc = qs < 2 ? 6 : 3;
      const int n = 64 * (tw * npc + j) + l, pos = n / 6, ch = n - pos * 6;
      const int k = 256 * qs + ((pos & ~12) | ((pos & 4) << 1) | ((pos & 8) >> 1));
      if (ch < 5) gldsi<qs * S1 + j * 1024>(k * 3840 + (40 * p + 8 * ch) * 2, wqkv, tw * npc * 1024);
    };
    v4u xs[20][2];
    f32x4 sq[2], sm[2], cs0, cs1;
    v4u ones_ = ONES;
    asm volatile("" : "+v"(ones_));
    {
      const int xw = tw * 32 * 128;
      int xbs[3][2], ebs[3][2];
#pragma unroll
      for (int q = 0; q < 3; ++q) { xbs[q][0] = xw + xo0 + 2 * q * S1; xbs[q][1] = xw + (xo0 ^ 64) + 2 * q * S1; ebs[q][0] = kvo + tw * 32 + 2 * q * S1; ebs[q][1] = kvo + 4 * 32 + 2 * q * S1; }
      asm volatile("" : "+v"(xbs[0][0]), "+v"(xbs[0][1]), "+v"(xbs[1][0]), "+v"(xbs[1][1]), "+v"(xbs[2][0]), "+v"(xbs[2][1]),
                        "+v"(ebs[0][0]), "+v"(ebs[0][1]), "+v"(ebs[1][0]), "+v"(ebs[1][1]), "+v"(ebs[2][0]), "+v"(ebs[2][1]));
      v4u e[2][2];
      auto rd1 = [&](auto KK, auto I) {
        constexpr int kk = KK, i = I, slot = (kk >> 1) % 5, ks = kk & 1;
        if constexpr (i < 2) xs[kk][i] = lds128(L, xbs[slot >> 1][ks] + (slot & 1) * S1 + i * 2048);
        else {
          constexpr int t = (i - 2) >> 1, h = i & 1;
          const v2u v = __builtin_bit_cast(v2u, __builtin_amdgcn_ds_read_tr16_b64_v4i16((LAS s16x4*)(L + ebs[slot >> 1][t] + (slot & 1) * S1 + ks * 32 * 160 + h * 8 * 160)));
          if constexpr (h == 0) { e[kk & 1][t].x = v.x; e[kk & 1][t].y = v.y; } else { e[kk & 1][t].z = v.x; e[kk & 1][t].w = v.y; }
        }
      };
      auto mma1 = [&](auto KK, auto I) {
        constexpr int kk = KK, i = I;
        if constexpr (i < 2) mfma_a<(kk == 0)>(sq[i], xs[kk][i], xs[kk][i]);
        else if constexpr (i < 4) mfma_a<(kk == 0)>(sm[i - 2], ones_, xs[kk][i - 2]);
        else if constexpr (i == 4) mfma_a<(kk == 0)>(cs0, ones_, e[kk & 1][0]);
        else mfma_a<(kk == 0)>(cs1, ones_, e[kk & 1][1]);
      };
      waitbar<7>();
      if (w == 7) {
        v4u fr[5];
#pragma unroll
        for (int t = 0; t < 5; ++t) fr[t] = ldtr(L, AREA_O + (4 * g + q4) * 160 + 8 * pp + t * 32, 16 * 160);
        const unsigned ob = r2 < 20 ? WS_W2 + (cs * 20 + min(r2, 19)) * 5 * 1024 + l * 16 : 0x7FFF0000u;
#pragma unroll
        for (int t = 0; t < 5; ++t) stg_sc1(rs, ob + t * 1024, fr[t]);
      }
      static_for<0, 6>([&](auto I) { rd1(IC<0>{}, I); });
      static_for<0, 10>([&](auto ST) {
        constexpr int st = ST;
        static_for<0, 6>([&](auto I) {
          constexpr int i = I;
          mma1(IC<2 * st>{}, I);
          rd1(IC<2 * st + 1>{}, I);
          if constexpr (st == 0) { piece(IC<2>{}, I); if constexpr (i == 5) piece(IC<2>{}, IC<6>{}); }
          if constexpr (st == 1) { piece(IC<4>{}, I); if constexpr (i == 5) piece(IC<4>{}, IC<6>{}); }
          SB();
        });
        if constexpr (st < 9) { if constexpr (st == 0) waitbar<7>(); else if constexpr (st <= 6) waitbar<14>(); else if constexpr (st == 7) waitbar<13>(); else waitbar<12>(); }
        if constexpr (st == 6) {
          if (w == 7 && l == 0) set_flag(ws, WS_FW + cs * 256, r2, *(const LAS unsigned*)(L + L_SYNC + 36) + 1u);
        }
        static_for<0, 6>([&](auto I) {
          constexpr int i = I;
          mma1(IC<2 * st + 1>{}, I);
          if constexpr (st == 0) { piece(IC<3>{}, I); if constexpr (i == 5) piece(IC<3>{}, IC<6>{}); }
          if constexpr (st >= 1 && st <= 5) { piece(IC<st + 4>{}, I); if constexpr (i == 5) piece(IC<st + 4>{}, IC<6>{}); }
          if constexpr (st == 6 || st == 7) pieceQ(IC<st - 6>{}, I);
          if constexpr (st == 8 && i < 3) pieceQ(IC<2>{}, I);
          if constexpr (st < 9) rd1(IC<2 * st + 2>{}, I);
          SB();
        });
      });
      MFMA_FENCE();
    }
#pragma unroll
    for (int s = 0; s < 2; ++s) {
      const int e_ = c & 3;
      const float v = e_ == 0 ? sq[s][0] : e_ == 1 ? sq[s][1] : e_ == 2 ? sq[s][2] : sq[s][3];
      const float m = sm[s][0] * (1.0f / 640.0f);
      const float rs_ = __builtin_amdgcn_rsqf(fmaxf(v * (1.0f / 640.0f) - m * m + 1e-5f, 1e-5f));
      const bool own = g == (c >> 2);
      *(LAS float*)(L + (own ? L_STAT + (32 * tw + 16 * s + c) * 4 : L_SYNC + 512 + l * 4)) = rs_;
      *(LAS float*)(L + (own ? L_STAT + 512 + (32 * tw + 16 * s + c) * 4 : L_SYNC + 768 + l * 4)) = -m * rs_;
    }
    *(LAS float*)(L + (g == 0 ? L_CSUM + (16 * tw + c) * 4 : L_SYNC + 512 + l * 4)) = cs0[0];
    *(LAS float*)(L + (g == 0 ? L_CSUM + (64 + c) * 4 : L_SYNC + 512 + l * 4)) = cs1[0];
    LBAR();
    tag = __builtin_amdgcn_readfirstlane(*(const LAS unsigned*)(L + L_SYNC + 32)) + 1u;
    DRAIN();
    group_sync(L, 7, l);
    lds_spin(L, 5, 4);
    f32x4 qacc[3][2], csq;
    {
      int qbs[3];
#pragma unroll
      for (int q = 0; q < 3; ++q) qbs[q] = q * S1 + wrow * 96 + 8 * pp;
      asm volatile("" : "+v"(qbs[0]), "+v"(qbs[1]), "+v"(qbs[2]));
      int cbs[3];
#pragma unroll
      for (int q = 0; q < 3; ++q) cbs[q] = qbs[q] + (tw < 3 ? tw : 2) * 32;
      asm volatile("" : "+v"(cbs[0]), "+v"(cbs[1]), "+v"(cbs[2]));
      v4u f[2][4];
      auto rd1 = [&](auto KK, auto I) {
        constexpr int kk = KK, i = I, t = i >> 1, h = i & 1;
        const int base = t < 3 ? qbs[kk >> 3] + t * 32 : cbs[kk >> 3];
        const v2u v = __builtin_bit_cast(v2u, __builtin_amdgcn_ds_read_tr16_b64_v4i16((LAS s16x4*)(L + base + (kk & 7) * 32 * 96 + h * 8 * 96)));
        if constexpr (h == 0) { f[kk & 1][t].x = v.x; f[kk & 1][t].y = v.y; } else { f[kk & 1][t].z = v.x; f[kk & 1][t].w = v.y; }
      };
      static_for<0, 8>([&](auto I) { rd1(IC<0>{}, I); });
      static_for<0, 20>([&](auto KK) {
        constexpr int kk = KK;
        static_for<0, 7>([&](auto I) {
          constexpr int i = I;
          if constexpr (i < 6) mfma_a<(kk == 0)>(qacc[i >> 1][i & 1], f[kk & 1][i >> 1], xs[kk][i & 1]);
          else mfma_a<(kk == 0)>(csq, ones_, f[kk & 1][3]);
          if constexpr (kk < 19) { rd1(IC<kk + 1>{}, I); if constexpr (i == 6) rd1(IC<kk + 1>{}, IC<7>{}); }
          SB();
        });
      });
      MFMA_FENCE();
    }
    *(LAS float*)(L + (g == 0 && tw < 3 ? L_CSUM + (80 + 16 * tw + c) * 4 : L_SYNC + 512 + l * 4)) = csq[0];
    asm volatile("s_waitcnt lgkmcnt(0)" ::: "memory");
    if (l == 0) lds_add(L, 4);
    lds_spin(L, 4, 4);
    v4u pqA[2], pqB[2];
#pragma unroll
    for (int s = 0; s < 2; ++s) {
      const float rs_ = *(const LAS float*)(L + L_STAT + (32 * tw + 16 * s + c) * 4);
      const float mr = *(const LAS float*)(L + L_STAT + 512 + (32 * tw + 16 * s + c) * 4);
      unsigned pq[6];
#pragma unroll
      for (int t = 0; t < 3; ++t) {
        const f32x4 cw = *(const LAS f32x4*)(L + L_CSUM + (80 + 16 * t + 4 * g) * 4);
        pq[2 * t] = pk2v(phi2(pfma(lo2(qacc[t][s]), sp2(rs_), pfma(lo2(cw), sp2(mr), sp2(1e-6f)))));
        pq[2 * t + 1] = pk2v(phi2(pfma(hi2(qacc[t][s]), sp2(rs_), pfma(hi2(cw), sp2(mr), sp2(1e-6f)))));
      }
      pqA[s] = v4u{pq[0], pq[1], pq[2], pq[3]};
      pqB[s] = v4u{g < 2 ? pq[4] : 0u, g < 2 ? pq[5] : 0u, 0u, 0u};
    }
    asm volatile("" : "+v"(pqA[0]), "+v"(pqA[1]), "+v"(pqB[0]), "+v"(pqB[1]));
    if (w == 4) { wait16(ws, WS_FKV + p * 256, tag, l); if (l == 0) lds_add(L, 0); } else lds_spin(L, 0, 1);
    {
      const int t = min(tid - 256, 209);
      v4u v[16];
#pragma unroll
      for (int i = 0; i < 16; ++i) v[i] = ldg_sc1(rs, WS_KVP + (p * 16 + i) * KVP_STRIDE + t * 16);
      f32x4 s = {0, 0, 0, 0};
#pragma unroll
      for (int i = 0; i < 16; ++i) s += __builtin_bit_cast(f32x4, v[i]);
      const v2u pk = {pk2v(lo2(s)), pk2v(hi2(s))};
      const int fo = t / 5, f = t < 200 ? 4 * (t - fo * 5) + (fo >= 20 ? 20 : 0) : 4 * (t - 200);
      const int kst = f >> 5, tt = (f >> 4) & 1, gg = (f >> 2) & 3;
      if (t < 200) *(LAS v2u*)(L + L_BD + ((fo >> 4) * 2 + kst) * 1024 + (gg * 16 + (fo & 15)) * 16 + tt * 8) = pk;
      else {
#pragma unroll
        for (int i = 0; i < 8; ++i) *(LAS v2u*)(L + L_BD + (6 + kst) * 1024 + (gg * 16 + 2 * i + (f >= 20)) * 16 + tt * 8) = pk;
      }
    }
    asm volatile("s_waitcnt lgkmcnt(0)" ::: "memory");
    if (l == 0) lds_add(L, 1);
    lds_spin(L, 1, 4);
    v4u bd[8];
#pragma unroll
    for (int i = 0; i < 8; ++i) bd[i] = lds128(L, L_BD + i * 1024 + l * 16);
#pragma unroll
    for (int s = 0; s < 2; ++s) {
      f32x4 den = mfma(bd[6], pqA[s], f32x4{0, 0, 0, 0}); den = mfma(bd[7], pqB[s], den);
      f32x4 num[3];
#pragma unroll
      for (int ft = 0; ft < 3; ++ft) { num[ft] = mfma(bd[2 * ft], pqA[s], f32x4{0, 0, 0, 0}); num[ft] = mfma(bd[2 * ft + 1], pqB[s], num[ft]); }
      const float r0 = __builtin_amdgcn_rcpf(den[0] + 1e-6f), r1 = __builtin_amdgcn_rcpf(den[1] + 1e-6f);
      const float rm = g == 0 ? r0 : r1;
      const unsigned base = WS_O + (8 * r + 2 * tw + s) * 20 * 1024;
      stg_sc1(rs, base + p * 1024 + l * 16, v4u{pk2v(lo2(num[0]) * r0), pk2v(hi2(num[0]) * r0), pk2v(lo2(num[1]) * rm), pk2v(hi2(num[1]) * rm)});
      stg2_sc1(rs, g < 2 ? base + (16 + (p >> 2)) * 1024 + ((2 * ((p & 3) >> 1) + g) * 16 + c) * 16 + (p & 1) * 8 : 0x7FFFFFF0u,
               v2u{pk2v(lo2(num[2]) * r1), pk2v(hi2(num[2]) * r1)});
    }
    DRAIN();
    if (l == 0 && lds_add(L, 2) == 3) set_flag(ws, WS_FO + r * 256, p, tag);
  }
  LBAR();

  const int th = w & 1, kq = w >> 1;
  const int sl = 2 * th + (kq & 1);
  const int tok = 16 * (4 * r2 + sl) + c;
  {
    v2u xr[3];
    const __amdgpu_buffer_rsrc_t rsx = __builtin_amdgcn_make_buffer_rsrc((void*)x, 0, 2048 * 640 * 2, 0x00020000);
#pragma unroll
    for (int i = 0; i < 3; ++i) {
      const int T = kq < 2 ? (i == 2 ? 4 : i) : (i == 2 ? 2 : 2 + i);
      xr[i] = __builtin_amdgcn_raw_buffer_load_b64(rsx, (tok * 640 + 80 * cs + 16 * T + 4 * g) * 2, 0, 0);
    }
    v4u wf[5][5];
#pragma unroll
    for (int k = 0; k < 5; ++k)
#pragma unroll
      for (int T = 0; T < 5; ++T) wf[k][T] = lds128(L, ((5 * kq + k) * 5 + T) * 1024 + l * 16);
#pragma unroll
    for (int k = 0; k < 5; ++k) asm volatile("" : "+v"(wf[k][0]), "+v"(wf[k][1]), "+v"(wf[k][2]), "+v"(wf[k][3]), "+v"(wf[k][4]));
    if (w == 0) wait16(ws, WS_FO + (r2 >> 1) * 256, tag, l);
    if (w == 1) wait16(ws, WS_FC + c3 * 256, tag, l);
    if (w == 2) wait32(ws, WS_FD + cs * 256, tag, l);
    LBAR();
    if (b == 0 && tid == 0) __hip_atomic_store((gu32*)(ws + WS_EPOCH), tag, RLX_AGENT);
    v4u xf[5][2];
#pragma unroll
    for (int k = 0; k < 5; ++k)
#pragma unroll
      for (int s = 0; s < 2; ++s) xf[k][s] = ldg_sc1(rs, WS_O + ((4 * r2 + 2 * th + s) * 20 + 5 * kq + k) * 1024 + l * 16);
    f32x4 acc[5][2];
#pragma unroll
    for (int T = 0; T < 5; ++T) { acc[T][0] = f32x4{0, 0, 0, 0}; acc[T][1] = f32x4{0, 0, 0, 0}; }
    SB();
#pragma unroll
    for (int k = 0; k < 5; ++k)
#pragma unroll
      for (int T = 0; T < 5; ++T)
#pragma unroll
        for (int s = 0; s < 2; ++s) acc[T][s] = mfma(wf[k][T], xf[k][s], acc[T][s]);
    f32x4 own[3];
    splitk_exchange(L, acc, own, th, kq, l);
    const bool hi = kq >= 2;
    unsigned hp[3][2];
    f32x2 sm2 = {0.f, 0.f}, sq2 = {0.f, 0.f};
#pragma unroll
    for (int i = 0; i < 3; ++i) {
      hp[i][0] = pk2v(f32x2{bflo(xr[i].x), bfhi(xr[i].x)} + lo2(own[i]));
      hp[i][1] = pk2v(f32x2{bflo(xr[i].y), bfhi(xr[i].y)} + hi2(own[i]));
      if (i == 2 && hi) { hp[2][0] = 0u; hp[2][1] = 0u; }
      const f32x2 a0 = {bflo(hp[i][0]), bfhi(hp[i][0])}, a1 = {bflo(hp[i][1]), bfhi(hp[i][1])};
      sm2 += a0 + a1;
      sq2 = pfma(a0, a0, pfma(a1, a1, sq2));
    }
    float sm = sm2.x + sm2.y, sq = sq2.x + sq2.y;
    const unsigned hb = WS_H + (4 * r2 + sl) * 20 * 1024;
    stg_sc1(rs, hb + (2 * cs + (kq >> 1)) * 1024 + l * 16, v4u{hp[0][0], hp[0][1], hp[1][0], hp[1][1]});
    stg2_sc1(rs, hi ? 0x7FFFFFF0u : hb + (16 + (cs >> 1)) * 1024 + l * 16 + (cs & 1) * 8, v2u{hp[2][0], hp[2][1]});
    sm = rowsum4(sm); sq = rowsum4(sq);
    stg2_sc1(rs, g == 0 ? WS_HS + ((4 * r2 + sl) * 16 + cs * 2 + (kq >> 1)) * 128 + c * 8 : 0x7FFFFFF0u, v2u{__float_as_uint(sm), __float_as_uint(sq)});
    *(LAS v2u*)(L + L_HT + (sl * 5 + (hi ? 2 : 0)) * 512 + l * 8) = v2u{hp[0][0], hp[0][1]};
    *(LAS v2u*)(L + L_HT + (sl * 5 + (hi ? 3 : 1)) * 512 + l * 8) = v2u{hp[1][0], hp[1][1]};
    *(LAS v2u*)(L + (hi ? L_SYNC + 512 : L_HT + (sl * 5 + 4) * 512) + l * 8) = v2u{hp[2][0], hp[2][1]};
    DRAIN();
    LBAR();
    if (tid == 0) set_flag(ws, WS_FH + (r2 >> 1) * 256, (r2 & 1) * 8 + cs, tag);
  }
  {
    if (w < 4) p3_body<4, 0, false>(L, ws, rs, r3, c3, cs | ((r2 & 3) << 8), w, l, tag);
    else       p3_body<3, 4, true>(L, ws, rs, r3, c3, cs | ((r2 & 3) << 8), w, l, tag);
    DRAIN();
    LBAR();
    if (tid == 0) set_flag(ws, WS_FA + r3 * 256, c3, tag);
  }
  {
    f32x4 acc[5][2];
    if (th == 0) p4_main<0>(L, ws, r2 >> 1, r2, cs, kq, w, l, tag, acc); else p4_main<1>(L, ws, r2 >> 1, r2, cs, kq, w, l, tag, acc);
    LBAR();
    f32x4 own[3];
    splitk_exchange(L, acc, own, th, kq, l);
    const bool hi = kq >= 2;
    LBAR();
#pragma unroll
    for (int i = 0; i < 3; ++i) {
      const int T = i == 2 ? 4 : (hi ? 2 : 0) + i;
      const v2u hh = *(const LAS v2u*)(L + L_HT + (sl * 5 + T) * 512 + l * 8);
      const v2u o = {pk2v(f32x2{bflo(hh.x), bfhi(hh.x)} + lo2(own[i])), pk2v(f32x2{bflo(hh.y), bfhi(hh.y)} + hi2(own[i]))};
      *(LAS v2u*)(L + ((i < 2 || !hi) ? (16 * sl + c) * 176 + T * 32 + g * 8 : 16384 + l * 8)) = o;
    }
    LBAR();
    {
      const __amdgpu_buffer_rsrc_t rso = __builtin_amdgcn_make_buffer_rsrc((void*)out, 0, 2048 * 640 * 2, 0x00020000);
#pragma unroll
      for (int i = 0; i < 2; ++i) {
        const int n = i * 512 + tid, row = n / 10, ch = n - row * 10;
        const v4u v = lds128(L, min(row, 63) * 176 + ch * 16);
        __builtin_amdgcn_raw_buffer_store_b128(v, rso, n < 640 ? ((64 * r2 + row) * 640 + 80 * cs) * 2 + ch * 16 : 0x7FFFFFF0u, 0, 16);
      }
    }
  }
}

extern "C" void kernel_launch(void* const* d_in, const int* in_sizes, int n_in, void* d_out, int out_size, void* d_ws, size_t ws_size,
                              hipStream_t stream) {
  (void)in_sizes; (void)n_in; (void)out_size; (void)ws_size;
  (void)hipFuncSetAttribute((const void*)fused_block, hipFuncAttributeMaxDynamicSharedMemorySize, LDS_BYTES);
  fused_block<<<256, 512, LDS_BYTES, stream>>>((const u16*)d_in[0], (const u16*)d_in[1], (const u16*)d_in[2], (const u16*)d_in[4],
                                                (const u16*)d_in[5], (const u16*)d_in[6], (u16*)d_out, (char*)d_ws);
}
```
